```python
import torch

def _softmax(x, axis=-1):
    return torch.softmax(x, dim=axis)
def _gelu(x):
    return torch.nn.functional.gelu(x, approximate="tanh")
def _silu(x):
    return torch.nn.functional.silu(x)
def _sigmoid(x):
    return torch.sigmoid(x)
def _layernorm(x, axis=-1, eps=1e-5):
    m = x.mean(dim=axis, keepdim=True)
    v = x.var(dim=axis, keepdim=True, unbiased=False)
    return (x - m) / torch.sqrt(v + eps)
def _scatter_add(tgt, idx, src, axis=0):
    return tgt.clone().index_add_(axis, idx, src)
def _segment_sum(src, seg, n):
    out = torch.zeros((n,) + src.shape[1:], dtype=src.dtype)
    return out.index_add_(0, seg, src)
def _pad(x, pw):
    flat = tuple(v for pair in reversed(pw) for v in pair)
    return torch.nn.functional.pad(x, flat)

kernel_name = 'parallel_sum__ssm_conv_s5__glu_gated_geglu__prompt'

def setup_inputs():

    _w = (lambda a: (lambda t: t.to(torch.bfloat16) if t.is_floating_point() else t)(torch.as_tensor(a)))
    def _A_setup():
        B, S, D = 1, 2048, 512
        P = D
        Lambda_re = -0.5 * (lambda _x: torch.exp(torch.as_tensor(_x)))((lambda *s: torch.randn(*s, dtype=torch.bfloat16) * 0.2)(P))
        Lambda_im = (lambda *s: torch.randn(*s, dtype=torch.bfloat16) * 0.4)(P) * 1.0
        B_re = (lambda *s: torch.randn(*s, dtype=torch.bfloat16) * 0.2)(P, D) * (D ** -0.5)
        B_im = (lambda *s: torch.randn(*s, dtype=torch.bfloat16) * 0.2)(P, D) * (D ** -0.5)
        C_re = (lambda *s: torch.randn(*s, dtype=torch.bfloat16) * 0.2)(D, P) * (P ** -0.5)
        C_im = (lambda *s: torch.randn(*s, dtype=torch.bfloat16) * 0.2)(D, P) * (P ** -0.5)
        log_dt = (lambda *s: torch.randn(*s, dtype=torch.bfloat16) * 0.2)(P) * 0.3 - 2.0
        Dskip = (lambda *s: torch.randn(*s, dtype=torch.bfloat16) * 0.05)(D)
        return {
            "x": _w((lambda *s: torch.randn(*s, dtype=torch.bfloat16) * 0.05)(B, S, D)),
            "Lambda_re": _w(Lambda_re),
            "Lambda_im": _w(Lambda_im),
            "B_re": _w(B_re),
            "B_im": _w(B_im),
            "C_re": _w(C_re),
            "C_im": _w(C_im),
            "log_dt": _w(log_dt),
            "Dskip": _w(Dskip),
        }
    _A_in = _A_setup()

    def _B_setup():
        B, S, Dm, Df = 1, 2048, 512, 1376
        return {
            "x": _w((lambda *s: torch.randn(*s, dtype=torch.bfloat16) * 0.05)(B, S, Dm)),
            "w_gate": _w((lambda *s: torch.randn(*s, dtype=torch.bfloat16) * 0.4)(Dm, Df) * (Dm ** -0.5)),
            "w_up": _w((lambda *s: torch.randn(*s, dtype=torch.bfloat16) * 0.4)(Dm, Df) * (Dm ** -0.5)),
            "w_down": _w((lambda *s: torch.randn(*s, dtype=torch.bfloat16) * 0.4)(Df, Dm) * (Df ** -0.5)),
        }
    _B_in = _B_setup()

    return {
        "A_x": _A_in["x"],
        "A_Lambda_re": _A_in["Lambda_re"],
        "A_Lambda_im": _A_in["Lambda_im"],
        "A_B_re": _A_in["B_re"],
        "A_B_im": _A_in["B_im"],
        "A_C_re": _A_in["C_re"],
        "A_C_im": _A_in["C_im"],
        "A_log_dt": _A_in["log_dt"],
        "A_Dskip": _A_in["Dskip"],
        "B_x": _B_in["x"],
        "B_w_gate": _B_in["w_gate"],
        "B_w_up": _B_in["w_up"],
        "B_w_down": _B_in["w_down"],
    }

def reference(A_x, A_Lambda_re, A_Lambda_im, A_B_re, A_B_im, A_C_re, A_C_im, A_log_dt, A_Dskip, B_x, B_w_gate, B_w_up, B_w_down):
    def _A_ref(x, Lambda_re, Lambda_im, B_re, B_im, C_re, C_im, log_dt, Dskip):
        Bsz, L, H = x.shape
        Lam = Lambda_re + 1j * Lambda_im
        Bmat = B_re + 1j * B_im
        Cmat = C_re + 1j * C_im
        dt = torch.exp(log_dt)
        Lam_bar = torch.exp(dt * Lam)
        B_bar = ((Lam_bar - 1.0) / Lam)[:, None] * Bmat
        Bu = torch.einsum("bld,pd->blp", x + 0j, B_bar)
        h = torch.zeros_like(Bu[:, 0, :])
        hs = []
        for t in range(L):
            h = Lam_bar[None, :] * h + Bu[:, t, :]
            hs.append(h)
        hseq = torch.stack(hs, 1)
        y = 2.0 * torch.einsum("blp,dp->bld", hseq, Cmat).real + x * Dskip[None, None, :]
        return y

    def _B_ref(x, w_gate, w_up, w_down):
        g = torch.einsum("bsd,df->bsf", x, w_gate)
        u = torch.einsum("bsd,df->bsf", x, w_up)
        h = _gelu(g) * u
        return torch.einsum("bsf,fd->bsd", h, w_down)

    x = A_x
    xn = _layernorm(x, axis=-1)
    return x + _A_ref(xn, A_Lambda_re, A_Lambda_im, A_B_re, A_B_im, A_C_re, A_C_im, A_log_dt, A_Dskip) + _B_ref(xn, B_w_gate, B_w_up, B_w_down)
```

```cpp
#include <hip/hip_runtime.h>
#include <hip/hip_bf16.h>
typedef unsigned short u16;
__device__ __forceinline__ float bf2f(u16 v){ return __uint_as_float(((unsigned)v)<<16); }
__device__ __forceinline__ u16 f2bf(float f){ __hip_bfloat16 b = __float2bfloat16(f); return *(u16*)&b; }
__device__ __forceinline__ float rbf(float f){ return bf2f(f2bf(f)); }
constexpr int L=2048, D=512, P=512, F=1376;

__global__ void n_ln(const u16* x, u16* xn){
  int row = blockIdx.x; int t = threadIdx.x;
  float v[8]; float s=0;
  for(int i=0;i<8;i++){ v[i]=bf2f(x[row*D+t*8+i]); s+=v[i]; }
  for(int o=32;o>=1;o>>=1) s+=__shfl_xor(s,o);
  float m=s/D; float q=0;
  for(int i=0;i<8;i++){ float d=v[i]-m; q+=d*d; }
  for(int o=32;o>=1;o>>=1) q+=__shfl_xor(q,o);
  float mb=rbf(m), vb=rbf(q/D);
  float sd=rbf(sqrtf(rbf(vb+1e-5f)));
  for(int i=0;i<8;i++) xn[row*D+t*8+i]=f2bf(rbf(v[i]-mb)/sd);
}
__global__ void n_bu(const u16* xn,const u16* Bre,const u16* Bim,float2* bu){
  int p = blockIdx.x*64+threadIdx.x; int l=blockIdx.y;
  float a=0,b=0;
  for(int d=0;d<D;d++){ float xv=bf2f(xn[l*D+d]); a+=xv*bf2f(Bre[p*D+d]); b+=xv*bf2f(Bim[p*D+d]); }
  bu[l*P+p]=make_float2(a,b);
}
__global__ void n_scan(const u16* lre,const u16* lim,const u16* ldt,float2* bu){
  int p = blockIdx.x*64+threadIdx.x;
  float lr=bf2f(lre[p]), li=bf2f(lim[p]); float dt=rbf(expf(bf2f(ldt[p])));
  float er=expf(dt*lr); float br=er*cosf(dt*li), bi=er*sinf(dt*li);
  float nr=br-1.f, ni=bi; float den=lr*lr+li*li;
  float cr=(nr*lr+ni*li)/den, ci=(ni*lr-nr*li)/den;
  float hr=0,hi=0;
  for(int l=0;l<L;l++){ float2 v=bu[l*P+p]; float ur=cr*v.x-ci*v.y, ui=cr*v.y+ci*v.x;
    float nhr=br*hr-bi*hi+ur, nhi=br*hi+bi*hr+ui; hr=nhr; hi=nhi; bu[l*P+p]=make_float2(hr,hi);}
}
__global__ void n_glu(const u16* xn,const u16* wg,const u16* wu,u16* hg){
  int f = blockIdx.x*32+threadIdx.x; int l=blockIdx.y;
  float a=0,b=0;
  for(int d=0;d<D;d++){ float xv=bf2f(xn[l*D+d]); a+=xv*bf2f(wg[d*F+f]); b+=xv*bf2f(wu[d*F+f]); }
  a=rbf(a); b=rbf(b);
  float t=tanhf(0.7978845608028654f*(a+0.044715f*a*a*a));
  float g=rbf(0.5f*a*(1.f+t));
  hg[l*F+f]=f2bf(g*b);
}
__global__ void n_out(const u16* x,const u16* xn,const float2* h,const u16* Cre,const u16* Cim,const u16* Dk,const u16* hg,const u16* wd,float* out){
  int d = blockIdx.x*64+threadIdx.x; int l=blockIdx.y;
  float a=0;
  for(int p=0;p<P;p++){ float2 v=h[l*P+p]; a+=v.x*bf2f(Cre[d*P+p])-v.y*bf2f(Cim[d*P+p]); }
  float b=0;
  for(int f=0;f<F;f++) b+=bf2f(hg[l*F+f])*bf2f(wd[f*D+d]);
  float y=2.f*a+rbf(bf2f(xn[l*D+d])*bf2f(Dk[d]));
  out[l*D+d]=(bf2f(x[l*D+d])+y)+rbf(b);
}
#define NAIVE_NAME kernel_launch
extern "C" void NAIVE_NAME(void* const* d_in, const int* in_sizes, int n_in, void* d_out, int out_size, void* d_ws, size_t ws_size, hipStream_t stream){
  const u16* x=(const u16*)d_in[0];
  char* ws=(char*)d_ws;
  u16* xn=(u16*)ws; float2* bu=(float2*)(ws+(4<<20)); u16* hg=(u16*)(ws+(16<<20));
  n_ln<<<L,64,0,stream>>>(x,xn);
  n_bu<<<dim3(P/64,L),64,0,stream>>>(xn,(const u16*)d_in[3],(const u16*)d_in[4],bu);
  n_scan<<<P/64,64,0,stream>>>((const u16*)d_in[1],(const u16*)d_in[2],(const u16*)d_in[7],bu);
  n_glu<<<dim3(F/32,L),32,0,stream>>>(xn,(const u16*)d_in[10],(const u16*)d_in[11],hg);
  n_out<<<dim3(D/64,L),64,0,stream>>>(x,xn,bu,(const u16*)d_in[5],(const u16*)d_in[6],(const u16*)d_in[8],hg,(const u16*)d_in[12],(float*)d_out);
}
```

```cpp
#include <hip/hip_runtime.h>
#include <hip/hip_bf16.h>
#include <type_traits>

typedef unsigned short u16;
typedef unsigned long long u64;
typedef short s16x4 __attribute__((ext_vector_type(4)));
typedef __bf16 bf16x2 __attribute__((ext_vector_type(2)));
typedef __bf16 bf16x8 __attribute__((ext_vector_type(8)));
typedef float f32x2 __attribute__((ext_vector_type(2)));
typedef float f32x4 __attribute__((ext_vector_type(4)));
typedef float f32x16 __attribute__((ext_vector_type(16)));
typedef unsigned u32x2 __attribute__((ext_vector_type(2)));
typedef unsigned u32x4 __attribute__((ext_vector_type(4)));
typedef int i32x4 __attribute__((ext_vector_type(4)));
#define LAS __attribute__((address_space(3)))
#define DEVI __device__ __forceinline__

constexpr int SL = 2048, SD = 512, SP = 512, SF = 1376;
constexpr int NKS = 150;
constexpr unsigned WS_EPOCH = 0;
constexpr unsigned WS_GRAN = 4096;
constexpr unsigned WS_STATS = WS_GRAN + 131072;
constexpr unsigned WS_A2 = 262144;
constexpr unsigned A2_BYTES = 64u * NKS * 1024u;
constexpr unsigned WS_BYTES = WS_A2 + A2_BYTES;
constexpr int REG = 40960;
constexpr int LDS_BYTES = 163840;

#define STAMP_DECL
#define STAMP(i) {}
#define STAMP_OUT(k)
#define STAMP_RT0
template <int B, int E, class Fn> DEVI void sfor(Fn&& f) {
  if constexpr (B < E) { f(std::integral_constant<int, B>{}); sfor<B + 1, E>(f); }
}
DEVI i32x4 mk_srd(const void* p, unsigned bytes) {
  u64 a = (u64)p; return (i32x4){(int)(unsigned)a, (int)((unsigned)(a >> 32) & 0xffffu), (int)bytes, 0x00020000};
}
DEVI void dma(i32x4 srd, unsigned voff, unsigned soff, unsigned lds) {
  asm volatile("s_mov_b32 m0, %0\n\ts_nop 0\n\tbuffer_load_dwordx4 %1, %2, %3 offen lds" ::"s"(lds), "v"(voff), "s"(srd), "s"(soff) : "memory");
}
template <int N> DEVI void wait_vm() { asm volatile("s_waitcnt vmcnt(%0)" ::"n"(N) : "memory"); }
DEVI void wait_lgkm0() { asm volatile("s_waitcnt lgkmcnt(0)" ::: "memory"); }
DEVI void bar() { asm volatile("s_barrier" ::: "memory"); }
DEVI float bf2f(u16 v) { return __uint_as_float(((unsigned)v) << 16); }
DEVI unsigned pk2(float a, float b) { f32x2 v = {a, b}; return __builtin_bit_cast(unsigned, __builtin_convertvector(v, bf16x2)); }
DEVI float rbf(float a) { return __uint_as_float(pk2(a, a) << 16); }
DEVI u32x4 lds_r128(unsigned a) { return *(LAS u32x4*)(size_t)a; }
DEVI u32x2 lds_tr(unsigned a) { return __builtin_bit_cast(u32x2, __builtin_amdgcn_ds_read_tr16_b64_v4i16((LAS s16x4*)(size_t)a)); }
DEVI u32x4 lds_tr2(unsigned a, unsigned d) { u32x2 x = lds_tr(a), y = lds_tr(a + d); return (u32x4){x[0], x[1], y[0], y[1]}; }
DEVI f32x16 mfma(u32x4 a, u32x4 b, f32x16 c) {
  return __builtin_amdgcn_mfma_f32_32x32x16_bf16(__builtin_bit_cast(bf16x8, a), __builtin_bit_cast(bf16x8, b), c, 0, 0, 0);
}
DEVI float xhalf(float v) {
  return __shfl_xor(v, 32);
}

__global__ __launch_bounds__(256, 1) void k1(const u16* __restrict__ x, const u16* __restrict__ lam_re, const u16* __restrict__ lam_im,
                                             const u16* __restrict__ Bre, const u16* __restrict__ Bim, const u16* __restrict__ logdt,
                                             const u16* __restrict__ wg, const u16* __restrict__ wu, char* __restrict__ ws) {
  extern __shared__ __attribute__((aligned(16))) char shm[];
  STAMP_DECL STAMP_RT0 STAMP(0)
  const int lane = threadIdx.x & 63;
  const int w = __builtin_amdgcn_readfirstlane(threadIdx.x >> 6);
  const int b = blockIdx.x, xcd = b & 7, idx = b >> 3;
  const int c = (xcd >> 1) * 4 + (idx >> 3), g = (xcd & 1) * 8 + (idx & 7);
  const int rot = (g + 2 * c) & 7;
  const int l5 = lane & 31, h = lane >> 5;
  const unsigned lbase = (unsigned)(size_t)(LAS char*)shm;
  const unsigned myreg = lbase + w * REG;
  const int f0 = (g * 96 < SF - 96) ? g * 96 : SF - 96;

  const i32x4 sx = mk_srd(x, SL * SD * 2), sbr = mk_srd(Bre, SP * SD * 2), sbi = mk_srd(Bim, SP * SD * 2);
  const i32x4 sw = mk_srd((w >> 1) ? wu : wg, SD * SF * 2);
  const __amdgpu_buffer_rsrc_t rws = __builtin_amdgcn_make_buffer_rsrc(ws, 0, WS_BYTES, 0x00020000);

  const int p = g * 32 + l5;
  const float lr = bf2f(lam_re[p]), li = bf2f(lam_im[p]), ldt = bf2f(logdt[p]);
  const u32x2 ep = __builtin_amdgcn_raw_buffer_load_b64(rws, WS_EPOCH + g * 128, 0, 16);

  const unsigned sw0 = ((lane & 7) ^ (lane >> 4)) * 16;
  const unsigned vx0 = (16 * (lane >> 5) + ((lane >> 3) & 3)) * 1024 + sw0, vx1 = vx0 ^ 64;
  const unsigned vb0 = (lane >> 3) * 1024 + sw0, vb1 = vb0 ^ 64;
  unsigned vw[3];
  for (int j = 0; j < 3; ++j) { int n = 64 * j + lane; vw[j] = (n / 12) * (SF * 2) + (n % 12) * 16; }

  const unsigned xrow = (c * 128 + w * 32) * 1024;
  sfor<0, 8>([&](auto V) { constexpr int v = V;
    const unsigned kc = ((v + rot) & 7) * 128;
    sfor<0, 4>([&](auto R) { constexpr int rq = R; dma(sx, (rq & 1) ? vx1 : vx0, xrow + rq * 4096 + kc, myreg + v * 4096 + rq * 1024); });
  });
  auto dma_bs = [&](int v, unsigned dst) {
    const unsigned kc = ((v + rot) & 7) * 128;
    sfor<0, 4>([&](auto R) { constexpr int rq = R; dma(sbr, (rq & 1) ? vb1 : vb0, (g * 32 + rq * 8) * 1024 + kc, dst + rq * 1024); });
    sfor<0, 4>([&](auto R) { constexpr int rq = R; dma(sbi, (rq & 1) ? vb1 : vb0, (g * 32 + rq * 8) * 1024 + kc, dst + 4096 + rq * 1024); });
  };
  dma_bs(w, myreg + 32768);
  STAMP(1)
  auto dma_w = [&](int st) {
    const unsigned k0 = ((((st >> 1) + rot) & 7) * 64 + (st & 1) * 32 + (w & 1) * 16);
    const unsigned so = k0 * (SF * 2) + f0 * 2;
    const unsigned dst = myreg + 8192 + (st % 7) * 3072;
    dma(sw, vw[0], so, dst); dma(sw, vw[1], so, dst + 1024); dma(sw, vw[2], so, dst + 2048);
  };

  unsigned ra[4];
  for (int s = 0; s < 4; ++s) ra[s] = l5 * 128 + (((2 * s + h) ^ ((l5 >> 1) & 7)) * 16);
  u32x4 xf[32];
  float s1[2] = {0.f, 0.f}, s2[2] = {0.f, 0.f};
  const bf16x2 ones = __builtin_bit_cast(bf16x2, 0x3f803f80u);
  sfor<0, 8>([&](auto V) { constexpr int v = V;
    constexpr int extra = (v >= 2 ? 8 : 0) + (v >= 3 ? 3 * ((4 * (v - 2)) / 3) : 0);
    constexpr int issued = 40 + extra;
    wait_vm<issued - 4 * (v + 1)>();
    if constexpr (v==0) STAMP(2)
    sfor<0, 4>([&](auto S) { constexpr int s = S; xf[4 * v + s] = lds_r128(myreg + v * 4096 + ra[s]); });
    sfor<0, 4>([&](auto S) { constexpr int s = S;
      for (int e = 0; e < 4; ++e) { const unsigned tu = xf[4 * v + s][e]; bf16x2 t = __builtin_bit_cast(bf16x2, tu);
        s1[e & 1] = __builtin_amdgcn_fdot2_f32_bf16(t, ones, s1[e & 1], false);
        s2[e & 1] = __builtin_amdgcn_fdot2_f32_bf16(t, t, s2[e & 1], false); }
    });
    wait_lgkm0();
    if constexpr (v == 1) dma_bs(4 + w, myreg);
    if constexpr (v >= 2) {
      constexpr int nb = (4 * (v - 2)) / 3, na = (4 * (v - 1)) / 3 > 7 ? 7 : (4 * (v - 1)) / 3;
      sfor<nb, na>([&](auto ST) { dma_w(ST); });
    }
  });
  STAMP(3)
  float sum = s1[0] + s1[1], sq = s2[0] + s2[1];
  sum += xhalf(sum); sq += xhalf(sq);
  const float mean = sum * (1.f / SD);
  const float var = fmaxf(sq * (1.f / SD) - mean * mean, 0.f);
  const float mb = rbf(mean);
  const float sd = rbf(__builtin_sqrtf(rbf(rbf(var) + 1e-5f)));
  const float rs = 1.f / sd, rc = -mb * rs;
  if (g == 0 && h == 0) {
    const int tau = 16 * ((l5 >> 2) & 1) + (l5 & 3) + 4 * (l5 >> 3);
    __builtin_amdgcn_raw_buffer_store_b64((u32x2){__float_as_uint(rs), __float_as_uint(rc)}, rws, WS_STATS + (c * 128 + w * 32 + tau) * 8, 0, 16);
  }
  const float dt = rbf(expf(ldt));
  const float er = expf(dt * lr);
  const float ar = er * cosf(dt * li), ai = er * sinf(dt * li);
  float kr, ki;
  { const float nr = ar - 1.f, ni = ai, den = lr * lr + li * li; kr = 2.f * (nr * lr + ni * li) / den; ki = 2.f * (ni * lr - nr * li) / den; }
  float a16r = ar, a16i = ai;
  for (int i = 0; i < 4; ++i) { float t = a16r * a16r - a16i * a16i; a16i = 2.f * a16r * a16i; a16r = t; }

  f32x16 vr = {}, vi = {};
  STAMP(4)
  wait_vm<29>(); bar();
  STAMP(5)
  sfor<0, 32>([&](auto S) { constexpr int s = S; constexpr int v = s >> 2;
    if constexpr (s == 16) { wait_vm<21>(); bar(); }
    for (int e = 0; e < 4; ++e) { unsigned u = xf[s][e];
      xf[s][e] = pk2(__builtin_fmaf(__uint_as_float(u << 16), rs, rc), __builtin_fmaf(__uint_as_float(u & 0xffff0000u), rs, rc)); }
    const unsigned bb = lbase + (v & 3) * REG + (v < 4 ? 32768 : 0) + ra[s & 3];
    u32x4 br = lds_r128(bb), bi = lds_r128(bb + 4096);
    vr = mfma(xf[s], br, vr); vi = mfma(xf[s], bi, vi);
  });
  STAMP(6)
  {
    float er_ = 0.f, ei_ = 0.f;
    for (int i = 0; i < 16; ++i) { float t = __builtin_fmaf(ar, er_, __builtin_fmaf(-ai, ei_, vr[i])); ei_ = __builtin_fmaf(ar, ei_, __builtin_fmaf(ai, er_, vi[i])); er_ = t; }
    *(LAS f32x2*)(size_t)(myreg + 29696 + lane * 8) = (f32x2){er_, ei_};
  }
  wait_lgkm0(); wait_vm<18>(); bar();
  float prer = 0.f, prei = 0.f, pwr = 1.f, pwi = 0.f, a128r, a128i;
  {
    float rr = 0.f, ri = 0.f, qr = 1.f, qi = 0.f;
    const int myseg = 2 * w + h;
    for (int s = 0; s < 8; ++s) {
      f32x2 e = *(LAS f32x2*)(size_t)(lbase + (s >> 1) * REG + 29696 + ((s & 1) * 32 + l5) * 8);
      if (s == myseg) { prer = rr; prei = ri; pwr = qr; pwi = qi; }
      float t = __builtin_fmaf(a16r, rr, __builtin_fmaf(-a16i, ri, e[0])); ri = __builtin_fmaf(a16r, ri, __builtin_fmaf(a16i, rr, e[1])); rr = t;
      t = a16r * qr - a16i * qi; qi = a16r * qi + a16i * qr; qr = t;
    }
    a128r = qr; a128i = qi;
    const u64 tag = (((u64)ep[1] << 32) | ep[0]) + 1;
    if (w == 0 && h == 0)
      __builtin_amdgcn_raw_buffer_store_b128((u32x4){__float_as_uint(rr), (unsigned)tag, __float_as_uint(ri), (unsigned)(tag >> 32)}, rws,
                                             WS_GRAN + ((g * 16 + c) * 32 + l5) * 16, 0, 16);
  }

  STAMP(7)
  f32x16 ag[3] = {}, au[3] = {};
  const int q = (lane & 15) >> 2, pp = lane & 3, blk = (lane >> 4) & 1;
  const unsigned ta = lbase + 8192 + (8 * h + q) * 192 + blk * 32 + (((pp & 1) << 1) | (pp >> 1)) * 8;
  u32x4 wf[2][6];
  auto rd_w = [&](auto KS) { constexpr int ks = KS; constexpr int st = ks >> 1;
    sfor<0, 6>([&](auto J) { constexpr int j = J;
      wf[ks & 1][j] = lds_tr2(ta + ((j / 3) * 2 + (ks & 1)) * REG + (st % 7) * 3072 + (j % 3) * 64, 768); });
  };
  rd_w(std::integral_constant<int, 0>{});
  sfor<0, 32>([&](auto KS) { constexpr int ks = KS; constexpr int st = ks >> 1;
    if constexpr (ks==8) STAMP(8)
    if constexpr (ks==16) STAMP(9)
    if constexpr (ks==24) STAMP(10)
    if constexpr (ks + 1 < 32) {
      if constexpr (ks & 1) {
        constexpr int issued_st = (st + 6 > 16) ? 16 : (st + 6 < 7 ? 7 : st + 6);
        wait_vm<3 * (issued_st - (st + 2))>(); bar();
        if constexpr (st >= 1 && st + 6 < 16) dma_w(st + 6);
      }
      rd_w(std::integral_constant<int, ks + 1>{});
    }
    sfor<0, 3>([&](auto U) { constexpr int u = U;
      ag[u] = mfma(wf[ks & 1][u], xf[ks], ag[u]); au[u] = mfma(wf[ks & 1][3 + u], xf[ks], au[u]); });
  });

  const unsigned a2base = WS_A2 + (unsigned)(c * 4 + w) * (NKS * 1024) + lane * 16;
  STAMP(11)
  sfor<0, 3>([&](auto U) { constexpr int u = U;
    unsigned o[8];
    for (int i = 0; i < 8; ++i) { float r[2];
      for (int e = 0; e < 2; ++e) { float gv = ag[u][2 * i + e], uv = au[u][2 * i + e];
        float z = gv * __builtin_fmaf(gv * gv, -0.1029432397f, -2.302208198f);
        r[e] = gv * uv * __builtin_amdgcn_rcpf(1.f + __builtin_amdgcn_exp2f(z)); }
      o[i] = pk2(r[0], r[1]); }
    const unsigned ks = 64 + (f0 >> 4) + 2 * u;
    __builtin_amdgcn_raw_buffer_store_b128((u32x4){o[0], o[1], o[2], o[3]}, rws, a2base, ks * 1024, 0);
    __builtin_amdgcn_raw_buffer_store_b128((u32x4){o[4], o[5], o[6], o[7]}, rws, a2base, ks * 1024 + 1024, 0);
  });

  STAMP(12)
  float cr = 0.f, ci = 0.f;
  {
    const u64 tag = (((u64)ep[1] << 32) | ep[0]) + 1;
    const unsigned tlo = (unsigned)tag, thi = (unsigned)(tag >> 32);
    u32x4 ge[8];
    for (unsigned spins = 0; spins < (1u << 20); ++spins) {
      bool ok = true;
      for (int i = 0; i < 8; ++i) { ge[i] = __builtin_amdgcn_raw_buffer_load_b128(rws, WS_GRAN + ((g * 16 + h) * 32 + l5) * 16 + i * 1024, 0, 16);
        ok &= (2 * i + h >= c) || (ge[i][1] == tlo && ge[i][3] == thi); }
      if (__all(ok)) break;
      __builtin_amdgcn_s_sleep(1);
    }
    const float a256r = a128r * a128r - a128i * a128i, a256i = 2.f * a128r * a128i;
    for (int i = 0; i < 8; ++i) {
      float t = __builtin_fmaf(a256r, cr, __builtin_fmaf(-a256i, ci, __uint_as_float(ge[i][0])));
      float t2 = __builtin_fmaf(a256r, ci, __builtin_fmaf(a256i, cr, __uint_as_float(ge[i][2])));
      if (2 * i + h < c) { cr = t; ci = t2; }
    }
    if (((c - 1 - h) & 1) != 0) { float t = a128r * cr - a128i * ci; ci = a128r * ci + a128i * cr; cr = t; }
    cr += xhalf(cr); ci += xhalf(ci);
    if (c == 15 && w == 0 && lane == 0) __builtin_amdgcn_raw_buffer_store_b64((u32x2){tlo, thi}, rws, WS_EPOCH + g * 128, 0, 16);
  }
  STAMP(13)
  {
    float gr = prer + pwr * cr - pwi * ci, gi = prei + pwr * ci + pwi * cr;
    float hr[16], hi[16];
    for (int i = 0; i < 16; ++i) {
      float t = __builtin_fmaf(ar, gr, __builtin_fmaf(-ai, gi, vr[i])); gi = __builtin_fmaf(ar, gi, __builtin_fmaf(ai, gr, vi[i])); gr = t;
      hr[i] = kr * gr - ki * gi; hi[i] = -(kr * gi + ki * gr);
    }
    const unsigned tw = myreg + 32768 + l5 * 64 + h * 32;
    *(LAS u32x4*)(size_t)(tw) = (u32x4){pk2(hr[0], hr[1]), pk2(hr[2], hr[3]), pk2(hr[4], hr[5]), pk2(hr[6], hr[7])};
    *(LAS u32x4*)(size_t)(tw + 16) = (u32x4){pk2(hr[8], hr[9]), pk2(hr[10], hr[11]), pk2(hr[12], hr[13]), pk2(hr[14], hr[15])};
    *(LAS u32x4*)(size_t)(tw + 2048) = (u32x4){pk2(hi[0], hi[1]), pk2(hi[2], hi[3]), pk2(hi[4], hi[5]), pk2(hi[6], hi[7])};
    *(LAS u32x4*)(size_t)(tw + 2064) = (u32x4){pk2(hi[8], hi[9]), pk2(hi[10], hi[11]), pk2(hi[12], hi[13]), pk2(hi[14], hi[15])};
    wait_lgkm0();
    const unsigned tr = myreg + 32768 + (8 * h + q) * 64 + (16 * (pp & 1) + 8 * blk + 4 * (pp >> 1)) * 2;
    sfor<0, 4>([&](auto J) { constexpr int j = J;
      u32x4 fr = lds_tr2(tr + (j >> 1) * 2048 + (j & 1) * 1024, 256);
      __builtin_amdgcn_raw_buffer_store_b128(fr, rws, a2base, ((j >> 1) * 32 + 2 * g + (j & 1)) * 1024, 0);
    });
  }
  STAMP(14)
  wait_vm<0>();
  STAMP(15)
  STAMP_OUT(0)
}

__global__ __launch_bounds__(256, 1) void k2(const u16* __restrict__ x, const u16* __restrict__ Cre, const u16* __restrict__ Cim,
                                             const u16* __restrict__ dskip, const u16* __restrict__ wd, const char* __restrict__ ws,
                                             float* __restrict__ out) {
  extern __shared__ __attribute__((aligned(16))) char shm[];
  const int lane = threadIdx.x & 63;
  const int w = __builtin_amdgcn_readfirstlane(threadIdx.x >> 6);
  const int b = blockIdx.x, xcd = b & 7, idx = b >> 3;
  const int rt = xcd * 4 + (idx >> 3), ct = idx & 7;
  STAMP_DECL STAMP_RT0 STAMP(0)
  const int l5 = lane & 31, h = lane >> 5;
  const unsigned lbase = (unsigned)(size_t)(LAS char*)shm;
  const unsigned myreg = lbase + w * REG;
  const i32x4 scr = mk_srd(Cre, SD * SP * 2), sci = mk_srd(Cim, SD * SP * 2), swd = mk_srd(wd, SF * SD * 2), sa = mk_srd(ws + WS_A2, A2_BYTES);
  const unsigned sw0 = ((lane & 7) ^ (lane >> 4)) * 16;
  const unsigned vb0 = (lane >> 3) * 1024 + sw0, vb1 = vb0 ^ 64;
  const unsigned vd = (lane >> 3) * 1024 + (((lane & 7) ^ (((lane >> 4) & 1) << 2)) * 16);
  const unsigned va = lane * 16;
  constexpr int RING = 40, NP = 64 + 88;
  auto issue = [&](auto N) { constexpr int n = N;
    const unsigned dst = myreg + (n % RING) * 1024;
    if constexpr (n < 64) { constexpr int G = n >> 4, r = n & 15;
      if constexpr (r < 8) dma((G >> 1) ? sci : scr, (r & 1) ? vb1 : vb0, (ct * 64 + r * 8) * 1024 + w * 256 + (G & 1) * 128, dst);
      else dma(sa, va, ((2 * rt + ((r - 8) & 1)) * NKS + (G >> 1) * 32 + 8 * w + 4 * (G & 1) + ((r - 8) >> 1)) * 1024, dst);
    } else { constexpr int m = n - 64, ks = m >> 2, r = m & 3;
      if constexpr (r < 2) dma(swd, vd, ((22 * w + ks) * 16 + r * 8) * 1024 + ct * 128, dst);
      else dma(sa, va, ((2 * rt + (r - 2)) * NKS + 64 + 22 * w + ks) * 1024, dst);
    }
  };
  sfor<0, RING>(issue);
  STAMP(1)
  unsigned ra[4];
  for (int s = 0; s < 4; ++s) ra[s] = l5 * 128 + (((2 * s + h) ^ ((l5 >> 1) & 7)) * 16);
  const int q = (lane & 15) >> 2, pp = lane & 3, blk = (lane >> 4) & 1;
  const unsigned td0 = (8 * h + q) * 128 + (((blk * 2 + (pp >> 1)) ^ ((q >> 1) << 2)) * 16) + (pp & 1) * 8, td1 = td0 ^ 64;
  f32x16 acc[2][2] = {};
  sfor<0, 38>([&](auto T) { constexpr int t = T;
    constexpr int need = t < 16 ? (t >> 2) * 16 + 8 + 2 * (t & 3) + 1 : 64 + (t - 16) * 4 + 3;
    constexpr int freed_prev = t == 0 ? 0 : (t - 1 < 16 ? (((t - 1) & 3) == 3 ? ((t - 1) >> 2) * 16 + 16 : ((t - 1) >> 2) * 16) : 64 + (t - 16) * 4);
    constexpr int issued = (freed_prev + RING > NP) ? NP : freed_prev + RING;
    wait_vm<issued - need - 1>();
    if constexpr ((t&3)==0) STAMP(2+(t>>2))
    u32x4 af[2], bf[2];
    if constexpr (t < 16) { constexpr int G = t >> 2, j = t & 3;
      for (int cb = 0; cb < 2; ++cb) bf[cb] = lds_r128(myreg + ((G * 16 + cb * 4) % RING) * 1024 + ra[j]);
      for (int rb = 0; rb < 2; ++rb) af[rb] = lds_r128(myreg + ((G * 16 + 8 + 2 * j + rb) % RING) * 1024 + va);
    } else { constexpr int n0 = 64 + (t - 16) * 4;
      bf[0] = lds_tr2(myreg + (n0 % RING) * 1024 + td0, 512); bf[1] = lds_tr2(myreg + (n0 % RING) * 1024 + td1, 512);
      for (int rb = 0; rb < 2; ++rb) af[rb] = lds_r128(myreg + ((n0 + 2 + rb) % RING) * 1024 + va);
    }
    wait_lgkm0();
    constexpr int freed = t < 16 ? ((t & 3) == 3 ? (t >> 2) * 16 + 16 : (t >> 2) * 16) : 64 + (t - 15) * 4;
    constexpr int issued2 = (freed + RING > NP) ? NP : freed + RING;
    sfor<issued, issued2>(issue);
    if (t < 36 || w < 3) {
      for (int cb = 0; cb < 2; ++cb) for (int rb = 0; rb < 2; ++rb) acc[cb][rb] = mfma(bf[cb], af[rb], acc[cb][rb]);
    }
  });
  STAMP(12)
  for (int cb = 0; cb < 2; ++cb) for (int rb = 0; rb < 2; ++rb) for (int rq = 0; rq < 4; ++rq) {
    const int chunk = cb * 8 + rq * 2 + h;
    *(LAS f32x4*)(size_t)(myreg + (rb * 32 + l5) * 256 + ((chunk ^ (l5 & 7)) * 16)) =
        (f32x4){acc[cb][rb][4 * rq], acc[cb][rb][4 * rq + 1], acc[cb][rb][4 * rq + 2], acc[cb][rb][4 * rq + 3]};
  }
  wait_lgkm0(); bar();
  STAMP(13)
  const int tid = threadIdx.x, ch = tid & 15;
  const u32x2 dk = *(const u32x2*)(dskip + ct * 64 + ch * 4);
  for (int it = 0; it < 4; ++it) {
    const int lam = it * 16 + (tid >> 4), r5 = lam & 31;
    const int row = rt * 64 + (lam >> 5) * 32 + 16 * ((r5 >> 2) & 1) + (r5 & 3) + 4 * (r5 >> 3);
    f32x4 s = {};
    for (int ww = 0; ww < 4; ++ww) s += *(LAS f32x4*)(size_t)(lbase + ww * REG + lam * 256 + ((ch ^ (lam & 7)) * 16));
    const u32x2 xv = *(const u32x2*)(x + row * SD + ct * 64 + ch * 4);
    const f32x2 st = *(const f32x2*)(ws + WS_STATS + row * 8);
    f32x4 o;
    for (int e = 0; e < 4; ++e) {
      const float xe = __uint_as_float((e & 1) ? (xv[e >> 1] & 0xffff0000u) : (xv[e >> 1] << 16));
      const float de = __uint_as_float((e & 1) ? (dk[e >> 1] & 0xffff0000u) : (dk[e >> 1] << 16));
      o[e] = xe + (s[e] + rbf(rbf(__builtin_fmaf(xe, st[0], st[1])) * de));
    }
    *(f32x4*)(out + row * SD + ct * 64 + ch * 4) = o;
  }
  STAMP(14)
  wait_vm<0>();
  STAMP(15)
  STAMP_OUT(1)
}

extern "C" void kernel_launch(void* const* d_in, const int* in_sizes, int n_in, void* d_out, int out_size, void* d_ws, size_t ws_size,
                              hipStream_t stream) {
  (void)hipFuncSetAttribute((const void*)k1, hipFuncAttributeMaxDynamicSharedMemorySize, LDS_BYTES);
  (void)hipFuncSetAttribute((const void*)k2, hipFuncAttributeMaxDynamicSharedMemorySize, LDS_BYTES);
  k1<<<256, 256, LDS_BYTES, stream>>>((const u16*)d_in[0], (const u16*)d_in[1], (const u16*)d_in[2], (const u16*)d_in[3], (const u16*)d_in[4],
                                      (const u16*)d_in[7], (const u16*)d_in[10], (const u16*)d_in[11], (char*)d_ws);
  k2<<<256, 256, LDS_BYTES, stream>>>((const u16*)d_in[0], (const u16*)d_in[5], (const u16*)d_in[6], (const u16*)d_in[8], (const u16*)d_in[12],
                                      (const char*)d_ws, (float*)d_out);
}
```

```cpp
#include <hip/hip_runtime.h>
#include <hip/hip_bf16.h>
#include <type_traits>

typedef unsigned short u16;
typedef unsigned long long u64;
typedef short s16x4 __attribute__((ext_vector_type(4)));
typedef __bf16 bf16x2 __attribute__((ext_vector_type(2)));
typedef __bf16 bf16x8 __attribute__((ext_vector_type(8)));
typedef float f32x2 __attribute__((ext_vector_type(2)));
typedef float f32x4 __attribute__((ext_vector_type(4)));
typedef float f32x16 __attribute__((ext_vector_type(16)));
typedef unsigned u32x2 __attribute__((ext_vector_type(2)));
typedef unsigned u32x4 __attribute__((ext_vector_type(4)));
typedef int i32x4 __attribute__((ext_vector_type(4)));
#define LAS __attribute__((address_space(3)))
#define DEVI __device__ __forceinline__

constexpr int SL = 2048, SD = 512, SP = 512, SF = 1376;
constexpr int NKS = 150;
constexpr unsigned WS_E1 = 0;
constexpr unsigned WS_E2 = 2048;
constexpr unsigned WS_GRAN = 32768;
constexpr unsigned WS_FLAG = 262144;
constexpr unsigned WS_A2 = 524288;
constexpr unsigned A2_BYTES = 64u * NKS * 1024u;
constexpr unsigned WS_BYTES = WS_A2 + A2_BYTES;
constexpr unsigned OOB = 0x40000000u;
constexpr int SLOT = 49408;
constexpr int OFF_B = 16384, OFF_WG = 24576, OFF_WU = 36864 + 32;
constexpr int MISC = 3 * SLOT;
constexpr int M_STAT = MISC, M_RS = MISC + 2048, M_CNT = MISC + 2560, M_TCH = MISC + 2816, M_T = MISC + 3072;
constexpr int LDS_BYTES = 163840;
constexpr unsigned SPIN_MAX = 1u << 16;
constexpr int hcell_c(int e) { return e < 6 ? 2 * SLOT + e * 2048 : (e - 6) * 2048; }
constexpr int cgrp_c(int q) { return q == 0 ? 32768 : SLOT + (q - 1) * 4096; }

#define STAMP_DECL
#define STAMP(i) {}
#define TICK(i) {}
#define STAMP_OUT

template <int B, int E, class Fn> DEVI void sfor(Fn&& f) {
  if constexpr (B < E) { f(std::integral_constant<int, B>{}); sfor<B + 1, E>(f); }
}
DEVI i32x4 mk_srd(const void* p, unsigned bytes) {
  u64 a = (u64)p; return (i32x4){(int)(unsigned)a, (int)((unsigned)(a >> 32) & 0xffffu), (int)bytes, 0x00020000};
}
DEVI void dma(i32x4 srd, unsigned voff, unsigned soff, unsigned lds) {
  asm volatile("s_mov_b32 m0, %0\n\ts_nop 0\n\tbuffer_load_dwordx4 %1, %2, %3 offen lds" ::"s"(lds), "v"(voff), "s"(srd), "s"(soff) : "memory");
}
template <int OFF> DEVI void ld128_sc1(u32x4& d, i32x4 srd, unsigned voff, unsigned soff) {
  asm volatile("buffer_load_dwordx4 %0, %1, %2, %3 offen offset:%4 sc1" : "=v"(d) : "v"(voff), "s"(srd), "s"(soff), "n"(OFF) : "memory");
}
DEVI void ld64_sc1(u32x2& d, i32x4 srd, unsigned voff, unsigned soff) {
  asm volatile("buffer_load_dwordx2 %0, %1, %2, %3 offen sc1" : "=v"(d) : "v"(voff), "s"(srd), "s"(soff) : "memory");
}
DEVI void ld64(u32x2& d, i32x4 srd, unsigned voff, unsigned soff) {
  asm volatile("buffer_load_dwordx2 %0, %1, %2, %3 offen" : "=v"(d) : "v"(voff), "s"(srd), "s"(soff) : "memory");
}
DEVI void ld16(unsigned& d, i32x4 srd, unsigned voff) {
  asm volatile("buffer_load_ushort %0, %1, %2, 0 offen" : "=v"(d) : "v"(voff), "s"(srd) : "memory");
}
template <int N> DEVI void wait_vm() { asm volatile("s_waitcnt vmcnt(%0)" ::"n"(N) : "memory"); }
template <class T> DEVI void pin(T& a) { asm volatile("" : "+v"(a)::"memory"); }
template <class T> DEVI void use(const T& a) { asm volatile("" ::"v"(a) : "memory"); }
DEVI int lnd(int a) { asm volatile("" : "+s"(a)); return a; }
DEVI void wait_lgkm0() { asm volatile("s_waitcnt lgkmcnt(0)" ::: "memory"); }
DEVI void bar() { asm volatile("s_barrier" ::: "memory"); }
DEVI void sb() { __builtin_amdgcn_sched_barrier(0); }
DEVI float lo2f(unsigned u) { return __uint_as_float(u << 16); }
DEVI float hi2f(unsigned u) { return __uint_as_float(u & 0xffff0000u); }
DEVI unsigned pk2(float a, float b) { f32x2 v = {a, b}; return __builtin_bit_cast(unsigned, __builtin_convertvector(v, bf16x2)); }
DEVI float rbf(float a) { return lo2f(pk2(a, a)); }
DEVI u32x4 lds_r128(unsigned a) { return *(LAS u32x4*)(size_t)a; }
DEVI u32x2 lds_tr(unsigned a) { return __builtin_bit_cast(u32x2, __builtin_amdgcn_ds_read_tr16_b64_v4i16((LAS s16x4*)(size_t)a)); }
DEVI u32x4 lds_tr2(unsigned a, unsigned d) { u32x2 x = lds_tr(a), y = lds_tr(a + d); return (u32x4){x[0], x[1], y[0], y[1]}; }
DEVI f32x16 mfma(u32x4 a, u32x4 b, f32x16 c) {
  return __builtin_amdgcn_mfma_f32_32x32x16_bf16(__builtin_bit_cast(bf16x8, a), __builtin_bit_cast(bf16x8, b), c, 0, 0, 0);
}
DEVI float dot2(unsigned a, unsigned b, float c) {
  return __builtin_amdgcn_fdot2_f32_bf16(__builtin_bit_cast(bf16x2, a), __builtin_bit_cast(bf16x2, b), c, false);
}
DEVI void swap16(float& a, float& b) { auto r = __builtin_amdgcn_permlane16_swap(__float_as_uint(a), __float_as_uint(b), false, false); a = __uint_as_float(r[0]); b = __uint_as_float(r[1]); }
DEVI void swap32(float& a, float& b) { auto r = __builtin_amdgcn_permlane32_swap(__float_as_uint(a), __float_as_uint(b), false, false); a = __uint_as_float(r[0]); b = __uint_as_float(r[1]); }
DEVI float sum32(float v) { float a = v, b = v; swap32(a, b); return a + b; }
DEVI float sum16(float v) { float a = v, b = v; swap16(a, b); return a + b; }
DEVI void cmul(float& dr, float& di, float ar, float ai, float br, float bi) { float t = ar * br - ai * bi; di = ar * bi + ai * br; dr = t; }
DEVI void cfma(float& hr, float& hi, float ar, float ai, float br, float bi) {
  float t = __builtin_fmaf(ar, hr, __builtin_fmaf(-ai, hi, br)); hi = __builtin_fmaf(ar, hi, __builtin_fmaf(ai, hr, bi)); hr = t;
}

template <int OFF> DEVI void dmao(i32x4 srd, unsigned voff, unsigned soff) {
  asm volatile("buffer_load_dwordx4 %0, %1, %2 offen offset:%3 lds" ::"v"(voff), "s"(srd), "s"(soff), "n"(OFF) : "memory");
}
DEVI void setm0(unsigned lds) { asm volatile("s_mov_b32 m0, %0\n\ts_nop 0" ::"s"(lds) : "memory"); }
DEVI void touch(i32x4 srd, unsigned voff, unsigned soff) {
  asm volatile("buffer_load_dword %0, %1, %2 offen lds" ::"v"(voff), "s"(srd), "s"(soff) : "memory");
}
DEVI f32x4 mfma16(u32x4 a, u32x4 b, f32x4 c) {
  return __builtin_amdgcn_mfma_f32_16x16x32_bf16(__builtin_bit_cast(bf16x8, a), __builtin_bit_cast(bf16x8, b), c, 0, 0, 0);
}
DEVI float vadd(float a, float b) { float r; asm("v_add_f32 %0, %1, %2" : "=v"(r) : "v"(a), "v"(b)); return r; }
DEVI float vfma(float a, float b, float c) { float r; asm("v_fma_f32 %0, %1, %2, %3" : "=v"(r) : "v"(a), "v"(b), "v"(c)); return r; }
DEVI f32x2 pfma(f32x2 a, f32x2 b, f32x2 c) { return __builtin_elementwise_fma(a, b, c); }
DEVI f32x2 sp(float a) { return (f32x2){a, a}; }
DEVI void cfma2(f32x2& hr, f32x2& hi, float ar, float ai, f32x2 br, f32x2 bi) {
  f32x2 t = pfma(sp(ar), hr, pfma(sp(-ai), hi, br)); hi = pfma(sp(ar), hi, pfma(sp(ai), hr, bi)); hr = t;
}
#define SLP 4
#define ROT2 0
#define TDELAY 0
#define NTOUCH 2
constexpr int WN = 12;

__global__ __launch_bounds__(512, 2) void fused(const u16* __restrict__ x, const u16* __restrict__ lam_re, const u16* __restrict__ lam_im,
                                                const u16* __restrict__ Bre, const u16* __restrict__ Bim, const u16* __restrict__ Cre,
                                                const u16* __restrict__ Cim, const u16* __restrict__ logdt, const u16* __restrict__ dskip,
                                                const u16* __restrict__ wg, const u16* __restrict__ wu, const u16* __restrict__ wd,
                                                char* __restrict__ ws, float* __restrict__ out) {
  extern __shared__ __attribute__((aligned(16))) char shm[];
  STAMP_DECL STAMP(0)
  const int lane = threadIdx.x & 63, tid = threadIdx.x;
  const int j = __builtin_amdgcn_readfirstlane(threadIdx.x >> 6), w = j & 3;
  const int b = blockIdx.x, xcd = b & 7, idx = b >> 3;
  const int c = (xcd >> 1) * 4 + (idx >> 3), rsel = xcd & 1, ct = idx & 7, g = rsel * 8 + ct;
#define ROT 0
  const int rot = ROT;
  const int l5 = lane & 31, h = lane >> 5, blk = (lane >> 4) & 1, q = (lane & 15) >> 2, pp = lane & 3;
  const i32x4 sx = mk_srd(x, SL * SD * 2);
  const i32x4 sws = mk_srd(ws, WS_BYTES);
  const __amdgpu_buffer_rsrc_t rws = __builtin_amdgcn_make_buffer_rsrc(ws, 0, WS_BYTES, 0x00020000);
  const unsigned sw0 = ((lane & 7) ^ (lane >> 4)) * 16;
  const unsigned va = lane * 16;
  const unsigned flagb = WS_FLAG + c * 16384;
  const unsigned a2c = WS_A2 + (unsigned)(c * 4 + 2 * rsel) * (NKS * 1024);
  u32x2 ep2;
  ld64_sc1(ep2, sws, 0, WS_E2 + c * 128);
  u64 tag2 = 0;
  auto poll = [&](unsigned fo, int n) {
#pragma unroll 1
    for (unsigned spins = 0; spins < SPIN_MAX; ++spins) {
      u32x2 fv;
      ld64_sc1(fv, sws, lane < n ? fo : OOB, flagb); wait_vm<0>(); pin(fv);
      const bool ok = lane >= n || (fv[0] == (unsigned)tag2 && fv[1] == (unsigned)(tag2 >> 32));
      if (__all(ok)) break;
      __builtin_amdgcn_s_sleep(SLP);
    }
  };
  f32x16 acc2[2][2] = {};
  u32x2 xv[2], dk;
  const int trow = 64 * ((tid >> 6) & 1) + 16 * ((tid >> 4) & 1) + 8 * rsel + 2 * (tid >> 7) + ((tid >> 5) & 1);
  const int irow = 64 * ((tid >> 6) & 1) + 16 * rsel + 4 * (tid >> 7) + ((tid >> 4) & 3);
  auto ld_skip = [&]() {
    const unsigned vo = trow * 1024 + (tid & 15) * 8;
    ld64(xv[0], sx, vo, c * 131072 + ct * 128); ld64(xv[1], sx, vo, c * 131072 + 32768 + ct * 128);
    ld64(dk, mk_srd(dskip, SD * 2), (tid & 15) * 8, ct * 128);
  };
  unsigned pa[2];

  if (j >= 4) {
    const i32x4 sbm = mk_srd((w >> 1) ? Bim : Bre, SP * SD * 2);
    const i32x4 sw = mk_srd((w >> 1) ? wu : wg, g == 15 ? 0 : SD * SF * 2);
    unsigned vx[4], vb[2], vw[6];
    for (int r = 0; r < 4; ++r) vx[r] = (64 * (lane >> 5) + 16 * ((lane >> 3) & 1) + ((lane >> 4) & 1) + r) * 1024 + (sw0 ^ ((r & 1) * 64));
    for (int r = 0; r < 2; ++r) vb[r] = ((lane >> 3) + 7 * r) * 1024 + (sw0 ^ (r * 64));
    for (int i = 0; i < 6; ++i) { int n = 64 * i + lane; vw[i] = (n / 12) * (SF * 2) + (n % 12) * 16 - (i % 3) * 1024; }
    const unsigned so_x = (c * 128 + 32 * (w & 1) + 8 * (w >> 1)) * 1024;
    const unsigned so_b = (g * 32 + (w & 1) * 16) * 1024;
    const unsigned so_w = (w & 1) * 32 * (SF * 2) + g * 192;
    const unsigned ld_w = ((w >> 1) ? OFF_WU : OFF_WG) + (w & 1) * 6144;
    auto st_first = [&](int v, unsigned slot) {
      const unsigned kc = (v + rot) & 7;
      setm0(slot + w * 4096);
      { const unsigned so = so_x + kc * 128; dmao<0>(sx, vx[0], so); dmao<1024>(sx, vx[1], so); dmao<2048>(sx, vx[2], so); dmao<3072>(sx, vx[3], so); }
      setm0(slot + OFF_B + w * 2048);
      { const unsigned so = so_b + kc * 128; dmao<0>(sbm, vb[0], so); dmao<1024>(sbm, vb[1], so); }
      setm0(slot + ld_w);
      { const unsigned so = so_w + kc * (64 * SF * 2); dmao<0>(sw, vw[0], so); dmao<1024>(sw, vw[1], so); dmao<2048>(sw, vw[2], so); }
    };
    auto st_rest = [&](int v, unsigned slot) {
      const unsigned kc = (v + rot) & 7;
      setm0(slot + ld_w + 3072);
      const unsigned so = so_w + kc * (64 * SF * 2); dmao<0>(sw, vw[3], so); dmao<1024>(sw, vw[4], so); dmao<2048>(sw, vw[5], so);
    };
    st_first(0, 0); st_rest(0, 0);
    st_first(1, SLOT);
    STAMP(1)
    if (tid == 256) *(LAS unsigned*)(size_t)M_CNT = 0;
    const i32x4 swd = mk_srd(wd, SF * SD * 2);
    unsigned vd[2];
    for (int r = 0; r < 2; ++r) vd[r] = ((lane >> 3) + 7 * r) * 1024 + (((lane & 7) ^ (((lane >> 4) & 1) << 2)) * 16);
    const int kb = 22 * w - (w == 3), cnt = w < 2 ? 22 : 21, r2 = ROT2 ? (ct * 11) >> 2 : 0;
    auto kstep = [&](int m) { if (!ROT2) return m; int mm = m + lnd(r2); mm = mm >= 22 ? mm - 22 : mm; return mm; };
    const unsigned hb0 = w * 12288, hb1 = w * 8192, so_wd = kb * 16384 + ct * 128;
    auto dma_wd = [&](int m, int e) {
      const int mm = kstep(m);
      const unsigned so = mm < cnt ? lnd(so_wd) + mm * 16384 : OOB;
      setm0((e < 6 ? hb0 : hb1) + hcell_c(e)); dmao<0>(swd, vd[0], so); dmao<1024>(swd, vd[1], so);
    };
    float s1[4] = {}, s2[4] = {};
    auto stats = [&](unsigned slot) {
      u32x4 r[4];
      for (int i = 0; i < 4; ++i) r[i] = lds_r128(slot + w * 4096 + i * 1024 + va);
      for (int e = 0; e < 4; ++e) for (int i = 0; i < 4; ++i) { const unsigned u = r[i][e];
        const float lo = lo2f(u), hi = hi2f(u);
        s1[i] = vadd(s1[i], lo); s2[i] = vfma(lo, lo, s2[i]); s1[i] = vadd(s1[i], hi); s2[i] = vfma(hi, hi, s2[i]); }
    };
    unsigned slot = 0, slot1 = SLOT, slot2 = 2 * SLOT;
#pragma unroll 1
    for (int v = 0; v < 6; ++v) {
      wait_vm<17>(); stats(slot); wait_lgkm0(); wait_vm<9>(); bar();
      st_rest(v + 1, slot1); st_first(v + 2, slot2);
      const unsigned t = slot; slot = slot1; slot1 = slot2; slot2 = t;
    }
    pin(ep2); tag2 = (((u64)ep2[1] << 32) | ep2[0]) + 1;
    wait_vm<17>(); stats(0); wait_vm<9>(); bar();
    st_rest(7, SLOT); sfor<0, 5>([&](auto E) { constexpr int e = E; dma_wd(10 + e, e); });
    wait_vm<18>(); stats(SLOT);
    for (int o = 1; o < 8; o <<= 1) for (int i = 0; i < 4; ++i) { s1[i] += __shfl_xor(s1[i], o); s2[i] += __shfl_xor(s2[i], o); }
    { const int i = lane & 3;
      const float sm = i == 0 ? s1[0] : i == 1 ? s1[1] : i == 2 ? s1[2] : s1[3], sq = i == 0 ? s2[0] : i == 1 ? s2[1] : i == 2 ? s2[2] : s2[3];
      const float mean = sm * (1.f / SD);
      const float var = fmaxf(sq * (1.f / SD) - mean * mean, 0.f);
      const float sd = rbf(__builtin_sqrtf(rbf(rbf(var) + 1e-5f)));
      const float rs = 1.f / sd; const unsigned nm = pk2(-mean, -mean);
      const int ti = 64 * (lane >> 5) + 32 * (w & 1) + 16 * (w >> 1) + 4 * i + ((lane >> 3) & 3);
      if ((lane & 4) == 0) { *(LAS f32x4*)(size_t)(M_STAT + ti * 16) = (f32x4){rs, lo2f(nm) * rs, sd, __uint_as_float(nm)}; *(LAS float*)(size_t)(M_RS + ti * 4) = rs; } }
    wait_lgkm0(); wait_vm<10>(); bar();
    STAMP(2)
    sfor<5, 10>([&](auto E) { constexpr int e = E; dma_wd(10 + e, e); });
    const unsigned tdl = (8 * h + q) * 128 + (((blk * 2 + (pp >> 1)) ^ ((q >> 1) << 2)) * 16) + (pp & 1) * 8;
    const unsigned td0[2] = {tdl + hb0, (tdl ^ 64) + hb0}, td1[2] = {tdl + hb1, (tdl ^ 64) + hb1};
    auto rd_wd = [&](int e, u32x4* d) { for (int cb = 0; cb < 2; ++cb) d[cb] = lds_tr2((e < 6 ? td0[cb] : td1[cb]) + hcell_c(e), 512); };
    u32x4 wq[12][2];
    wait_vm<10>();
    sfor<0, 5>([&](auto E) { constexpr int e = E; rd_wd(e, wq[e]); });
    wait_lgkm0();
    dma_wd(20, 0); dma_wd(21, 1); dma_wd(2, 2); dma_wd(3, 3); dma_wd(4, 4);
    wait_vm<10>();
    sfor<5, 10>([&](auto E) { constexpr int e = E; rd_wd(e, wq[e]); });
    wait_lgkm0();
    sfor<5, 10>([&](auto E) { constexpr int e = E; dma_wd(e, e); });
    wait_vm<16>();
    rd_wd(0, wq[10]); rd_wd(1, wq[11]);
    wait_lgkm0();
    dma_wd(0, 0); dma_wd(1, 1);
    ld_skip();
    STAMP(3)
    { const int ks = kb + lane, pg = ks / 6, pb = ks - 6 * pg; poll((pg * 8 + (pb < 2 ? 2 + pb : 3 + (pb >> 1))) * 128, cnt); }
    pin(xv[0]); pin(xv[1]); pin(dk);
    STAMP(4)
    const unsigned a2g = lnd(a2c) + (64 + kb) * 1024;
    u32x4 L[44];
    auto ldL = [&](auto M) { constexpr int m = M;
      const int mm = kstep(m);
      const unsigned so = mm < cnt ? a2g + mm * 1024 : OOB;
      ld128_sc1<0>(L[2 * m], sws, va, so); ld128_sc1<0>(L[2 * m + 1], sws, va, so + NKS * 1024);
    };
    sfor<0, WN / 2>(ldL);
    u32x4 bf[2][2];
    rd_wd(0, bf[0]);
    sfor<0, 22>([&](auto M) { constexpr int m = M;
      if constexpr (m + 1 < 10) rd_wd(m + 1, bf[(m + 1) & 1]);
      constexpr int iss = (2 * m + WN < 44) ? 2 * m + WN : 44;
      wait_vm<iss - 2 * m - 2>(); pin(L[2 * m]); pin(L[2 * m + 1]);
      for (int cb = 0; cb < 2; ++cb) for (int rb = 0; rb < 2; ++rb) acc2[cb][rb] = mfma(m < 10 ? bf[m & 1][cb] : wq[m < 10 ? 0 : m - 10][cb], L[2 * m + rb], acc2[cb][rb]);
      sb();
      if constexpr (m + WN / 2 < 22) ldL(std::integral_constant<int, m + WN / 2>{});
    });
    STAMP(9)
    for (int rb = 0; rb < 2; ++rb) { const int e = 4 * rb + (l5 >> 3); pa[rb] = (e < 6 ? hb0 + 2 * SLOT + e * 2048 : hb1 + (e - 6) * 2048) + (l5 & 7) * 256; }
  } else {
    u32x2 ep1; unsigned plr, pli, pdt;
    ld64_sc1(ep1, sws, 0, WS_E1 + g * 128);
    { const unsigned po = (g * 32 + (w & 1) * 16 + (lane & 15)) * 2;
      ld16(plr, mk_srd(lam_re, SP * 2), po); ld16(pli, mk_srd(lam_im, SP * 2), po); ld16(pdt, mk_srd(logdt, SP * 2), po); }
    { __builtin_amdgcn_s_sleep(TDELAY);
      setm0(M_TCH);
      const unsigned v43 = lane < 43 ? lane * 128 : OOB, v16 = lane < 16 ? lane * 128 : OOB;
      if (w == 0) { if (NTOUCH & 1) touch(sx, lane * 128, b * 8192); }
      else if (w == 3) { if (NTOUCH & 2) { touch(mk_srd(wd, SF * SD * 2), v43, b * 5504); touch(mk_srd(Cre, SD * SP * 2), v16, b * 2048); touch(mk_srd(Cim, SD * SP * 2), v16, b * 2048); } }
      else if (NTOUCH & 1) { touch(mk_srd(w == 1 ? wg : wu, SD * SF * 2), v43, b * 5504); touch(mk_srd(w == 1 ? Bre : Bim, SP * SD * 2), v16, b * 2048); } }
    unsigned ra[4];
    for (int s = 0; s < 4; ++s) ra[s] = l5 * 128 + (((2 * s + h) ^ ((l5 >> 1) & 7)) * 16);
    const unsigned selv = (((lane & 15) == 0 && blk == 0) || ((lane & 15) == 4 && blk == 1)) ? 0x3f803f80u : 0u;
    const u32x4 sel = {selv, selv, selv, selv};
    const unsigned wt = (blk ? OFF_WU : OFF_WG) + (8 * h + q) * 192 + (((pp & 1) << 1) | (pp >> 1)) * 8;
    wait_vm<0>(); pin(ep2); pin(ep1); pin(plr); pin(pli); pin(pdt);
    tag2 = (((u64)ep2[1] << 32) | ep2[0]) + 1;
    f32x16 acc[2][4] = {};
    f32x4 csa[2] = {};

    auto main_loop = [&](auto SSM_) { constexpr bool SSM = SSM_;
      unsigned wa[4];
      if constexpr (SSM) { for (int s = 0; s < 4; ++s) { const int row = w * 16 + (lane & 15); wa[s] = OFF_B + blk * 4096 + row * 128 + (((2 * s + h) ^ ((row >> 1) & 7)) * 16); } }
      const unsigned wtb = wt + (SSM ? w : 2 * w - 1) * 32;
      unsigned slot = 0;
      u32x4 af[2][4]; u32x2 wf[2][2][2];
      auto rd1 = [&](auto S, auto I, unsigned a_, unsigned w_, unsigned t_) { constexpr int s = S, i = I;
        if constexpr (i == 0) { if constexpr (SSM) { const u32x4 t = lds_r128(w_); wf[s & 1][0][0] = (u32x2){t[0], t[1]}; wf[s & 1][0][1] = (u32x2){t[2], t[3]}; } else wf[s & 1][0][0] = lds_tr(t_ + s * 3072); }
        if constexpr (i == 1) { if constexpr (!SSM) wf[s & 1][0][1] = lds_tr(t_ + s * 3072 + 768); }
        if constexpr (i == 2) af[s & 1][0] = lds_r128(a_);
        if constexpr (i == 3) wf[s & 1][1][0] = lds_tr(t_ + 32 + s * 3072);
        if constexpr (i == 4) wf[s & 1][1][1] = lds_tr(t_ + 32 + s * 3072 + 768);
        if constexpr (i >= 5) af[s & 1][i - 4] = lds_r128(a_ + (i - 4) * 4096);
      };
      auto W = [&](int p, int k) { return (u32x4){wf[p][k][0][0], wf[p][k][0][1], wf[p][k][1][0], wf[p][k][1][1]}; };
      bar();
      sfor<0, 8>([&](auto I) { rd1(std::integral_constant<int, 0>{}, I, ra[0], SSM ? wa[0] : 0u, wtb - 32); });
#pragma unroll 1
      for (int v = 0; v < 8; ++v) {
        const unsigned nslot = (slot == 2 * SLOT) ? 0 : slot + SLOT;
        sfor<0, 4>([&](auto S) { constexpr int s = S, sn = (s + 1) & 3;
          if constexpr (s == 3) { wait_lgkm0(); if (v < 7) bar(); }
          const unsigned sl = s == 3 ? nslot : slot;
          const unsigned a_ = sl + ra[sn], w_ = SSM ? sl + wa[sn] : 0u, t_ = sl + wtb - 32;
          sb();
          sfor<0, 8>([&](auto I) { constexpr int i = I, rb = i >> 1;
            if constexpr (i & 1) acc[1][rb] = mfma(W(s & 1, 1), af[s & 1][rb], acc[1][rb]);
            else acc[0][rb] = SSM ? mfma(af[s & 1][rb], W(s & 1, 0), acc[0][rb]) : mfma(W(s & 1, 0), af[s & 1][rb], acc[0][rb]);
            sb();
            rd1(std::integral_constant<int, sn>{}, I, a_, w_, t_);
            sb();
          });
          csa[0] = mfma16(sel, W(s & 1, 0), csa[0]); csa[1] = mfma16(sel, W(s & 1, 1), csa[1]);
          sb();
        });
        slot = nslot;
      }
      if (lane == 0) __hip_atomic_fetch_add((LAS unsigned*)(size_t)M_CNT, 1u, __ATOMIC_RELAXED, __HIP_MEMORY_SCOPE_WORKGROUP);
      STAMP(2)
    };
    f32x4 st[4];
    auto rank1 = [&](auto SSM_) { constexpr bool SSM = SSM_;
      const unsigned sta = M_STAT + (64 * ((l5 >> 2) & 1) + 4 * (l5 >> 3) + (l5 & 3)) * 16;
      for (int rb = 0; rb < 4; ++rb) st[rb] = *(LAS f32x4*)(size_t)(sta + (32 * (rb & 1) + 16 * (rb >> 1)) * 16);
      u32x4 cf[2];
      for (int k = 0; k < 2; ++k) { const float cs = h ? 0.f : csa[k][0]; const float c0 = rbf(cs), r1 = cs - c0, c1 = rbf(r1), c2 = r1 - c1;
        cf[k] = (u32x4){pk2(c0, c1), pk2(c2, 0.f), 0u, 0u}; }
      for (int rb = 0; rb < 4; ++rb) { const unsigned nm = __float_as_uint(st[rb][3]); const u32x4 mf = {nm, nm, 0u, 0u};
        acc[0][rb] = SSM ? mfma(mf, cf[0], acc[0][rb]) : mfma(cf[0], mf, acc[0][rb]);
        acc[1][rb] = mfma(cf[1], mf, acc[1][rb]); }
    };
    const unsigned a2p = WS_A2 + (unsigned)(c * 4) * (NKS * 1024) + va;
    auto glu_epi = [&](auto K, int ksp) { constexpr int k = K;
      const unsigned vst = ksp < 86 ? a2p + (64 + ksp) * 1024 : OOB;
      sfor<0, 4>([&](auto R) { constexpr int rb = R;
        const float rs = st[rb][0], k1 = -2.302208198f * rs, k2 = -0.1029432397f * rs * rs * rs, s2 = st[rb][2] * st[rb][2];
        unsigned o[4];
        for (int i = 0; i < 4; ++i) {
          const f32x2 gv = {acc[k][rb][2 * i], acc[k][rb][2 * i + 1]}, uv = {acc[k][rb][8 + 2 * i], acc[k][rb][9 + 2 * i]};
          const f32x2 z = gv * pfma(gv * gv, sp(k2), sp(k1));
          const f32x2 e = {__builtin_amdgcn_exp2f(z[0]), __builtin_amdgcn_exp2f(z[1])};
          const f32x2 d = pfma(e, sp(s2), sp(s2));
          const f32x2 r = (gv * uv) * (f32x2){__builtin_amdgcn_rcpf(d[0]), __builtin_amdgcn_rcpf(d[1])};
          o[i] = pk2(r[0], r[1]); }
        __builtin_amdgcn_raw_buffer_store_b128((u32x4){o[0], o[1], o[2], o[3]}, rws, vst + rb * NKS * 1024, 0, 16);
      });
    };
    auto set_flag = [&](int fi) {
      __builtin_amdgcn_raw_buffer_store_b64((u32x2){(unsigned)tag2, (unsigned)(tag2 >> 32)}, rws, lane == 0 ? flagb + (g * 8 + fi) * 128 : OOB, 0, 16);
    };
    unsigned vc[4];
    for (int r = 0; r < 4; ++r) vc[r] = ((lane >> 3) + 7 * r) * 1024 + (sw0 ^ ((r & 1) * 64));
    const unsigned cb0 = w * 4096, cb1 = w * 12288;
    auto dma_c = [&](int ga, int cb, int qg) {
      const int ag = ROT2 ? (ga + lnd(ct)) & 3 : ga;
      const i32x4 sc = mk_srd((ag >> 1) ? Cim : Cre, SD * SP * 2);
      const unsigned so = (ct * 64 + cb * 32) * 1024 + w * 256 + (ag & 1) * 128;
      setm0((qg == 0 ? cb0 : cb1) + cgrp_c(qg)); dmao<0>(sc, vc[0], so); dmao<1024>(sc, vc[1], so); dmao<2048>(sc, vc[2], so); dmao<3072>(sc, vc[3], so);
    };
    auto wait_cnt = [&]() {
#pragma unroll 1
      for (unsigned spins = 0; spins < SPIN_MAX; ++spins)
        if (__builtin_amdgcn_readfirstlane(__hip_atomic_load((LAS unsigned*)(size_t)M_CNT, __ATOMIC_RELAXED, __HIP_MEMORY_SCOPE_WORKGROUP)) == 4u) break;
    };
    auto c_round = [&](int ga0) { dma_c(ga0, 0, 0); dma_c(ga0, 1, 1); dma_c(ga0 + 1, 0, 2); dma_c(ga0 + 1, 1, 3); };
    auto phase2 = [&]() {
      const int sx_ = ROT2 ? (ct >> 2) * 2 : 0;
      unsigned rc0[4], rc1[4];
      for (int s = 0; s < 4; ++s) { const unsigned r = l5 * 128 + (((2 * (s ^ sx_) + h) ^ ((l5 >> 1) & 7)) * 16); rc0[s] = r + cb0; rc1[s] = r + cb1; }
      auto rd_c = [&](int qg, int s) { return lds_r128((qg == 0 ? rc0[s] : rc1[s]) + cgrp_c(qg)); };
      u32x4 cq[8][2];
      for (int m = 0; m < 8; ++m) for (int cb = 0; cb < 2; ++cb) cq[m][cb] = rd_c((m >> 2) * 2 + cb, m & 3);
      wait_lgkm0();
      c_round(0);
      ld_skip();
      STAMP(6)
      poll(((4 * w + (lane >> 1)) * 8 + (lane & 1)) * 128, 8);
      pin(xv[0]); pin(xv[1]); pin(dk);
      STAMP(7)
      const unsigned a2s = lnd(a2c) + w * 8192;
      u32x4 L[32];
      auto ldL = [&](auto M) { constexpr int m = M;
        const int ag = ROT2 ? ((m >> 2) + lnd(ct)) & 3 : (m >> 2);
        const unsigned so = a2s + ((ag >> 1) * 32 + (ag & 1) * 4 + ((m & 3) ^ sx_)) * 1024;
        ld128_sc1<0>(L[2 * m], sws, va, so); ld128_sc1<0>(L[2 * m + 1], sws, va, so + NKS * 1024);
      };
      sfor<0, WN / 2>(ldL);
      u32x4 bf[2][2];
      for (int cb = 0; cb < 2; ++cb) bf[0][cb] = rd_c(cb, 0);
      sfor<0, 16>([&](auto M) { constexpr int m = M;
        if constexpr (m + 1 < 8) for (int cb = 0; cb < 2; ++cb) bf[(m + 1) & 1][cb] = rd_c(((m + 1) >> 2) * 2 + cb, (m + 1) & 3);
        constexpr int iss = (2 * m + WN < 32) ? 2 * m + WN : 32;
        wait_vm<iss - 2 * m - 2>(); pin(L[2 * m]); pin(L[2 * m + 1]);
        for (int cb = 0; cb < 2; ++cb) for (int rb = 0; rb < 2; ++rb) acc2[cb][rb] = mfma(m < 8 ? bf[m & 1][cb] : cq[m < 8 ? 0 : m - 8][cb], L[2 * m + rb], acc2[cb][rb]);
        sb();
        if constexpr (m + WN / 2 < 16) ldL(std::integral_constant<int, m + WN / 2>{});
      });
      STAMP(9)
    };

    if (w < 2) {
      const float lr = lo2f(plr), li = lo2f(pli);
      const float dt = rbf(__expf(lo2f(pdt)));
      const float er = __expf(dt * lr);
      const float rev = dt * li * 0.15915494309189535f;
      const float fr = rev - __builtin_rintf(rev);
      const float ar = er * __builtin_amdgcn_cosf(fr), ai = er * __builtin_amdgcn_sinf(fr);
      float kr, ki;
      { const float nr = ar - 1.f, ni = ai, den = 2.f / (lr * lr + li * li); kr = (nr * lr + ni * li) * den; ki = (ni * lr - nr * li) * den; }
      float a16r = ar, a16i = ai, a32r, a32i;
      for (int i = 0; i < 4; ++i) cmul(a16r, a16i, a16r, a16i, a16r, a16i);
      cmul(a32r, a32i, a16r, a16i, a16r, a16i);
      main_loop(std::true_type{});
      rank1(std::true_type{});
      const int seg = 2 * h + blk;
      f32x2 vr[16], vi[16];
      sfor<0, 8>([&](auto I) { constexpr int i = I;
        const f32x4 rs4 = *(LAS f32x4*)(size_t)(M_RS + seg * 128 + i * 16);
        for (int e = 0; e < 2; ++e) { const int n = 2 * i + e;
          float a0 = acc[0][2 * (n >> 3)][2 * (n & 7)], b0 = acc[0][2 * (n >> 3) + 1][2 * (n & 7)];
          float a1 = acc[0][2 * (n >> 3)][2 * (n & 7) + 1], b1 = acc[0][2 * (n >> 3) + 1][2 * (n & 7) + 1];
          swap16(a0, b0); swap16(a1, b1);
          const f32x2 rs2 = {rs4[2 * e], rs4[2 * e + 1]};
          vr[n] = (f32x2){a0, a1} * rs2; vi[n] = (f32x2){b0, b1} * rs2; }
      });
      f32x2 e2r = {}, e2i = {};
      for (int n = 0; n < 16; ++n) cfma2(e2r, e2i, ar, ai, vr[n], vi[n]);
      float er_ = e2r[0], ei_ = e2i[0];
      cfma(er_, ei_, a16r, a16i, e2r[1], e2i[1]);
      float sr[4], si[4];
      { float a = er_, bq = er_; swap16(a, bq); sr[0] = a; sr[2] = a; swap32(sr[0], sr[2]); sr[1] = bq; sr[3] = bq; swap32(sr[1], sr[3]);
        a = ei_; bq = ei_; swap16(a, bq); si[0] = a; si[2] = a; swap32(si[0], si[2]); si[1] = bq; si[3] = bq; swap32(si[1], si[3]); }
      float prer = 0.f, prei = 0.f, pwr = 1.f, pwi = 0.f, rr = 0.f, ri = 0.f, qr = 1.f, qi = 0.f;
      for (int s = 0; s < 4; ++s) {
        if (s == seg) { prer = rr; prei = ri; pwr = qr; pwi = qi; }
        cfma(rr, ri, a32r, a32i, sr[s], si[s]); cmul(qr, qi, qr, qi, a32r, a32i);
      }
      const u64 tag1 = (((u64)ep1[1] << 32) | ep1[0]) + 1;
      const unsigned t1l = (unsigned)tag1, t1h = (unsigned)(tag1 >> 32);
      const unsigned granb = WS_GRAN + (g * 2 + w) * 4096;
      __builtin_amdgcn_raw_buffer_store_b128((u32x4){__float_as_uint(rr), t1l, __float_as_uint(ri), t1h}, rws, lane < 16 ? granb + c * 256 + lane * 16 : OOB, 0, 16);
      sb();
      STAMP(3)
      glu_epi(std::integral_constant<int, 1>{}, 6 * g + w);
      for (int n = 0; n < 16; ++n) { const f32x2 t = pfma(sp(kr), vr[n], sp(-ki) * vi[n]); vi[n] = pfma(sp(-kr), vi[n], sp(-ki) * vr[n]); vr[n] = t; }
      float a256r, a256i, a512r, a512i, mr = 1.f, mi = 0.f;
      cmul(a256r, a256i, qr, qi, qr, qi); cmul(a512r, a512i, a256r, a256i, a256r, a256i);
      { const int ex = (c - 1 - seg) & 3; if (ex & 1) { mr = qr; mi = qi; } if (ex & 2) cmul(mr, mi, mr, mi, a256r, a256i); }
      sb();
      STAMP(10)
      u32x4 ge[4];
#pragma unroll 1
      for (unsigned spins = 0; spins < SPIN_MAX; ++spins) {
        sfor<0, 4>([&](auto I) { constexpr int i = I; ld128_sc1<i * 1024>(ge[i], sws, va, granb); });
        wait_vm<0>(); pin(ge[0]); pin(ge[1]); pin(ge[2]); pin(ge[3]);
        if (spins == 0) set_flag(2 + w);
        bool ok = true;
        for (int i = 0; i < 4; ++i) ok &= (4 * i + seg >= c) || (ge[i][1] == t1l && ge[i][3] == t1h);
        if (__all(ok)) break;
        __builtin_amdgcn_s_sleep(SLP);
      }
      STAMP(4)
      float cr = 0.f, ci = 0.f;
      for (int i = 0; i < 4; ++i) { float tr = cr, ti = ci; cfma(tr, ti, a512r, a512i, __uint_as_float(ge[i][0]), __uint_as_float(ge[i][2])); if (4 * i + seg < c) { cr = tr; ci = ti; } }
      cmul(cr, ci, cr, ci, mr, mi);
      cr = sum32(sum16(cr)); ci = sum32(sum16(ci));
      float g0r = prer + pwr * cr - pwi * ci, g0i = prei + pwr * ci + pwi * cr;
      float g1r = g0r, g1i = g0i;
      cfma(g1r, g1i, a16r, a16i, e2r[0], e2i[0]);
      f32x2 gr = {g0r, g1r}, gi = {g0i, g1i};
      { const f32x2 t = pfma(sp(kr), gr, sp(-ki) * gi); gi = pfma(sp(-kr), gi, sp(-ki) * gr); gr = t; }
      unsigned hp[2][16];
      for (int n = 0; n < 16; ++n) { cfma2(gr, gi, ar, -ai, vr[n], vi[n]); hp[0][n] = pk2(gr[0], gr[1]); hp[1][n] = pk2(gi[0], gi[1]); }
      const unsigned tw = M_T + w * 4608 + (lane & 15) * 288 + seg * 64;
      const unsigned trd = M_T + w * 4608 + (8 * h + q) * 288 + (64 * (pp & 1) + 8 * blk + 4 * (pp >> 1)) * 2;
      sfor<0, 2>([&](auto P) { constexpr int part = P;
        for (int i = 0; i < 4; ++i) *(LAS u32x4*)(size_t)(tw + i * 16) = (u32x4){hp[part][4 * i], hp[part][4 * i + 1], hp[part][4 * i + 2], hp[part][4 * i + 3]};
        wait_lgkm0();
        sfor<0, 4>([&](auto R) { constexpr int rb = R;
          u32x4 fr4 = lds_tr2(trd + (32 * (rb & 1) + 16 * (rb >> 1)) * 2, 4 * 288);
          __builtin_amdgcn_raw_buffer_store_b128(fr4, rws, a2p + (rb * NKS + part * 32 + 2 * g + w) * 1024, 0, 16); });
        wait_lgkm0();
      });
      STAMP(5)
      wait_vm<0>();
      set_flag(w);
      if (c == 15) __builtin_amdgcn_raw_buffer_store_b64((u32x2){t1l, t1h}, rws, (lane == 0 && w == 0) ? WS_E1 + g * 128 : OOB, 0, 16);
      STAMP(8)
      wait_cnt();
      c_round(2);
      wait_vm<0>();
      phase2();
    } else {
      main_loop(std::false_type{});
      rank1(std::false_type{});
      glu_epi(std::integral_constant<int, 0>{}, 6 * g + 2 * w - 2);
      glu_epi(std::integral_constant<int, 1>{}, 6 * g + 2 * w - 1);
      sb();
      STAMP(3)
      wait_cnt();
      c_round(2);
      wait_vm<16>();
      set_flag(2 + w);
      STAMP(8)
      wait_vm<1>();
      phase2();
    }
    for (int rb = 0; rb < 2; ++rb) { const int qg = 2 * rb + (l5 >> 4); pa[rb] = (qg == 0 ? cb0 + 32768 : cb1 + SLOT + (qg - 1) * 4096) + (l5 & 15) * 256; }
  }
  for (int rb = 0; rb < 2; ++rb)
    for (int cb = 0; cb < 2; ++cb) for (int rq = 0; rq < 4; ++rq)
      *(LAS f32x4*)(size_t)(pa[rb] + (((cb * 8 + rq * 2 + h) ^ (l5 & 7)) * 16)) =
          (f32x4){acc2[cb][rb][4 * rq], acc2[cb][rb][4 * rq + 1], acc2[cb][rb][4 * rq + 2], acc2[cb][rb][4 * rq + 3]};
  wait_lgkm0(); bar();
  STAMP(11)
  if (g == 0) __builtin_amdgcn_raw_buffer_store_b64((u32x2){(unsigned)tag2, (unsigned)(tag2 >> 32)}, rws, tid == 0 ? WS_E2 + c * 128 : OOB, 0, 16);
  for (int it = 0; it < 2; ++it) {
    const int r5 = tid >> 4, ch = tid & 15;
    const unsigned sw_ = (ch ^ (r5 & 7)) * 16;
    const int e = 4 * it + (r5 >> 3), qg = 2 * it + (r5 >> 4);
    const unsigned ph = (e < 6 ? 2 * SLOT + e * 2048 : (e - 6) * 2048) + (r5 & 7) * 256 + sw_, sh = e < 6 ? 12288 : 8192;
    const unsigned pc = (qg == 0 ? 32768 : SLOT + (qg - 1) * 4096) + (r5 & 15) * 256 + sw_, sc = qg == 0 ? 4096 : 12288;
    f32x4 s = *(LAS f32x4*)(size_t)(ph) + *(LAS f32x4*)(size_t)(pc);
    for (int ww = 1; ww < 4; ++ww) s += *(LAS f32x4*)(size_t)(ph + ww * sh) + *(LAS f32x4*)(size_t)(pc + ww * sc);
    const f32x2 st2 = *(LAS f32x2*)(size_t)(M_STAT + (irow + 32 * it) * 16);
    f32x4 o;
    for (int e2 = 0; e2 < 4; ++e2) {
      const unsigned xu = xv[it][e2 >> 1], du = dk[e2 >> 1];
      const float xe = (e2 & 1) ? hi2f(xu) : lo2f(xu), de = (e2 & 1) ? hi2f(du) : lo2f(du);
      o[e2] = xe + (s[e2] + rbf(rbf(__builtin_fmaf(xe, st2[0], st2[1])) * de));
    }
    *(f32x4*)(out + (c * 128 + trow + 32 * it) * SD + ct * 64 + ch * 4) = o;
  }
  STAMP(12)
  STAMP_OUT
}

extern "C" void kernel_launch(void* const* d_in, const int* in_sizes, int n_in, void* d_out, int out_size, void* d_ws, size_t ws_size,
                              hipStream_t stream) {
  (void)hipFuncSetAttribute((const void*)fused, hipFuncAttributeMaxDynamicSharedMemorySize, LDS_BYTES);
  fused<<<256, 512, LDS_BYTES, stream>>>((const u16*)d_in[0], (const u16*)d_in[1], (const u16*)d_in[2], (const u16*)d_in[3], (const u16*)d_in[4],
                                         (const u16*)d_in[5], (const u16*)d_in[6], (const u16*)d_in[7], (const u16*)d_in[8], (const u16*)d_in[10],
                                         (const u16*)d_in[11], (const u16*)d_in[12], (char*)d_ws, (float*)d_out);
}
```

```cpp
#include <hip/hip_runtime.h>
#include <hip/hip_bf16.h>
#include <type_traits>

typedef unsigned short u16;
typedef unsigned long long u64;
typedef short s16x4 __attribute__((ext_vector_type(4)));
typedef __bf16 bf16x2 __attribute__((ext_vector_type(2)));
typedef __bf16 bf16x8 __attribute__((ext_vector_type(8)));
typedef float f32x2 __attribute__((ext_vector_type(2)));
typedef float f32x4 __attribute__((ext_vector_type(4)));
typedef float f32x16 __attribute__((ext_vector_type(16)));
typedef unsigned u32x2 __attribute__((ext_vector_type(2)));
typedef unsigned u32x4 __attribute__((ext_vector_type(4)));
typedef int i32x4 __attribute__((ext_vector_type(4)));
#define LAS __attribute__((address_space(3)))
#define DEVI __device__ __forceinline__

constexpr int SL = 2048, SD = 512, SP = 512, SF = 1376;
constexpr int NKS = 150;
constexpr unsigned WS_E1 = 0;
constexpr unsigned WS_E2 = 2048;
constexpr unsigned WS_GRAN = 262144;
constexpr unsigned WS_FLAG = 524288;
constexpr unsigned WS_A2 = 786432;
constexpr unsigned A2_BYTES = 64u * NKS * 1024u;
constexpr unsigned WS_BYTES = WS_A2 + A2_BYTES;
constexpr unsigned OOB = 0x40000000u;
constexpr int SLT = 24832;
constexpr int OFF_B = 16384, OFF_WU = 12288 + 32;
constexpr int MISC = 6 * SLT;
constexpr int M_ST = MISC, M_SD = MISC + 1024, M_CNT = MISC + 2048, M_CS = MISC + 2304, M_T = MISC + 3840;
constexpr int LDS_BYTES = 163840;
constexpr unsigned SPIN_MAX = 1u << 16;
constexpr int cell_k(int e) { return e >= 15 ? 2 : (e % 5) < 3 ? 0 : 1; }
constexpr int cell_c(int e) { return e >= 15 ? ((e - 13) % 3) * SLT + OFF_B : (e % 5) < 3 ? (3 + (e / 5 + 2) % 3) * SLT + (e % 5) * 2048 : ((e / 5 + 2) % 3) * SLT + (e % 5 - 3) * 2048; }

#define STAMP_DECL
#define STAMP(i) {}
#define STAMP_OUT
#define STB(i) {}
template <int B, int E, class Fn> DEVI void sfor(Fn&& f) {
  if constexpr (B < E) { f(std::integral_constant<int, B>{}); sfor<B + 1, E>(f); }
}
DEVI i32x4 mk_srd(const void* p, unsigned bytes) {
  u64 a = (u64)p; return (i32x4){(int)(unsigned)a, (int)((unsigned)(a >> 32) & 0xffffu), (int)bytes, 0x00020000};
}
DEVI void dma(i32x4 srd, unsigned voff, unsigned soff, unsigned lds) {
  asm volatile("s_mov_b32 m0, %0\n\ts_nop 0\n\tbuffer_load_dwordx4 %1, %2, %3 offen lds" ::"s"(lds), "v"(voff), "s"(srd), "s"(soff) : "memory");
}
template <int OFF> DEVI void ld128_sc1(u32x4& d, i32x4 srd, unsigned voff, unsigned soff) {
  asm volatile("buffer_load_dwordx4 %0, %1, %2, %3 offen offset:%4 sc1" : "=v"(d) : "v"(voff), "s"(srd), "s"(soff), "n"(OFF) : "memory");
}
template <int OFF> DEVI void ld128_sc1_rw(u32x4& d, i32x4 srd, unsigned voff, unsigned soff) {
  asm volatile("buffer_load_dwordx4 %0, %1, %2, %3 offen offset:%4 sc1" : "+v"(d) : "v"(voff), "s"(srd), "s"(soff), "n"(OFF) : "memory");
}
DEVI void ld64_sc1(u32x2& d, i32x4 srd, unsigned voff, unsigned soff) {
  asm volatile("buffer_load_dwordx2 %0, %1, %2, %3 offen sc1" : "=v"(d) : "v"(voff), "s"(srd), "s"(soff) : "memory");
}
DEVI void ld64(u32x2& d, i32x4 srd, unsigned voff, unsigned soff) {
  asm volatile("buffer_load_dwordx2 %0, %1, %2, %3 offen" : "=v"(d) : "v"(voff), "s"(srd), "s"(soff) : "memory");
}
DEVI void ld16(unsigned& d, i32x4 srd, unsigned voff) {
  asm volatile("buffer_load_ushort %0, %1, %2, 0 offen" : "=v"(d) : "v"(voff), "s"(srd) : "memory");
}
template <int N> DEVI void wait_vm() { asm volatile("s_waitcnt vmcnt(%0)" ::"n"(N) : "memory"); }
template <class T> DEVI void pin(T& a) { asm volatile("" : "+v"(a)::"memory"); }
template <class T> DEVI void use(const T& a) { asm volatile("" ::"v"(a) : "memory"); }
DEVI int lnd(int a) { asm volatile("" : "+s"(a)); return a; }
DEVI int ftid() { int t = threadIdx.x; asm volatile("" : "+v"(t)); return t; }
DEVI void wait_lgkm0() { asm volatile("s_waitcnt lgkmcnt(0)" ::: "memory"); }
DEVI void bar() { asm volatile("s_barrier" ::: "memory"); }
DEVI void sb() { __builtin_amdgcn_sched_barrier(0); }
DEVI float lo2f(unsigned u) { return __uint_as_float(u << 16); }
DEVI float hi2f(unsigned u) { return __uint_as_float(u & 0xffff0000u); }
DEVI unsigned pk2(float a, float b) { f32x2 v = {a, b}; return __builtin_bit_cast(unsigned, __builtin_convertvector(v, bf16x2)); }
DEVI float rbf(float a) { return lo2f(pk2(a, a)); }
DEVI u32x4 lds_r128(unsigned a) { return *(LAS u32x4*)(size_t)a; }
DEVI u32x2 lds_tr(unsigned a) { return __builtin_bit_cast(u32x2, __builtin_amdgcn_ds_read_tr16_b64_v4i16((LAS s16x4*)(size_t)a)); }
DEVI u32x4 lds_tr2(unsigned a, unsigned d) { u32x2 x = lds_tr(a), y = lds_tr(a + d); return (u32x4){x[0], x[1], y[0], y[1]}; }
DEVI f32x16 mfma(u32x4 a, u32x4 b, f32x16 c) {
  return __builtin_amdgcn_mfma_f32_32x32x16_bf16(__builtin_bit_cast(bf16x8, a), __builtin_bit_cast(bf16x8, b), c, 0, 0, 0);
}
DEVI float dot2(unsigned a, unsigned b, float c) {
  return __builtin_amdgcn_fdot2_f32_bf16(__builtin_bit_cast(bf16x2, a), __builtin_bit_cast(bf16x2, b), c, false);
}
DEVI void swap16(float& a, float& b) { auto r = __builtin_amdgcn_permlane16_swap(__float_as_uint(a), __float_as_uint(b), false, false); a = __uint_as_float(r[0]); b = __uint_as_float(r[1]); }
DEVI void swap32(float& a, float& b) { auto r = __builtin_amdgcn_permlane32_swap(__float_as_uint(a), __float_as_uint(b), false, false); a = __uint_as_float(r[0]); b = __uint_as_float(r[1]); }
DEVI float sum32(float v) { float a = v, b = v; swap32(a, b); return a + b; }
DEVI float sum16(float v) { float a = v, b = v; swap16(a, b); return a + b; }
DEVI void cmul(float& dr, float& di, float ar, float ai, float br, float bi) { float t = ar * br - ai * bi; di = ar * bi + ai * br; dr = t; }
DEVI void cfma(float& hr, float& hi, float ar, float ai, float br, float bi) {
  float t = __builtin_fmaf(ar, hr, __builtin_fmaf(-ai, hi, br)); hi = __builtin_fmaf(ar, hi, __builtin_fmaf(ai, hr, bi)); hr = t;
}

template <int OFF> DEVI void dmao(i32x4 srd, unsigned voff, unsigned soff) {
  asm volatile("buffer_load_dwordx4 %0, %1, %2 offen offset:%3 lds" ::"v"(voff), "s"(srd), "s"(soff), "n"(OFF) : "memory");
}
DEVI void setm0(unsigned lds) { asm volatile("s_mov_b32 m0, %0\n\ts_nop 0" ::"s"(lds) : "memory"); }
DEVI void touch(i32x4 srd, unsigned voff, unsigned soff) {
  asm volatile("buffer_load_dword %0, %1, %2 offen lds" ::"v"(voff), "s"(srd), "s"(soff) : "memory");
}
DEVI f32x4 mfma16(u32x4 a, u32x4 b, f32x4 c) {
  return __builtin_amdgcn_mfma_f32_16x16x32_bf16(__builtin_bit_cast(bf16x8, a), __builtin_bit_cast(bf16x8, b), c, 0, 0, 0);
}
DEVI float vadd(float a, float b) { float r; asm("v_add_f32 %0, %1, %2" : "=v"(r) : "v"(a), "v"(b)); return r; }
DEVI float vfma(float a, float b, float c) { float r; asm("v_fma_f32 %0, %1, %2, %3" : "=v"(r) : "v"(a), "v"(b), "v"(c)); return r; }
DEVI f32x2 pfma(f32x2 a, f32x2 b, f32x2 c) { return __builtin_elementwise_fma(a, b, c); }
DEVI f32x2 sp(float a) { return (f32x2){a, a}; }
DEVI void cfma2(f32x2& hr, f32x2& hi, f32x2 a, f32x2 br, f32x2 bi) {
  f32x2 t1, t2;
  asm("v_pk_fma_f32 %2, %4, %1, %5 op_sel:[1,0,0] op_sel_hi:[1,1,1] neg_lo:[1,0,0] neg_hi:[1,0,0]\n\t"
      "v_pk_fma_f32 %3, %4, %0, %6 op_sel:[1,0,0] op_sel_hi:[1,1,1]\n\t"
      "v_pk_fma_f32 %0, %4, %0, %2 op_sel:[0,0,0] op_sel_hi:[0,1,1]\n\t"
      "v_pk_fma_f32 %1, %4, %1, %3 op_sel:[0,0,0] op_sel_hi:[0,1,1]"
      : "+v"(hr), "+v"(hi), "=&v"(t1), "=&v"(t2) : "v"(a), "v"(br), "v"(bi));
}
DEVI void cout2(unsigned& o0, unsigned& o1, f32x2 k, f32x2 gr, f32x2 gi) {
  f32x2 t1, t2;
  asm("v_pk_mul_f32 %0, %2, %4 op_sel:[1,0] op_sel_hi:[1,1]\n\t"
      "v_pk_mul_f32 %1, %2, %3 op_sel:[1,0] op_sel_hi:[1,1]\n\t"
      "v_pk_fma_f32 %0, %2, %3, %0 op_sel:[0,0,0] op_sel_hi:[0,1,1] neg_lo:[0,0,1] neg_hi:[0,0,1]\n\t"
      "v_pk_fma_f32 %1, %2, %4, %1 op_sel:[0,0,0] op_sel_hi:[0,1,1] neg_lo:[1,0,1] neg_hi:[1,0,1]"
      : "=&v"(t1), "=&v"(t2) : "v"(k), "v"(gr), "v"(gi));
  o0 = pk2(t1[0], t1[1]); o1 = pk2(t2[0], t2[1]);
}
#define SLP 4
#define WN 8
#define PGV 50
#define PCV 64
#define P2D 8
#define PTV 86
#define PSV 89
#define PFV 108
#define LEAD 1
#define IC(x) std::integral_constant<int, (x)>{}
constexpr int p2_extra(int m) { return m == 3 ? 4 : m == 5 ? 4 : m == 7 ? 8 : m == 11 ? 5 : m == 12 ? 2 : 0; }
constexpr int p2_l0(int m) { return m <= 13 ? (m + WN / 2 < 16 ? m + WN / 2 : 16) : (m + 6 < 38 ? m + 6 : 38); }
constexpr int p2_after(int m) {
  int pos = 0, posL[40] = {};
  for (int k = 0; k < p2_l0(0); ++k) { pos += 2; posL[k] = pos; }
  for (int s = 0; s < m; ++s) { pos += p2_extra(s); for (int k = p2_l0(s); k < p2_l0(s + 1); ++k) { pos += 2; posL[k] = pos; } }
  return pos - posL[m];
}
constexpr int PPUB = 25, PG = PGV, PC = PCV, P2 = PC + P2D, PT = PTV, PS = PSV, PF = PFV, PPOLL = 16 * 7 + 7;
constexpr int hb_ops(int n) { return n < 6 ? 10 : 14; }
constexpr int hb_g0(int n) { return n < 6 ? 3 : 4; }
constexpr int hb_extra(int P) { return P == PPUB ? 1 : P == PG ? 8 : P == PS ? 4 : P == PF ? 1 : P == PPOLL ? 1 : 0; }
constexpr int hb_cnt(int P, bool with_extra) {
  int t = 0;
  for (int n = 0; n < 8; ++n) for (int i = 0; i < hb_ops(n); ++i) { const int p = 16 * n + i; if (p > P) return t; t += 1; if (p < P || with_extra) t += hb_extra(p); }
  return t;
}
constexpr int nxt(int P, int d) { for (; d > 0; --d) { ++P; if (P % 16 >= hb_ops(P / 16)) P = (P / 16 + 1) * 16; } return P; }
DEVI u64 utag(u32x2 e) { const u64 t = (((u64)(unsigned)__builtin_amdgcn_readfirstlane(e[1]) << 32) | (unsigned)__builtin_amdgcn_readfirstlane(e[0])) + 1; return t ? t : 1; }
template <int CTRL> DEVI float dppf(float v) { return __int_as_float(__builtin_amdgcn_update_dpp(0, __float_as_int(v), CTRL, 0xf, 0xf, true)); }
DEVI void sline(u64 base, unsigned off) {
  asm volatile("s_load_dwordx2 s[98:99], %0, %1" ::"s"(base), "s"(off + 60) : "memory", "s98", "s99");
}
template <int IMM> DEVI void setm0i(unsigned base) { asm volatile("s_add_i32 m0, %0, %1\n\ts_nop 0" ::"s"(base), "n"(IMM) : "memory", "scc"); }
DEVI void cf4(float& hr, float& hi, float ar, float ai, float br, float bi) {
  float t, u;
  asm("v_fma_f32 %0, -%5, %3, %6\n\tv_fma_f32 %1, %5, %2, %7\n\tv_fmac_f32 %0, %4, %2\n\tv_fmac_f32 %1, %4, %3"
      : "=&v"(t), "=&v"(u) : "v"(hr), "v"(hi), "v"(ar), "v"(ai), "v"(br), "v"(bi));
  hr = t; hi = u;
}
DEVI void cf4c(float hr, float hi, float ar, float ai, float nai, float& br, float& bi) {
  asm("v_fmac_f32 %0, %5, %3\n\tv_fmac_f32 %1, %6, %2\n\tv_fmac_f32 %0, %4, %2\n\tv_fmac_f32 %1, %4, %3"
      : "+v"(br), "+v"(bi) : "v"(hr), "v"(hi), "v"(ar), "v"(ai), "v"(nai));
}
DEVI void sc4(float& r, float& i, float rs, float rc, float csr, float csi) {
  asm("v_mul_f32 %0, %2, %0\n\tv_mul_f32 %1, %2, %1\n\tv_fmac_f32 %0, %3, %4\n\tv_fmac_f32 %1, %3, %5"
      : "+v"(r), "+v"(i) : "v"(rs), "v"(rc), "v"(csr), "v"(csi));
}
DEVI void kc4(float& r, float& i, float kr, float nkr, float nki) {
  float t, u;
  asm("v_mul_f32 %0, %4, %2\n\tv_mul_f32 %1, %5, %3\n\tv_fmac_f32 %0, %6, %3\n\tv_fmac_f32 %1, %6, %2"
      : "=&v"(t), "=&v"(u) : "v"(r), "v"(i), "v"(kr), "v"(nkr), "v"(nki));
  r = t; i = u;
}

__global__ __launch_bounds__(512, 2) void fused(const u16* __restrict__ x, const u16* __restrict__ lam_re, const u16* __restrict__ lam_im,
                                                const u16* __restrict__ Bre, const u16* __restrict__ Bim, const u16* __restrict__ Cre,
                                                const u16* __restrict__ Cim, const u16* __restrict__ logdt, const u16* __restrict__ dskip,
                                                const u16* __restrict__ wg, const u16* __restrict__ wu, const u16* __restrict__ wd,
                                                char* __restrict__ ws, float* __restrict__ out) {
  extern __shared__ __attribute__((aligned(16))) char shm[];
  asm volatile(".Lpf_beg:");
  STAMP_DECL STAMP(0)
  const int j = __builtin_amdgcn_readfirstlane(threadIdx.x >> 6), w = j & 3, sb_ = w & 1, pi = w >> 1;
  const int b = blockIdx.x, xcd = b & 7, idx = b >> 3;
  const int c = (xcd >> 1) * 4 + (idx >> 3), rsel = xcd & 1, ct = idx & 7, g = rsel * 8 + ct;
  const i32x4 sx = mk_srd(x, SL * SD * 2);
  const i32x4 sws = mk_srd(ws, WS_BYTES);
  const __amdgpu_buffer_rsrc_t rws = __builtin_amdgcn_make_buffer_rsrc(ws, 0, WS_BYTES, 0x00020000);
  const unsigned flagb = WS_FLAG + c * 16384;
  const unsigned a2pb = WS_A2 + (unsigned)(c * 4 + 2 * pi) * (NKS * 1024);
  const unsigned w4 = w * 4096, w2 = w * 2048, w6 = w * 6144;
  const unsigned so_x = (c * 128 + 64 * (w >> 1) + 16 * (w & 1)) * 1024;
  const int rot = (2 * (xcd >> 1) + 4 * (xcd & 1)) & 7, lead = (c + g) & 3;
  const unsigned perm = lnd((((LEAD ? (lead == 0 ? 0x76453201u : lead == 1 ? 0x75643120u : lead == 2 ? 0x67542310u : 0x76534210u) : 0x76543210u) + rot * 0x11111111u) & 0x77777777u));
  auto chunk = [&](int v) { return (perm >> (4 * v)) & 7; };
  u64 tag2 = 0;
  auto set_flag = [&](int fi) {
    __builtin_amdgcn_raw_buffer_store_b64((u32x2){(unsigned)tag2, (unsigned)(tag2 >> 32)}, rws, (ftid() & 63) == 0 ? flagb + (g * 8 + fi) * 128 : OOB, 0, 16);
  };
  auto wait_cnt = [&]() {
#pragma unroll 1
    for (unsigned spins = 0; spins < SPIN_MAX; ++spins)
      if (__builtin_amdgcn_readfirstlane(__hip_atomic_load((LAS unsigned*)(size_t)M_CNT, __ATOMIC_RELAXED, __HIP_MEMORY_SCOPE_WORKGROUP)) == 4u) break;
    asm volatile("" ::: "memory");
  };

  if (j < 4) {
    const int lane = ftid() & 63, l5 = lane & 31, h = lane >> 5, blk = (lane >> 4) & 1, q = (lane & 15) >> 2, pp = lane & 3;
    unsigned vx[4], ra[4], wt[2];
    {
      const unsigned sw0 = ((lane & 7) ^ (lane >> 4)) * 16;
      const i32x4 sbm = mk_srd((w >> 1) ? Bim : Bre, SP * SD * 2);
      unsigned vb[2];
      for (int r = 0; r < 4; ++r) vx[r] = (32 * (lane >> 5) + 8 * ((lane >> 3) & 1) + ((lane >> 4) & 1) + r) * 1024 + (sw0 ^ ((r & 1) * 64));
      for (int r = 0; r < 2; ++r) vb[r] = ((lane >> 3) + 7 * r) * 1024 + (sw0 ^ (r * 64));
      const unsigned so_b = (g * 32 + (w & 1) * 16) * 1024;
      auto opA = [&](auto O) { constexpr int o = O, i = o % 6, s = o / 6, sl = ((s + 1) % 6) * SLT;
        const unsigned kc = chunk(s) * 128;
        if constexpr (i == 0 || i == 3) setm0i<sl>(w4);
        if constexpr (i == 4) setm0i<sl + OFF_B>(w2);
        if constexpr (i < 4) dmao<i * 1024>(sx, vx[i], so_x + kc); else dmao<(i - 4) * 1024>(sbm, vb[i - 4], so_b + kc);
      };
      sfor<0, 6>(opA);
      u32x2 ep2;
      ld64_sc1(ep2, sws, 0, WS_E2 + c * 128);
      sfor<6, 15>(opA);
      STAMP(1)
      if (w == 0 && lane == 0) *(LAS unsigned*)(size_t)M_CNT = 0;
      for (int s = 0; s < 4; ++s) { ra[s] = pi * 8192 + l5 * 128 + (((2 * (s ^ pi) + h) ^ ((l5 >> 1) & 7)) * 16); pin(ra[s]); }
      for (int p = 0; p < 2; ++p) { wt[p] = 3 * SLT + (blk ? OFF_WU : 0) + (8 * h + q) * 192 + (((pp & 1) << 1) | (pp >> 1)) * 8 + sb_ * 96 + (p ? -pi * 3072 : pi * 3072); pin(wt[p]); }
      const unsigned xa = w * 4096 + lane * 16;
      float s1[4] = {}, s2[4] = {};
      sfor<0, 8>([&](auto S) { constexpr int s = S;
        wait_vm<(s < 6 ? 9 : s == 6 ? 6 : 0)>(); if constexpr (s == 0) wait_lgkm0(); bar();
        if constexpr (s == 0) STAMP(20) if constexpr (s == 7) STAMP(21)
        u32x4 xr[4];
        for (int k = 0; k < 4; ++k) xr[k] = lds_r128(((s + 1) % 6) * SLT + k * 1024 + xa);
        sb();
        sfor<15 + 6 * s, (21 + 6 * s < 48 ? 21 + 6 * s : 48)>(opA);
        sb();
        for (int e = 0; e < 4; ++e) for (int i = 0; i < 4; ++i) { const unsigned u = xr[i][e]; s1[i] = dot2(u, 0x3f803f80u, s1[i]); s2[i] = dot2(u, u, s2[i]); }
        sb();
      });
      pin(ep2); tag2 = utag(ep2);
      float t1[2], t2[2];
      for (int k = 0; k < 2; ++k) { const bool o = lane & 1;
        t1[k] = (o ? s1[2 * k + 1] : s1[2 * k]) + dppf<0xB1>(o ? s1[2 * k] : s1[2 * k + 1]); t2[k] = (o ? s2[2 * k + 1] : s2[2 * k]) + dppf<0xB1>(o ? s2[2 * k] : s2[2 * k + 1]); }
      const bool o = lane & 2;
      float sm = (o ? t1[1] : t1[0]) + dppf<0x4E>(o ? t1[0] : t1[1]), sq = (o ? t2[1] : t2[0]) + dppf<0x4E>(o ? t2[0] : t2[1]);
      sm += dppf<0x104>(sm); sq += dppf<0x104>(sq);
      const float mean = sm * (1.f / SD);
      const float var = fmaxf(sq * (1.f / SD) - mean * mean, 0.f);
      const float sd = rbf(__builtin_amdgcn_sqrtf(rbf(rbf(var) + 1e-5f)));
      const float r_ = __builtin_amdgcn_rcpf(sd); const unsigned nm = pk2(-mean, -mean);
      const int ti = 64 * (w >> 1) + 32 * (lane >> 5) + 16 * (w & 1) + 4 * (lane & 3) + ((lane >> 3) & 3);
      if ((lane & 4) == 0) { *(LAS f32x2*)(size_t)(M_ST + ti * 8) = (f32x2){r_, lo2f(nm) * r_}; *(LAS f32x2*)(size_t)(M_SD + ti * 8) = (f32x2){sd, __uint_as_float(nm)}; }
    }
    const unsigned selv = (((lane & 15) == 0 && blk == 0) || ((lane & 15) == 4 && blk == 1)) ? 0x3f803f80u : 0u;
    const u32x4 sel = {selv, selv, selv, selv};
    f32x16 acc[3][2];
    f32x4 csg[3] = {};
    u32x4 af[2][2]; u32x2 wf[2][3][2];
    auto rd1 = [&](auto N, auto S, auto I) { constexpr int n = N, s = S, i = I;
      const unsigned a_ = ra[s] + (n % 3) * SLT, t_ = wt[s & 1] + (n % 3) * SLT + s * 3072;
      if constexpr (i == 0) { wf[s & 1][0][0] = lds_tr(t_); wf[s & 1][0][1] = lds_tr(t_ + 768); }
      if constexpr (i == 1) { af[s & 1][0] = lds_r128(a_); af[s & 1][1] = lds_r128(a_ + 4096); }
      if constexpr (i == 2) { wf[s & 1][1][0] = lds_tr(t_ + 32); wf[s & 1][1][1] = lds_tr(t_ + 32 + 768); }
      if constexpr (i == 3) { wf[s & 1][2][0] = lds_tr(t_ + 64); wf[s & 1][2][1] = lds_tr(t_ + 64 + 768); }
    };
    auto W = [&](int p, int k) { return (u32x4){wf[p][k][0][0], wf[p][k][0][1], wf[p][k][1][0], wf[p][k][1][1]}; };
    STAMP(22)
    wait_lgkm0(); bar();
    STAMP(2)
    sfor<0, 4>([&](auto I) { rd1(IC(0), IC(0), I); });
    sfor<0, 32>([&](auto K) { constexpr int n = K / 4, s = K % 4, nn = (K + 1) / 4, sn = (K + 1) % 4;
      sfor<0, 6>([&](auto I) { constexpr int i = I;
        acc[i >> 1][i & 1] = mfma(W(s & 1, i >> 1), af[s & 1][i & 1], K == 0 ? (f32x16){} : acc[i >> 1][i & 1]);
        sb();
        if constexpr (s == 3 && n < 7) { if constexpr (i == 0) { if constexpr (n >= 2) wait_vm<(n < 6 ? 4 : 0)>(); wait_lgkm0(); STB(32 + 2 * n) bar(); STB(33 + 2 * n) } if constexpr (i >= 1 && i < 5) rd1(IC(nn), IC(sn), IC(i - 1)); }
        else if constexpr (K < 31 && i < 4) rd1(IC(nn), IC(sn), I);
        if constexpr (i == 5 && K >= 3 && K < 23) { constexpr int xn = (K + 1) / 4 + 2, xi = (K + 1) % 4;
          if constexpr (xi == 0) setm0i<(xn % 3) * SLT>(w4);
          dmao<xi * 1024>(sx, vx[xi], so_x + chunk((xn + 5) & 7) * 128); }
        if constexpr (K == 31 && i == 1) { if (lane < 32) for (int k = 0; k < 3; ++k) *(LAS float*)(size_t)(M_CS + (w * 3 + k) * 128 + lane * 4) = csg[k][0]; }
        if constexpr (K == 31 && i == 4) { wait_lgkm0();
          if (lane == 0) __hip_atomic_fetch_add((LAS unsigned*)(size_t)M_CNT, 1u, __ATOMIC_RELAXED, __HIP_MEMORY_SCOPE_WORKGROUP); }
        sb();
      });
      if constexpr (!(s & 1)) { for (int k = 0; k < 3; ++k) csg[k] = mfma16(sel, W(s & 1, k), csg[k]); sb(); }
    });
    STAMP(8)
    f32x2 st[2], sd[2];
    { const unsigned ia = (64 * pi + 32 * ((lane >> 2) & 1) + 4 * ((lane >> 3) & 3) + (lane & 3)) * 8;
      for (int rb = 0; rb < 2; ++rb) { st[rb] = *(LAS f32x2*)(size_t)(M_ST + ia + rb * 128); sd[rb] = *(LAS f32x2*)(size_t)(M_SD + ia + rb * 128); } }
    wait_cnt();
    for (int k = 0; k < 3; ++k) {
      float cs = csg[k][0] + *(LAS float*)(size_t)(M_CS + ((w ^ 2) * 3 + k) * 128 + l5 * 4);
      cs = h ? 0.f : cs;
      const float c0 = rbf(cs), r1 = cs - c0, c1 = rbf(r1), c2 = r1 - c1;
      const u32x4 cf = {pk2(c0, c1), pk2(c2, 0.f), 0u, 0u};
      for (int rb = 0; rb < 2; ++rb) { const unsigned nm = __float_as_uint(sd[rb][1]); acc[k][rb] = mfma(cf, (u32x4){nm, nm, 0u, 0u}, acc[k][rb]); }
    }
    STAMP(7)
    sfor<0, 3>([&](auto K) { constexpr int k = K;
      const int ksp = 6 * g + 3 * sb_ + k;
      const unsigned vst = ksp < 86 ? a2pb + lane * 16 + (64 + ksp) * 1024 : OOB;
      sfor<0, 2>([&](auto R_) { constexpr int rb = R_;
        const float rs = st[rb][0], k1 = -2.302208198f * rs, k3 = -0.1029432397f * rs * rs * rs, s2 = sd[rb][0] * sd[rb][0];
        unsigned o[4];
        for (int i = 0; i < 4; ++i) {
          const f32x2 gv = {acc[k][rb][2 * i], acc[k][rb][2 * i + 1]}, uv = {acc[k][rb][8 + 2 * i], acc[k][rb][9 + 2 * i]};
          const f32x2 z = gv * pfma(gv * gv, sp(k3), sp(k1));
          const f32x2 d = pfma((f32x2){__builtin_amdgcn_exp2f(z[0]), __builtin_amdgcn_exp2f(z[1])}, sp(s2), sp(s2));
          const f32x2 r = (gv * uv) * (f32x2){__builtin_amdgcn_rcpf(d[0]), __builtin_amdgcn_rcpf(d[1])};
          o[i] = pk2(r[0], r[1]); }
        __builtin_amdgcn_raw_buffer_store_b128((u32x4){o[0], o[1], o[2], o[3]}, rws, vst + rb * NKS * 1024, 0, 16);
      });
    });
    sb();
    STAMP(9)
    wait_vm<0>();
    set_flag(4 + w);
    STAMP(10)
    const int t = ftid(), ch = t & 15, r4 = t >> 4;
    const int trow = 64 * rsel + 32 * ((r4 >> 2) & 1) + 8 * (r4 & 1) + 2 * (r4 >> 3) + ((r4 >> 1) & 1);
    const int irow = 64 * rsel + 32 * ((r4 >> 2) & 1) + 4 * (r4 >> 3) + (r4 & 3);
    u32x2 xv[4], dk;
    for (int it = 0; it < 4; ++it) ld64(xv[it], sx, trow * 1024 + ch * 8, c * 131072 + ct * 128 + (4 * (it & 1) + 16 * (it >> 1)) * 1024);
    ld64(dk, mk_srd(dskip, SD * 2), ch * 8, ct * 128);
    wait_vm<0>(); for (int it = 0; it < 4; ++it) pin(xv[it]); pin(dk);
    f32x4 base[4]; unsigned ph[4];
    for (int it = 0; it < 4; ++it) { const f32x2 s2 = *(LAS f32x2*)(size_t)(M_ST + (irow + 8 * (it & 1) + 16 * (it >> 1)) * 8);
      for (int e = 0; e < 4; ++e) { const unsigned xu = xv[it][e >> 1], du = dk[e >> 1];
        const float xe = (e & 1) ? hi2f(xu) : lo2f(xu), de = (e & 1) ? hi2f(du) : lo2f(du);
        base[it][e] = xe + rbf(rbf(__builtin_fmaf(xe, s2[0], s2[1])) * de); }
      const int e = 2 * it + (r4 >> 3), d = (e * 11) >> 5;
      ph[it] = (3 + d) * SLT + (e - 3 * d) * 2048 + (r4 & 7) * 256 + (ch ^ (r4 & 7)) * 16;
      pin(base[it]); pin(ph[it]); }
    float* op = out + (c * 128 + trow) * SD + ct * 64 + ch * 4;
    bar();
    STAMP(11)
    for (int it = 0; it < 4; ++it) {
      f32x4 p[4];
      for (int ww = 0; ww < 4; ++ww) p[ww] = *(LAS f32x4*)(size_t)(ph[it] + ww * 6144);
      *(f32x4*)(op + (4 * (it & 1) + 16 * (it >> 1)) * SD) = base[it] + ((p[0] + p[1]) + (p[2] + p[3]));
    }
    if (g == 0) __builtin_amdgcn_raw_buffer_store_b64((u32x2){(unsigned)tag2, (unsigned)(tag2 >> 32)}, rws, t == 0 ? WS_E2 + c * 128 : OOB, 0, 16);
  } else {
    const int lane = ftid() & 63, l5 = lane & 31, h = lane >> 5, blk = (lane >> 4) & 1, q = (lane & 15) >> 2, pp = lane & 3;
    const unsigned va = lane * 16;
    __builtin_amdgcn_s_setprio(3);
    u32x2 ep2, ep1; unsigned plr, pli, pdt;
    ld64_sc1(ep2, sws, 0, WS_E2 + c * 128);
    ld64_sc1(ep1, sws, 0, WS_E1 + g * 128);
    { const unsigned po = (g * 32 + sb_ * 16 + (lane & 15)) * 2;
      ld16(plr, mk_srd(lam_re, SP * 2), po); ld16(pli, mk_srd(lam_im, SP * 2), po); ld16(pdt, mk_srd(logdt, SP * 2), po); }
    { u64 pc; unsigned len, back;
      asm volatile("s_getpc_b64 %0\n.Lpf_here:\n\ts_mov_b32 %1, .Lpf_end-.Lpf_beg\n\ts_mov_b32 %2, .Lpf_here-.Lpf_beg" : "=s"(pc), "=s"(len), "=s"(back));
      for (unsigned o = (idx * 4 + w) * 128; o + 128 < len; o += 16384) sline((pc - back) & ~127ull, o + 128); }
    const unsigned sw0 = ((lane & 7) ^ (lane >> 4)) * 16;
    unsigned vb[2], vw[6], vd[2];
    for (int r = 0; r < 2; ++r) vb[r] = ((lane >> 3) + 7 * r) * 1024 + (sw0 ^ (r * 64));
    for (int i = 0; i < 6; ++i) { int n = 64 * i + lane; vw[i] = (n / 12) * (SF * 2) + (n % 12) * 16 - (i % 3) * 1024; }
    for (int r = 0; r < 2; ++r) vd[r] = ((lane >> 3) + 7 * r) * 1024 + (((lane & 7) ^ (((lane >> 4) & 1) << 2)) * 16);
    const i32x4 sw = mk_srd((w >> 1) ? wu : wg, g == 15 ? 0 : SD * SF * 2);
    const unsigned so_w = (w & 1) * 32 * (SF * 2) + g * 192;
    const unsigned ld_w = ((w >> 1) ? OFF_WU : 0) + (w & 1) * 6144;
    auto opW = [&](auto N, auto I) { constexpr int n = N, i = I;
      if constexpr (i % 3 == 0) setm0i<(3 + n % 3) * SLT + i * 1024>(ld_w);
      dmao<(i % 3) * 1024>(sw, vw[i], so_w + chunk((n + 5) & 7) * (64 * SF * 2)); };
    unsigned ra[4], wa[4];
    for (int s = 0; s < 4; ++s) { ra[s] = pi * 8192 + l5 * 128 + (((2 * s + h) ^ ((l5 >> 1) & 7)) * 16);
      const int row = sb_ * 16 + (lane & 15); wa[s] = OFF_B + blk * 4096 + row * 128 + (((2 * s + h) ^ ((row >> 1) & 7)) * 16); }
    const unsigned selv = (((lane & 15) == 0 && blk == 0) || ((lane & 15) == 4 && blk == 1)) ? 0x3f803f80u : 0u;
    const u32x4 sel = {selv, selv, selv, selv};
    for (int i = 0; i < 6; ++i) pin(vw[i]);
    for (int s = 0; s < 4; ++s) { pin(ra[s]); pin(wa[s]); }
    wait_vm<0>(); pin(ep2); pin(ep1); pin(plr); pin(pli); pin(pdt);
    tag2 = utag(ep2);
    const u64 tag1 = utag(ep1);
    const unsigned t1l = (unsigned)tag1, t1h = (unsigned)(tag1 >> 32);
    const int cc = 2 * c + pi;
    float ar, ai, nai, kr, nkr, nki, a8r, a8i, a16r, a16i, pwr = 1.f, pwi = 0.f, a256r, a256i, mr = 1.f, mi = 0.f;
    { const float lr = lo2f(plr), li = lo2f(pli);
      const float dt = rbf(__expf(lo2f(pdt)));
      const float er = __expf(dt * lr);
      const float rev = dt * li * 0.15915494309189535f;
      const float fr = rev - __builtin_rintf(rev);
      ar = er * __builtin_amdgcn_cosf(fr); ai = er * __builtin_amdgcn_sinf(fr);
      const float nr = ar - 1.f, ni = ai, den = 2.f * __builtin_amdgcn_rcpf(lr * lr + li * li); kr = (nr * lr + ni * li) * den; nki = (nr * li - ni * lr) * den; nkr = -kr; nai = -ai;
      a8r = ar; a8i = ai;
      for (int i = 0; i < 3; ++i) cmul(a8r, a8i, a8r, a8i, a8r, a8i);
      cmul(a16r, a16i, a8r, a8i, a8r, a8i);
      float a32r, a32i, a64r, a64i, a128r, a128i;
      cmul(a32r, a32i, a16r, a16i, a16r, a16i); cmul(a64r, a64i, a32r, a32i, a32r, a32i); cmul(a128r, a128i, a64r, a64i, a64r, a64i); cmul(a256r, a256i, a128r, a128i, a128r, a128i);
      if (blk) { pwr = a16r; pwi = a16i; mr = a64r; mi = a64i; }
      if (h) { cmul(pwr, pwi, pwr, pwi, a32r, a32i); cmul(mr, mi, mr, mi, a128r, a128i); } }
    pin(ar); pin(ai); pin(nai); pin(kr); pin(nkr); pin(nki); pin(a8r); pin(a8i); pin(a16r); pin(a16i); pin(pwr); pin(pwi); pin(a256r); pin(a256i); pin(mr); pin(mi);
    __builtin_amdgcn_s_setprio(0);
    f32x16 sa[2] = {};
    f32x4 csa = {};
    { u32x4 af[2][4][2], wf[2][4];
      auto rdA = [&](int p, int s, unsigned slot) { wf[p][s] = lds_r128(slot + wa[s]); af[p][s][0] = lds_r128(slot + ra[s]); af[p][s][1] = lds_r128(slot + ra[s] + 4096); };
      auto mmA = [&](int p, int s) { sa[0] = mfma(af[p][s][0], wf[p][s], sa[0]); sa[1] = mfma(af[p][s][1], wf[p][s], sa[1]); pin(sa[0]); pin(sa[1]); };
      auto csA = [&](int p) { for (int s = 0; s < 4; ++s) csa = mfma16(sel, wf[p][s], csa); pin(csa); sb(); };
      auto stA = [&](int p, unsigned slot) { wait_lgkm0(); bar(); for (int s = 0; s < 4; ++s) { mmA(p ^ 1, s); sb(); rdA(p, s, slot); sb(); } csA(p ^ 1); };
      bar(); for (int s = 0; s < 4; ++s) rdA(0, s, SLT); sb();
      stA(1, 2 * SLT); { const unsigned hi = lnd(3 * SLT); stA(0, hi); stA(1, hi + SLT); stA(0, hi + 2 * SLT); }
      stA(1, 0); sfor<0, 3>([&](auto I) { opW(IC(0), I); });
      stA(0, SLT); sfor<3, 6>([&](auto I) { opW(IC(0), I); }); sfor<0, 3>([&](auto I) { opW(IC(1), I); });
      stA(1, 2 * SLT);
      sfor<0, 3>([&](auto I) { mmA(1, I); sb(); opW(IC(1), IC(3 + I)); sb(); });
      mmA(1, 3);
      csA(1);
    }
    const i32x4 swd = mk_srd(wd, SF * SD * 2);
    const int kb = 22 * w - (w == 3), cnt = w < 2 ? 22 : 21;
    const unsigned so_wd = kb * 16384 + ct * 128, so_c = ct * 65536 + w * 256;
    const int r2 = (xcd * 11) >> 2;
    auto kstep = [&](int m) { int mm = m + lnd(r2); return mm >= 22 ? mm - 22 : mm; };
    unsigned so_t = 0; i32x4 sc = swd;
    auto wd_op = [&](auto M, auto E, auto R) { constexpr int m = M, e = E, r = R;
      if constexpr (r == 0) { const int mm = kstep(m); so_t = mm < cnt ? so_wd + mm * 16384 : OOB; setm0i<cell_c(e)>(cell_k(e) == 0 ? w6 : cell_k(e) == 1 ? w4 : w2); }
      dmao<r * 1024>(swd, vd[r], so_t);
    };
    auto dma_wd = [&](auto M, auto E) { wd_op(M, E, IC(0)); wd_op(M, E, IC(1)); };
    auto c_op = [&](auto N, auto I) { constexpr int n = N, i = I;
      if constexpr (i == 0) { const int kg = ((n >> 1) + lnd(xcd)) & 3; so_t = so_c + (n & 1) * 32768 + (kg & 1) * 128; sc = mk_srd((kg >> 1) ? Cim : Cre, SD * SP * 2); }
      if constexpr (!(i & 1)) setm0i<cell_c(16 + (i >> 1))>(w2);
      dmao<(i & 1) * 1024>(sc, vb[i & 1], so_t + (i >> 1) * 16384);
    };
    unsigned rcx[4];
    for (int s = 0; s < 4; ++s) rcx[s] = OFF_B + w * 2048 + blk * SLT + (lane & 15) * 128 + (((2 * s + h) ^ ((l5 >> 1) & 7)) * 16);
    const unsigned granb = WS_GRAN + (g * 2 + sb_) * 8192;
    const i32x4 sgr = mk_srd(ws + granb, 8192);
    const unsigned vg = (32 - cc) * 256 + va;
    float R[16], I_[16], csr, csi, er[2], ei[2], sr[4], si[4], prer, prei, cr, ci;
    f32x4 tb[8];
    u32x4 ge[8], fr4[4];
    unsigned hp[2][8];
    int nok = 0;
    auto side = [&](auto P_) { constexpr int P = P_;
      if constexpr (P == 0) { for (int k = 0; k < 8; ++k) tb[k] = *(LAS f32x4*)(size_t)(M_ST + pi * 512 + (lane >> 4) * 128 + k * 16);
        csr = csa[0]; float t = csr; swap32(csr, t); csi = csr; swap16(csr, csi); }
      if constexpr (P >= 1 && P < 9) { constexpr int k = P - 1;
        for (int r = 0; r < 2; ++r) { R[2 * k + r] = sa[0][2 * k + r]; I_[2 * k + r] = sa[1][2 * k + r]; swap16(R[2 * k + r], I_[2 * k + r]); }
        for (int r = 0; r < 2; ++r) sc4(R[2 * k + r], I_[2 * k + r], tb[k][2 * r], tb[k][2 * r + 1], csr, csi); }
      if constexpr (P == 9) { for (int r = 0; r < 2; ++r) { er[r] = R[r]; ei[r] = I_[r]; cf4(er[r], ei[r], ar, ai, R[2 + r], I_[2 + r]); } }
      if constexpr (P >= 16 && P < 22) { constexpr int k = P - 14; for (int r = 0; r < 2; ++r) cf4(er[r], ei[r], ar, ai, R[2 * k + r], I_[2 * k + r]); }
      if constexpr (P == 22) { sr[0] = er[0]; si[0] = ei[0]; cf4(sr[0], si[0], a8r, a8i, er[1], ei[1]); sr[1] = sr[0]; swap16(sr[0], sr[1]); si[1] = si[0]; swap16(si[0], si[1]); }
      if constexpr (P == 23) { sr[2] = sr[0]; swap32(sr[0], sr[2]); si[2] = si[0]; swap32(si[0], si[2]); sr[3] = sr[1]; swap32(sr[1], sr[3]); si[3] = si[1]; swap32(si[1], si[3]); }
      if constexpr (P == 24) { cr = sr[0]; ci = si[0]; cf4(cr, ci, a16r, a16i, sr[1], si[1]); sr[1] = cr; si[1] = ci; cf4(cr, ci, a16r, a16i, sr[2], si[2]); sr[2] = cr; si[2] = ci; }
      if constexpr (P == PPUB) { cf4(cr, ci, a16r, a16i, sr[3], si[3]);
        __builtin_amdgcn_raw_buffer_store_b128((u32x4){__float_as_uint(cr), t1l, __float_as_uint(ci), t1h}, rws, lane < 16 ? granb + (31 - cc) * 256 + va : OOB, 0, 16); }
      if constexpr (P == 32) { prer = h ? (blk ? sr[2] : sr[1]) : (blk ? sr[0] : 0.f); prei = h ? (blk ? si[2] : si[1]) : (blk ? si[0] : 0.f); }
      if constexpr (P >= 33 && P < 41) { constexpr int k = P - 33; for (int r = 0; r < 2; ++r) kc4(R[2 * k + r], I_[2 * k + r], kr, nkr, nki); }
      if constexpr (P == PG) sfor<0, 8>([&](auto K) { constexpr int k = K; ld128_sc1<(k & 3) * 1024>(ge[k], sgr, vg, (k >> 2) * 4096); });
      if constexpr (P == PC) { wait_vm<hb_cnt(PC, false) - hb_cnt(PG, true)>(); for (int i = 0; i < 8; ++i) pin(ge[i]); }
      if constexpr (P == PC || P == PC + 1) { for (int i = 4 * (P - PC); i < 4 * (P - PC) + 4; ++i) nok += (((ge[i][1] ^ t1l) | (ge[i][3] ^ t1h)) == 0u); }
      if constexpr (P == PC + 2) { const int need = (cc + 3 - (lane >> 4)) >> 2;
#pragma unroll 1
        for (unsigned spins = 0; spins < SPIN_MAX; ++spins) {
          if (__all(nok == need)) break;
          __builtin_amdgcn_s_sleep(SLP);
          sfor<0, 8>([&](auto I) { constexpr int i = I; ld128_sc1_rw<(i & 3) * 1024>(ge[i], sgr, vg, (i >> 2) * 4096); });
          wait_vm<0>(); for (int i = 0; i < 8; ++i) pin(ge[i]);
          nok = 0;
          for (int i = 0; i < 8; ++i) nok += (((ge[i][1] ^ t1l) | (ge[i][3] ^ t1h)) == 0u);
        }
        cr = __uint_as_float(ge[7][0]); ci = __uint_as_float(ge[7][2]); cf4(cr, ci, a256r, a256i, __uint_as_float(ge[6][0]), __uint_as_float(ge[6][2])); }
      if constexpr (P == PC + 3 || P == PC + 4) { for (int i = 5 - 3 * (P - PC - 3); i > 2 - 3 * (P - PC - 3); --i) cf4(cr, ci, a256r, a256i, __uint_as_float(ge[i][0]), __uint_as_float(ge[i][2])); }
      if constexpr (P == PC + 5) { cf4(cr, ci, mr, mi, 0.f, 0.f); float t = cr, u = ci; swap16(cr, t); swap16(ci, u); cr = vadd(cr, t); ci = vadd(ci, u); }
      if constexpr (P == PC + 6) { float t = cr, u = ci; swap32(cr, t); swap32(ci, u); cr = vadd(cr, t); ci = vadd(ci, u); cf4(cr, ci, pwr, pwi, prer, prei); }
      if constexpr (P == PC + 7) { sr[0] = cr; si[0] = ci; cf4(cr, ci, a8r, a8i, er[0], ei[0]); er[0] = sr[0]; ei[0] = si[0]; er[1] = cr; ei[1] = ci;
        kc4(er[0], ei[0], kr, nkr, nki); kc4(er[1], ei[1], kr, nkr, nki); }
      sfor<0, 8>([&](auto K) { constexpr int k = K; if constexpr (P == nxt(P2, k)) {
        for (int r = 0; r < 2; ++r) cf4c(k ? R[2 * k - 2 + r] : er[r], k ? I_[2 * k - 2 + r] : ei[r], ar, ai, nai, R[2 * k + r], I_[2 * k + r]);
        hp[0][k] = pk2(R[2 * k], R[2 * k + 1]); hp[1][k] = pk2(I_[2 * k], I_[2 * k + 1]); } });
      static_assert(PT > nxt(P2, 7) && PS > PT && PF > PS && PC % 16 + 7 < 10, "scan schedule");
      if constexpr (P == PT) {
        const unsigned tw = M_T + w * 2560 + (lane & 15) * 160 + (lane >> 4) * 32;
        const unsigned trd = M_T + w * 2560 + (8 * h + q) * 160 + 64 * (pp & 1) + 16 * blk + 8 * (pp >> 1);
        for (int part = 0; part < 2; ++part) {
          for (int i = 0; i < 2; ++i) *(LAS u32x4*)(size_t)(tw + i * 16) = (u32x4){hp[part][4 * i], hp[part][4 * i + 1], hp[part][4 * i + 2], hp[part][4 * i + 3]};
          asm volatile("" ::: "memory");
          fr4[2 * part] = lds_tr2(trd, 640); fr4[2 * part + 1] = lds_tr2(trd + 32, 640);
          asm volatile("" ::: "memory"); } }
      if constexpr (P == PS) { for (int k = 0; k < 4; ++k)
          __builtin_amdgcn_raw_buffer_store_b128(fr4[k], rws, a2pb + (2 * g + sb_) * 1024 + va + ((k & 1) * NKS + (k >> 1) * 32) * 1024, 0, 16); }
      if constexpr (P == PF) { wait_vm<hb_cnt(PF, false) - hb_cnt(PS, true)>(); set_flag(w);
        if (cc == 31) __builtin_amdgcn_raw_buffer_store_b64((u32x2){t1l, t1h}, rws, (lane == 0 && sb_ == 0) ? WS_E1 + g * 128 : OOB, 0, 16); }
    };
    u32x4 cq[8][4];
    u32x2 fs;
    const unsigned fo_s = lane < 8 ? ((4 * w + (lane >> 1)) * 8 + 2 * rsel + (lane & 1)) * 128 : OOB;
    sfor<0, 8>([&](auto N) { constexpr int n = N, g0 = hb_g0(n), done = n ? hb_cnt(16 * n - 1, true) : 0;
      wait_vm<n == 0 ? 6 : n == 1 ? done : done - hb_cnt(16 * (n - 2) + 9, false)>(); if constexpr (n == 0) wait_lgkm0(); STB(30 + 2 * n) bar(); STB(31 + 2 * n)
      if constexpr (n == 0) STAMP(2)
      if constexpr (n > 0) { wait_vm<done - hb_cnt(16 * (n - 1) + hb_g0(n - 1) + 3, false)>(); for (int s = 0; s < 4; ++s) cq[n - 1][s] = lds_r128(rcx[s]); sb(); }
      sfor<0, hb_ops(n)>([&](auto I) { constexpr int i = I, io = i < g0 ? i : i - 4;
        if constexpr (i >= g0 && i < g0 + 4) { if constexpr (i == g0 && n > 0) { for (int s = 0; s < 4; ++s) use(cq[n - 1][s]); } c_op(N, IC(i - g0)); }
        else if constexpr (n < 6) opW(IC(n + 2), IC(io));
        else { constexpr int e = 5 * (n - 6) + (io >> 1); wd_op(IC(e < 4 ? 18 + e : e), IC(e), IC(io & 1)); }
        if constexpr (16 * n + i == PPOLL) ld64_sc1(fs, sws, fo_s, flagb);
        side(IC(16 * n + i));
        sb();
      });
    });
    STAMP(3)
    auto poll = [&](unsigned fo, u32x2 fv) {
#pragma unroll 1
      for (unsigned spins = 0; spins < SPIN_MAX; ++spins) {
        const bool ok = fo == OOB || (fv[0] == (unsigned)tag2 && fv[1] == (unsigned)(tag2 >> 32));
        if (__all(ok)) break;
        __builtin_amdgcn_s_sleep(SLP);
        ld64_sc1(fv, sws, fo, flagb); wait_vm<0>(); pin(fv);
      }
    };
    dma_wd(IC(15), IC(15));
    wait_vm<2 + hb_cnt(127, true) - hb_cnt(PPOLL, true)>(); pin(fs);
    poll(fo_s, fs);
    STAMP(4)
    const unsigned a2c = WS_A2 + (unsigned)(c * 4 + 2 * rsel) * (NKS * 1024);
    const unsigned a2s = a2c + w * 8192, a2g = a2c + (64 + kb) * 1024;
    const unsigned tdl = (8 * h + q) * 128 + (((blk * 2 + (pp >> 1)) ^ ((q >> 1) << 2)) * 16) + (pp & 1) * 8;
    auto rd_wd = [&](int e, u32x4* d) { const unsigned o = (cell_k(e) == 0 ? w6 : cell_k(e) == 1 ? w4 : w2) + cell_c(e);
      d[0] = lds_tr2(tdl + o, 512); d[1] = lds_tr2((tdl ^ 64) + o, 512); };
    f32x16 acc2[2][2];
    u32x4 L[76], wq[4][2], bf[2][2];
    const unsigned va2 = va + NKS * 1024;
    unsigned so_l = 0;
    auto ldH = [&](auto M, auto R) { constexpr int m = M, r = R;
      if constexpr (r == 0) {
        if constexpr (m < 16) { if constexpr (!(m & 3)) { const int kg = ((m >> 2) + lnd(xcd)) & 3; so_l = a2s + ((kg >> 1) * 32 + (kg & 1) * 4) * 1024; } }
        else { const int mm = kstep(m - 16); so_l = mm < cnt ? a2g + mm * 1024 : OOB; } }
      ld128_sc1<(m < 16 ? (m & 3) * 1024 : 0)>(L[2 * m + r], sws, r ? va2 : va, so_l);
    };
    auto ldL = [&](auto M) { ldH(M, IC(0)); ldH(M, IC(1)); };
    sfor<0, p2_l0(0)>(ldL);
    unsigned fo_g, pa[2];
    sfor<0, 38>([&](auto M) { constexpr int m = M, mm = m - 16;
      wait_vm<p2_after(m)>(); pin(L[2 * m]); pin(L[2 * m + 1]);
      auto mm2 = [&](int cb, int rb) {
        if constexpr (m < 16) acc2[cb][rb] = mfma(cq[2 * (m >> 2) + cb][m & 3], L[2 * m + rb], m == 0 ? (f32x16){} : acc2[cb][rb]);
        else acc2[cb][rb] = mfma(mm < 18 ? bf[mm & 1][cb] : wq[mm < 18 ? 0 : mm - 18][cb], L[2 * m + rb], acc2[cb][rb]);
        sb(); };
      mm2(0, 0);
      if constexpr (m >= 15 && mm + 1 < 18) { rd_wd(mm + 1, bf[(mm + 1) & 1]); sb(); }
      mm2(0, 1);
      if constexpr (m == 3) { for (int s = 0; s < 4; ++s) cq[7][s] = lds_r128(rcx[s]); wait_lgkm0(); dma_wd(IC(16), IC(16)); dma_wd(IC(17), IC(17)); }
      if constexpr (m == 7) { for (int e = 0; e < 4; ++e) rd_wd(e, wq[e]); wait_lgkm0(); sfor<0, 4>([&](auto E) { dma_wd(E, E); }); }
      if constexpr (m == 5) { wait_cnt(); dma_wd(IC(10), IC(10)); dma_wd(IC(11), IC(11)); }
      if constexpr (m == 11) { const int ln = ftid() & 63, ks = kb + ln, pg = ks / 6, pb = ks - 6 * pg;
        fo_g = ln < cnt ? (pg * 8 + 4 + 2 * rsel + (pb >= 3)) * 128 : OOB; ld64_sc1(fs, sws, fo_g, flagb); dma_wd(IC(12), IC(12)); dma_wd(IC(13), IC(13)); }
      if constexpr (m == 12) dma_wd(IC(14), IC(14));
      if constexpr (m == 13) { STAMP(5) wait_vm<6 + 2 * (16 - p2_l0(11))>(); pin(fs); poll(fo_g, fs); STAMP(6) }
      if constexpr (m == 33) { const int ln = ftid();
        for (int rb = 0; rb < 2; ++rb) { const int e = 4 * rb + ((ln >> 3) & 3), d = (e * 11) >> 5;
          pa[rb] = (3 + d) * SLT + (e - 3 * d) * 2048 + w6 + (ln & 7) * 256 + ((((ln >> 5) & 1) ^ (ln & 7)) * 16); pin(pa[rb]); } }
      if constexpr (p2_l0(m + 1) == p2_l0(m) + 1) { ldH(IC(p2_l0(m)), IC(0)); sb(); mm2(1, 0); ldH(IC(p2_l0(m)), IC(1)); sb(); mm2(1, 1); }
      else { sfor<p2_l0(m), p2_l0(m + 1)>(ldL); sb(); mm2(1, 0); mm2(1, 1); }
    });
    STAMP(9)
    for (int rb = 0; rb < 2; ++rb) for (int rq = 0; rq < 4; ++rq) for (int cb = 0; cb < 2; ++cb)
      *(LAS f32x4*)(size_t)((pa[rb] ^ (rq * 32)) + cb * 128) =
          (f32x4){acc2[cb][rb][4 * rq], acc2[cb][rb][4 * rq + 1], acc2[cb][rb][4 * rq + 2], acc2[cb][rb][4 * rq + 3]};
    wait_lgkm0(); bar();
    STAMP(11)
  }
  STAMP(12)
  STAMP_OUT
  asm volatile(".Lpf_end:");
}

extern "C" void kernel_launch(void* const* d_in, const int* in_sizes, int n_in, void* d_out, int out_size, void* d_ws, size_t ws_size,
                              hipStream_t stream) {
  (void)hipFuncSetAttribute((const void*)fused, hipFuncAttributeMaxDynamicSharedMemorySize, LDS_BYTES);
  fused<<<256, 512, LDS_BYTES, stream>>>((const u16*)d_in[0], (const u16*)d_in[1], (const u16*)d_in[2], (const u16*)d_in[3], (const u16*)d_in[4],
                                         (const u16*)d_in[5], (const u16*)d_in[6], (const u16*)d_in[7], (const u16*)d_in[8], (const u16*)d_in[10],
                                         (const u16*)d_in[11], (const u16*)d_in[12], (char*)d_ws, (float*)d_out);
}
```

```cpp
#include <hip/hip_runtime.h>
#include <hip/hip_bf16.h>
#include <type_traits>

typedef unsigned short u16;
typedef unsigned long long u64;
typedef short s16x4 __attribute__((ext_vector_type(4)));
typedef __bf16 bf16x2 __attribute__((ext_vector_type(2)));
typedef __bf16 bf16x8 __attribute__((ext_vector_type(8)));
typedef float f32x2 __attribute__((ext_vector_type(2)));
typedef float f32x4 __attribute__((ext_vector_type(4)));
typedef float f32x16 __attribute__((ext_vector_type(16)));
typedef unsigned u32x2 __attribute__((ext_vector_type(2)));
typedef unsigned u32x4 __attribute__((ext_vector_type(4)));
typedef int i32x4 __attribute__((ext_vector_type(4)));
#define LAS __attribute__((address_space(3)))
#define DEVI __device__ __forceinline__

constexpr int SL = 2048, SD = 512, SP = 512, SF = 1376;
constexpr int NKS = 150;
constexpr unsigned WS_E1 = 0;
constexpr unsigned WS_E2 = 2048;
constexpr unsigned WS_GRAN = 262144;
constexpr unsigned WS_FLAG = 524288;
constexpr unsigned WS_A2 = 786432;
constexpr unsigned A2_BYTES = 64u * NKS * 1024u;
constexpr unsigned WS_BYTES = WS_A2 + A2_BYTES;
constexpr unsigned OOB = 0x40000000u;
constexpr int SLT = 24832;
constexpr int OFF_B = 16384, OFF_WU = 12288 + 32;
constexpr int MISC = 6 * SLT;
constexpr int M_ST = MISC, M_SD = MISC + 1024, M_CNT = MISC + 2048, M_CS = MISC + 2304, M_T = MISC + 3840;
constexpr int LDS_BYTES = 163840;
constexpr unsigned SPIN_MAX = 1u << 16;
constexpr int cell_k(int e) { return e >= 15 ? 2 : (e % 5) < 3 ? 0 : 1; }
constexpr int cell_c(int e) { return e >= 15 ? ((e - 13) % 3) * SLT + OFF_B : (e % 5) < 3 ? (3 + (e / 5 + 2) % 3) * SLT + (e % 5) * 2048 : ((e / 5 + 2) % 3) * SLT + (e % 5 - 3) * 2048; }

#define STAMP_DECL
#define STAMP(i) {}
#define STAMP_OUT
#define STB(i) {}
template <int B, int E, class Fn> DEVI void sfor(Fn&& f) {
  if constexpr (B < E) { f(std::integral_constant<int, B>{}); sfor<B + 1, E>(f); }
}
DEVI i32x4 mk_srd(const void* p, unsigned bytes) {
  u64 a = (u64)p; return (i32x4){(int)(unsigned)a, (int)((unsigned)(a >> 32) & 0xffffu), (int)bytes, 0x00020000};
}
DEVI void dma(i32x4 srd, unsigned voff, unsigned soff, unsigned lds) {
  asm volatile("s_mov_b32 m0, %0\n\ts_nop 0\n\tbuffer_load_dwordx4 %1, %2, %3 offen lds" ::"s"(lds), "v"(voff), "s"(srd), "s"(soff) : "memory");
}
template <int OFF> DEVI void ld128_sc1(u32x4& d, i32x4 srd, unsigned voff, unsigned soff) {
  asm volatile("buffer_load_dwordx4 %0, %1, %2, %3 offen offset:%4 sc1" : "=v"(d) : "v"(voff), "s"(srd), "s"(soff), "n"(OFF) : "memory");
}
template <int OFF> DEVI void ld128_sc1_rw(u32x4& d, i32x4 srd, unsigned voff, unsigned soff) {
  asm volatile("buffer_load_dwordx4 %0, %1, %2, %3 offen offset:%4 sc1" : "+v"(d) : "v"(voff), "s"(srd), "s"(soff), "n"(OFF) : "memory");
}
DEVI void ld64_sc1(u32x2& d, i32x4 srd, unsigned voff, unsigned soff) {
  asm volatile("buffer_load_dwordx2 %0, %1, %2, %3 offen sc1" : "=v"(d) : "v"(voff), "s"(srd), "s"(soff) : "memory");
}
DEVI void ld64(u32x2& d, i32x4 srd, unsigned voff, unsigned soff) {
  asm volatile("buffer_load_dwordx2 %0, %1, %2, %3 offen" : "=v"(d) : "v"(voff), "s"(srd), "s"(soff) : "memory");
}
DEVI void ld16(unsigned& d, i32x4 srd, unsigned voff) {
  asm volatile("buffer_load_ushort %0, %1, %2, 0 offen" : "=v"(d) : "v"(voff), "s"(srd) : "memory");
}
template <int N> DEVI void wait_vm() { asm volatile("s_waitcnt vmcnt(%0)" ::"n"(N) : "memory"); }
template <class T> DEVI void pin(T& a) { asm volatile("" : "+v"(a)::"memory"); }
template <class T> DEVI void use(const T& a) { asm volatile("" ::"v"(a) : "memory"); }
DEVI int lnd(int a) { asm volatile("" : "+s"(a)); return a; }
DEVI int ftid() { int t = threadIdx.x; asm volatile("" : "+v"(t)); return t; }
DEVI void wait_lgkm0() { asm volatile("s_waitcnt lgkmcnt(0)" ::: "memory"); }
DEVI void bar() { asm volatile("s_barrier" ::: "memory"); }
DEVI void sb() { __builtin_amdgcn_sched_barrier(0); }
DEVI float lo2f(unsigned u) { return __uint_as_float(u << 16); }
DEVI float hi2f(unsigned u) { return __uint_as_float(u & 0xffff0000u); }
DEVI unsigned pk2(float a, float b) { f32x2 v = {a, b}; return __builtin_bit_cast(unsigned, __builtin_convertvector(v, bf16x2)); }
DEVI float rbf(float a) { return lo2f(pk2(a, a)); }
DEVI u32x4 lds_r128(unsigned a) { return *(LAS u32x4*)(size_t)a; }
DEVI u32x2 lds_tr(unsigned a) { return __builtin_bit_cast(u32x2, __builtin_amdgcn_ds_read_tr16_b64_v4i16((LAS s16x4*)(size_t)a)); }
DEVI u32x4 lds_tr2(unsigned a, unsigned d) { u32x2 x = lds_tr(a), y = lds_tr(a + d); return (u32x4){x[0], x[1], y[0], y[1]}; }
DEVI f32x16 mfma(u32x4 a, u32x4 b, f32x16 c) {
  return __builtin_amdgcn_mfma_f32_32x32x16_bf16(__builtin_bit_cast(bf16x8, a), __builtin_bit_cast(bf16x8, b), c, 0, 0, 0);
}
DEVI float dot2(unsigned a, unsigned b, float c) {
  return __builtin_amdgcn_fdot2_f32_bf16(__builtin_bit_cast(bf16x2, a), __builtin_bit_cast(bf16x2, b), c, false);
}
DEVI void swap16(float& a, float& b) { auto r = __builtin_amdgcn_permlane16_swap(__float_as_uint(a), __float_as_uint(b), false, false); a = __uint_as_float(r[0]); b = __uint_as_float(r[1]); }
DEVI void swap32(float& a, float& b) { auto r = __builtin_amdgcn_permlane32_swap(__float_as_uint(a), __float_as_uint(b), false, false); a = __uint_as_float(r[0]); b = __uint_as_float(r[1]); }
DEVI float sum32(float v) { float a = v, b = v; swap32(a, b); return a + b; }
DEVI float sum16(float v) { float a = v, b = v; swap16(a, b); return a + b; }
DEVI void cmul(float& dr, float& di, float ar, float ai, float br, float bi) { float t = ar * br - ai * bi; di = ar * bi + ai * br; dr = t; }
DEVI void cfma(float& hr, float& hi, float ar, float ai, float br, float bi) {
  float t = __builtin_fmaf(ar, hr, __builtin_fmaf(-ai, hi, br)); hi = __builtin_fmaf(ar, hi, __builtin_fmaf(ai, hr, bi)); hr = t;
}

template <int OFF> DEVI void dmao(i32x4 srd, unsigned voff, unsigned soff) {
  asm volatile("buffer_load_dwordx4 %0, %1, %2 offen offset:%3 lds" ::"v"(voff), "s"(srd), "s"(soff), "n"(OFF) : "memory");
}
DEVI void setm0(unsigned lds) { asm volatile("s_mov_b32 m0, %0\n\ts_nop 0" ::"s"(lds) : "memory"); }
DEVI void touch(i32x4 srd, unsigned voff, unsigned soff) {
  asm volatile("buffer_load_dword %0, %1, %2 offen lds" ::"v"(voff), "s"(srd), "s"(soff) : "memory");
}
DEVI f32x4 mfma16(u32x4 a, u32x4 b, f32x4 c) {
  return __builtin_amdgcn_mfma_f32_16x16x32_bf16(__builtin_bit_cast(bf16x8, a), __builtin_bit_cast(bf16x8, b), c, 0, 0, 0);
}
DEVI float vadd(float a, float b) { float r; asm("v_add_f32 %0, %1, %2" : "=v"(r) : "v"(a), "v"(b)); return r; }
DEVI float vfma(float a, float b, float c) { float r; asm("v_fma_f32 %0, %1, %2, %3" : "=v"(r) : "v"(a), "v"(b), "v"(c)); return r; }
DEVI f32x2 pfma(f32x2 a, f32x2 b, f32x2 c) { return __builtin_elementwise_fma(a, b, c); }
DEVI f32x2 sp(float a) { return (f32x2){a, a}; }
DEVI void cfma2(f32x2& hr, f32x2& hi, f32x2 a, f32x2 br, f32x2 bi) {
  f32x2 t1, t2;
  asm("v_pk_fma_f32 %2, %4, %1, %5 op_sel:[1,0,0] op_sel_hi:[1,1,1] neg_lo:[1,0,0] neg_hi:[1,0,0]\n\t"
      "v_pk_fma_f32 %3, %4, %0, %6 op_sel:[1,0,0] op_sel_hi:[1,1,1]\n\t"
      "v_pk_fma_f32 %0, %4, %0, %2 op_sel:[0,0,0] op_sel_hi:[0,1,1]\n\t"
      "v_pk_fma_f32 %1, %4, %1, %3 op_sel:[0,0,0] op_sel_hi:[0,1,1]"
      : "+v"(hr), "+v"(hi), "=&v"(t1), "=&v"(t2) : "v"(a), "v"(br), "v"(bi));
}
DEVI void cout2(unsigned& o0, unsigned& o1, f32x2 k, f32x2 gr, f32x2 gi) {
  f32x2 t1, t2;
  asm("v_pk_mul_f32 %0, %2, %4 op_sel:[1,0] op_sel_hi:[1,1]\n\t"
      "v_pk_mul_f32 %1, %2, %3 op_sel:[1,0] op_sel_hi:[1,1]\n\t"
      "v_pk_fma_f32 %0, %2, %3, %0 op_sel:[0,0,0] op_sel_hi:[0,1,1] neg_lo:[0,0,1] neg_hi:[0,0,1]\n\t"
      "v_pk_fma_f32 %1, %2, %4, %1 op_sel:[0,0,0] op_sel_hi:[0,1,1] neg_lo:[1,0,1] neg_hi:[1,0,1]"
      : "=&v"(t1), "=&v"(t2) : "v"(k), "v"(gr), "v"(gi));
  o0 = pk2(t1[0], t1[1]); o1 = pk2(t2[0], t2[1]);
}
#define SLP 4
#define WN 10
#define PGV 50
#define PCV 64
#define P2D 16
#define PTV 88
#define PSV 89
#define PFV 114
#define PFW 1
#define PFW_S 2
#define PFW_O 0
#define PFX 0
#define ROTX (2 * (xcd >> 1) + 4 * (xcd & 1))
#define PLATE 0
#define AVAR 0
#define STA(i) {}
#define HPA 0
#define WEARLY 0
#define PFW0 0
#define HPB 0
#define HPC 0
#define PFG 0
#define PFG_O 0
#define PCK 13
#define WG 6
#define EPK 1
#define CPE 0
#define LEADX (c + g)
#define LEAD 1
#define IC(x) std::integral_constant<int, (x)>{}
constexpr int p2_extra(int m) { return ((m == 0 || m == 1 || m == 5 || (m >= 7 && m <= 10)) ? 2 : 0) + (m == PCK - 1 ? 1 : 0); }
constexpr int p2_l0(int m) { return m <= PCK ? (m + WN / 2 < 16 ? m + WN / 2 : 16) : (m + WG < 38 ? m + WG : 38); }
constexpr int p2_after(int m) {
  int pos = 0, posL[40] = {};
  for (int k = 0; k < p2_l0(0); ++k) { pos += 2; posL[k] = pos; }
  for (int s = 0; s < m; ++s) { pos += p2_extra(s); for (int k = p2_l0(s); k < p2_l0(s + 1); ++k) { pos += 2; posL[k] = pos; } }
  return pos - posL[m];
}
constexpr int PPUB = 25, PG = PGV, PC = PCV, P2 = PC + P2D, PT = PTV, PS = PSV, PF = PFV, PPOLL = -1;
constexpr int hb_ops(int n) { return n < 6 ? 10 : 14; }
constexpr int hb_g0(int n) { return n < 6 ? 3 : 4; }
constexpr int hb_extra(int P) { return P == PPUB ? 1 : P == PG ? 8 : P == PS ? 4 : P == PF ? 1 : P == PPOLL ? 1 : 0; }
constexpr int hb_cnt(int P, bool with_extra) {
  int t = 0;
  for (int n = 0; n < 8; ++n) for (int i = 0; i < hb_ops(n); ++i) { const int p = 16 * n + i; if (p > P) return t; t += (n >= 6 || (i >= hb_g0(n) && i < hb_g0(n) + 4)); if (p < P || with_extra) t += hb_extra(p); }
  return t;
}
constexpr int nxt(int P, int d) { for (; d > 0; --d) { ++P; if (P % 16 >= hb_ops(P / 16)) P = (P / 16 + 1) * 16; } return P; }
DEVI u64 utag(u32x2 e) { const u64 t = (((u64)(unsigned)__builtin_amdgcn_readfirstlane(e[1]) << 32) | (unsigned)__builtin_amdgcn_readfirstlane(e[0])) + 1; return t ? t : 1; }
template <int CTRL> DEVI float dppf(float v) { return __int_as_float(__builtin_amdgcn_update_dpp(0, __float_as_int(v), CTRL, 0xf, 0xf, true)); }
DEVI void sline(u64 base, unsigned off) {
  asm volatile("s_load_dwordx2 s[98:99], %0, %1" ::"s"(base), "s"(off + 60) : "memory", "s98", "s99");
}
template <int IMM> DEVI void setm0i(unsigned base) { asm volatile("s_add_i32 m0, %0, %1\n\ts_nop 0" ::"s"(base), "n"(IMM) : "memory", "scc"); }
DEVI void miss2(unsigned& n, u32x4 a, u32x4 b, unsigned tl, unsigned th) {
  unsigned t, u;
  asm("v_xor_b32 %1, %7, %3\n\tv_xor_b32 %2, %7, %5\n\tv_bitop3_b32 %1, %4, %8, %1 bitop3:0xbe\n\tv_bitop3_b32 %2, %6, %8, %2 bitop3:0xbe\n\t"
      "v_min_u32 %1, 1, %1\n\tv_min_u32 %2, 1, %2\n\tv_add3_u32 %0, %0, %1, %2"
      : "+v"(n), "=&v"(t), "=&v"(u) : "v"(a[1]), "v"(a[3]), "v"(b[1]), "v"(b[3]), "s"(tl), "s"(th));
}
DEVI unsigned geglu2(float g0, float g1, float u0, float u1, float k1, float k3, float s2) {
  float a, b;
  asm("v_mul_f32 %0, %4, %4\n\tv_mul_f32 %1, %5, %5\n\tv_fma_f32 %0, %0, %7, %6\n\tv_fma_f32 %1, %1, %7, %6\n\t"
      "v_mul_f32 %0, %0, %4\n\tv_mul_f32 %1, %1, %5\n\tv_exp_f32 %0, %0\n\tv_exp_f32 %1, %1\n\tv_mul_f32 %2, %2, %4\n\t"
      "v_fma_f32 %0, %0, %8, %8\n\tv_fma_f32 %1, %1, %8, %8\n\tv_rcp_f32 %0, %0\n\tv_rcp_f32 %1, %1\n\tv_mul_f32 %3, %3, %5\n\t"
      "v_mul_f32 %0, %0, %2\n\tv_mul_f32 %1, %1, %3\n\tv_cvt_pk_bf16_f32 %0, %0, %1"
      : "=&v"(a), "=&v"(b), "+v"(u0), "+v"(u1) : "v"(g0), "v"(g1), "v"(k1), "v"(k3), "v"(s2));
  return __float_as_uint(a);
}
DEVI void cf4(float& hr, float& hi, float ar, float ai, float br, float bi) {
  float t, u;
  asm("v_fma_f32 %0, -%5, %3, %6\n\tv_fma_f32 %1, %5, %2, %7\n\tv_fmac_f32 %0, %4, %2\n\tv_fmac_f32 %1, %4, %3"
      : "=&v"(t), "=&v"(u) : "v"(hr), "v"(hi), "v"(ar), "v"(ai), "v"(br), "v"(bi));
  hr = t; hi = u;
}
DEVI void csq(float& dr, float& di, float r, float i) {
  float t, u;
  asm("v_mul_f32 %0, %2, %2\n\tv_mul_f32 %1, %2, %3\n\tv_fma_f32 %0, -%3, %3, %0\n\tv_add_f32 %1, %1, %1" : "=&v"(t), "=&v"(u) : "v"(r), "v"(i));
  dr = t; di = u;
}
DEVI void cf4c(float hr, float hi, float ar, float ai, float nai, float& br, float& bi) {
  asm("v_fmac_f32 %0, %5, %3\n\tv_fmac_f32 %1, %6, %2\n\tv_fmac_f32 %0, %4, %2\n\tv_fmac_f32 %1, %4, %3"
      : "+v"(br), "+v"(bi) : "v"(hr), "v"(hi), "v"(ar), "v"(ai), "v"(nai));
}
DEVI void sc4(float& r, float& i, float rs, float rc, float csr, float csi) {
  asm("v_mul_f32 %0, %2, %0\n\tv_mul_f32 %1, %2, %1\n\tv_fmac_f32 %0, %3, %4\n\tv_fmac_f32 %1, %3, %5"
      : "+v"(r), "+v"(i) : "v"(rs), "v"(rc), "v"(csr), "v"(csi));
}
DEVI void kc4(float& r, float& i, float kr, float nkr, float nki) {
  float t, u;
  asm("v_mul_f32 %0, %4, %2\n\tv_mul_f32 %1, %5, %3\n\tv_fmac_f32 %0, %6, %3\n\tv_fmac_f32 %1, %6, %2"
      : "=&v"(t), "=&v"(u) : "v"(r), "v"(i), "v"(kr), "v"(nkr), "v"(nki));
  r = t; i = u;
}

__global__ __launch_bounds__(512, 2) void fused(const u16* __restrict__ x, const u16* __restrict__ lam_re, const u16* __restrict__ lam_im,
                                                const u16* __restrict__ Bre, const u16* __restrict__ Bim, const u16* __restrict__ Cre,
                                                const u16* __restrict__ Cim, const u16* __restrict__ logdt, const u16* __restrict__ dskip,
                                                const u16* __restrict__ wg, const u16* __restrict__ wu, const u16* __restrict__ wd,
                                                char* __restrict__ ws, float* __restrict__ out) {
  extern __shared__ __attribute__((aligned(16))) char shm[];
  asm volatile(".Lpf_beg:");
  STAMP_DECL STAMP(0)
  unsigned perm;
  const i32x4 sx = mk_srd(x, SL * SD * 2);
  {
    const int b0 = blockIdx.x, t = threadIdx.x, lead = ((b0 >> 6) + (b0 >> 3)) & 3, rot = (b0 * 5) & 6;
    perm = (0x76543210u - (15u << (4 * lead)) * (1u + ((((lead + 1) >> 2) ^ 1) << 16)) + rot * 0x11111111u) & 0x77777777u;
    const unsigned v0 = (((t >> 2) & 64) + ((t >> 3) & 16) + ((t >> 4) & 5) + (t & 40)) * 1024 + (((t & 7) ^ ((t >> 4) & 3)) * 16);
    const unsigned so = (((b0 & 6) * 2 + (b0 >> 6)) << 17) + (perm & 7) * 128;
    asm volatile("" ::"s"(lam_re), "s"(lam_im), "s"(Bre), "s"(Bim), "s"(Cre), "s"(Cim), "s"(logdt), "s"(dskip), "s"(wg), "s"(wu), "s"(wd), "s"(ws), "s"(out));
    const int j0 = __builtin_amdgcn_readfirstlane(t >> 6);
    const unsigned v1 = (v0 ^ 64) + 1024;
    setm0i<SLT>(j0 * 2048); dmao<0>(sx, v0, so); dmao<1024>(sx, v1, so);
    const i32x4 sb0 = mk_srd((j0 >> 2) ? Bim : Bre, SP * SD * 2);
    const unsigned vb0 = ((t >> 3) & 31) * 1024 + (((t & 7) ^ ((t >> 4) & 7)) * 16), sob = (((b0 & 1) * 8 + ((b0 >> 3) & 7)) << 15);
    setm0i<SLT + OFF_B>(j0 * 1024); dmao<0>(sb0, vb0, sob + (perm & 7) * 128);
    const unsigned k1 = ((perm >> 4) & 7) * 128;
    setm0i<2 * SLT>(j0 * 2048); dmao<0>(sx, v0, (so & ~1023u) + k1); dmao<1024>(sx, v1, (so & ~1023u) + k1);
    setm0i<2 * SLT + OFF_B>(j0 * 1024); dmao<0>(sb0, vb0, sob + k1);
  }
  auto chunk = [&](int v) { return (perm >> (4 * v)) & 7; };
  const int j = __builtin_amdgcn_readfirstlane(ftid() >> 6), w = j & 3, sb_ = w & 1, pi = w >> 1;
  const int b = lnd(blockIdx.x), xcd = b & 7, idx = b >> 3;
  const int c = (xcd >> 1) * 4 + (idx >> 3), rsel = xcd & 1, gi = idx & 7, g = rsel * 8 + gi;
  auto get_fb = [&]() { const int l = (lnd(gi) + 1) >> 3; return (6 * (g - rsel)) * (1 - l) + (84 + rsel) * l; };
  auto get_ct = [&]() { const int slow = (3 - lnd((int)((u64)wd >> 7))) & 7; return lnd(gi == 7 ? slow : gi == slow ? 7 : gi); };
  const i32x4 sws = mk_srd(ws, WS_BYTES);
  const __amdgpu_buffer_rsrc_t rws = __builtin_amdgcn_make_buffer_rsrc(ws, 0, WS_BYTES, 0x00020000);
  const unsigned flagb = WS_FLAG + c * 16384;
  const unsigned a2pb = WS_A2 + (unsigned)(c * 4 + 2 * pi) * (NKS * 1024);
  const unsigned w4 = w * 4096, w2 = w * 2048, w6 = w * 6144;
  const unsigned so_x = (c * 128 + 64 * (w >> 1) + 16 * (w & 1)) * 1024;
  u64 tag2 = 0;
  auto set_flag = [&](int fi) {
    __builtin_amdgcn_raw_buffer_store_b64((u32x2){(unsigned)tag2, (unsigned)(tag2 >> 32)}, rws, (ftid() & 63) == 0 ? flagb + (g * 8 + fi) * 128 : OOB, 0, 16);
  };
  auto wait_cnt = [&]() {
#pragma unroll 1
    for (unsigned spins = 0; spins < SPIN_MAX; ++spins)
      if (__builtin_amdgcn_readfirstlane(__hip_atomic_load((LAS unsigned*)(size_t)M_CNT, __ATOMIC_RELAXED, __HIP_MEMORY_SCOPE_WORKGROUP)) == 4u) break;
    asm volatile("" ::: "memory");
  };

  if (j < 4) {
    const int lane = ftid() & 63, l5 = lane & 31, h = lane >> 5, blk = (lane >> 4) & 1, q = (lane & 15) >> 2, pp = lane & 3;
    unsigned vx[4], vw[3], ra[4], wt[2];
    const i32x4 sw = mk_srd((w >> 1) ? wu : wg, SD * SF * 2);
    const unsigned ld_w = ((w >> 1) ? OFF_WU : 0) + (w & 1) * 6144;
    unsigned so_w = 0;
    u32x4 sel;
    {
      const unsigned sw0 = ((lane & 7) ^ (lane >> 4)) * 16;
      for (int r = 0; r < 4; ++r) vx[r] = (32 * (lane >> 5) + 8 * ((lane >> 3) & 1) + ((lane >> 4) & 1) + r) * 1024 + (sw0 ^ ((r & 1) * 64));
      auto ldX = [&](auto S) { constexpr int s = S;
        const unsigned so = so_x + chunk(s) * 128;
        setm0i<((s + 1) % 6) * SLT>(w4);
        dmao<0>(sx, vx[0], so); dmao<1024>(sx, vx[1], so); dmao<2048>(sx, vx[2], so); dmao<3072>(sx, vx[3], so);
      };
      u32x2 ep2;
      STAMP(1)
      if (w == 0 && lane == 0) *(LAS unsigned*)(size_t)M_CNT = 0;
      const unsigned xa = w * 4096 + lane * 16;
      unsigned ti;
      auto setup = [&]() {
        for (int s = 0; s < 4; ++s) { ra[s] = pi * 8192 + l5 * 128 + (((2 * (s ^ pi) + h) ^ ((l5 >> 1) & 7)) * 16); pin(ra[s]); }
        for (int p = 0; p < 2; ++p) { wt[p] = 3 * SLT + (blk ? OFF_WU : 0) + (8 * h + q) * 192 + (((pp & 1) << 1) | (pp >> 1)) * 8 + sb_ * 96 + (p ? -pi * 3072 : pi * 3072); pin(wt[p]); }
        const unsigned selv = (((lane & 15) == 0 && blk == 0) || ((lane & 15) == 4 && blk == 1)) ? 0x3f803f80u : 0u; sel = (u32x4){selv, selv, selv, selv}; pin(sel);
        ti = (lane & 4) ? M_CNT + 64 - M_ST : (64 * (w >> 1) + 32 * (lane >> 5) + 16 * (w & 1) + 4 * (lane & 3) + ((lane >> 3) & 3)) * 8;
        pin(ti);
      };
      auto setup2 = [&]() { const int q12 = (lane * 43) >> 9, r12 = lane - 12 * q12;
        for (int i = 0; i < 3; ++i) { const int r = r12 + 4 * i, cy = r >= 12, col = r - 12 * cy;
          vw[i] = (gi == 7 && col >= 2) ? OOB : (5 * i + q12 + cy) * (SF * 2) + col * 16 - i * 1024; pin(vw[i]); }
        so_w = (w & 1) * 32 * (SF * 2) + get_fb() * 32; };
      float s1[4] = {}, s2[4] = {};
      auto stats = [&](auto S, auto&& mid) { constexpr int s = S;
        u32x4 xr[4];
        for (int k = 0; k < 4; ++k) xr[k] = lds_r128(((s + 1) % 6) * SLT + k * 1024 + xa);
        sb(); mid(); sb();
        for (int e = 0; e < 4; ++e) for (int i = 0; i < 4; ++i) { const unsigned u = xr[i][e]; s1[i] = dot2(u, 0x3f803f80u, s1[i]); s2[i] = dot2(u, u, s2[i]); }
        for (int i = 0; i < 4; ++i) { pin(s1[i]); pin(s2[i]); }
        sb(); };
      wait_vm<3>(); wait_lgkm0(); STA(46) bar(); STA(47)
      stats(IC(0), [&] { ldX(IC(2)); ld64_sc1(ep2, sws, 0, WS_E2 + c * 128); });
      wait_vm<5>(); STA(48) bar(); STA(49)
      stats(IC(1), [&] { ldX(IC(3)); setup(); });
      sfor<2, 8>([&](auto S) { constexpr int s = S;
        wait_vm<(s < 7 ? 4 : 0)>();
        stats(S, [&] { if constexpr (s < 6) ldX(IC(s + 2)); if constexpr (s == 6) setup2(); });
        if constexpr (s < 7) { STA(46 + 2 * s) bar(); STA(47 + 2 * s) }
      });
      pin(ep2); tag2 = utag(ep2);
      float t1[2], t2[2];
      for (int k = 0; k < 2; ++k) { const bool o = lane & 1;
        t1[k] = (o ? s1[2 * k + 1] : s1[2 * k]) + dppf<0xB1>(o ? s1[2 * k] : s1[2 * k + 1]); t2[k] = (o ? s2[2 * k + 1] : s2[2 * k]) + dppf<0xB1>(o ? s2[2 * k] : s2[2 * k + 1]); }
      const bool o = lane & 2;
      float sm = (o ? t1[1] : t1[0]) + dppf<0x4E>(o ? t1[0] : t1[1]), sq = (o ? t2[1] : t2[0]) + dppf<0x4E>(o ? t2[0] : t2[1]);
      sm += dppf<0x104>(sm); sq += dppf<0x104>(sq);
      const float mean = sm * (1.f / SD);
      const float var = fmaxf(sq * (1.f / SD) - mean * mean, 0.f);
      const float sd = rbf(__builtin_amdgcn_sqrtf(rbf(rbf(var) + 1e-5f)));
      const float r_ = __builtin_amdgcn_rcpf(sd); const unsigned nm = pk2(-mean, -mean);
      *(LAS f32x2*)(size_t)(M_ST + ti) = (f32x2){r_, lo2f(nm) * r_}; *(LAS f32x2*)(size_t)(M_SD + ti) = (f32x2){sd, __uint_as_float(nm)};
    }
    f32x16 acc[3][2];
    f32x4 csg[3] = {};
    u32x4 af[2][2]; u32x2 wf[2][3][2];
    auto rd1 = [&](auto N, auto S, auto I) { constexpr int n = N, s = S, i = I;
      const unsigned a_ = ra[s] + (n % 3) * SLT, t_ = wt[s & 1] + (n % 3) * SLT + s * 3072;
      if constexpr (i == 0) { wf[s & 1][0][0] = lds_tr(t_); wf[s & 1][0][1] = lds_tr(t_ + 768); }
      if constexpr (i == 1) { af[s & 1][0] = lds_r128(a_); af[s & 1][1] = lds_r128(a_ + 4096); }
      if constexpr (i == 2) { wf[s & 1][1][0] = lds_tr(t_ + 32); wf[s & 1][1][1] = lds_tr(t_ + 32 + 768); }
      if constexpr (i == 3) { wf[s & 1][2][0] = lds_tr(t_ + 64); wf[s & 1][2][1] = lds_tr(t_ + 64 + 768); }
    };
    auto W = [&](int p, int k) { return (u32x4){wf[p][k][0][0], wf[p][k][0][1], wf[p][k][1][0], wf[p][k][1][1]}; };
    STAMP(22)
    wait_vm<0>(); wait_lgkm0(); bar();
    STAMP(2)
    sfor<0, 4>([&](auto I) { rd1(IC(0), IC(0), I); });
    sfor<0, 32>([&](auto K) { constexpr int n = K / 4, s = K % 4, nn = (K + 1) / 4, sn = (K + 1) % 4;
      sfor<0, 6>([&](auto I) { constexpr int i = I;
        acc[i >> 1][i & 1] = mfma(W(s & 1, i >> 1), af[s & 1][i & 1], K == 0 ? (f32x16){} : acc[i >> 1][i & 1]);
        sb();
        if constexpr (s == 3 && n < 7) { if constexpr (i == 0) { if constexpr (n >= 1) wait_vm<(n < 6 ? 10 : 0)>(); wait_lgkm0(); STB(32 + 2 * n) bar(); STB(33 + 2 * n) } if constexpr (i >= 1 && i < 5) rd1(IC(nn), IC(sn), IC(i - 1)); }
        else if constexpr (K < 31 && i < 4) rd1(IC(nn), IC(sn), I);
        if constexpr (i >= 4 && K < 23) {
          constexpr int xn = (K + 1) / 4 + 2, kk = (K + 1) % 4, o0 = i == 5 ? (kk == 0 ? 0 : 3 * kk) : (kk == 0 ? 0 : 3 * kk - 1), o1 = i == 5 ? (kk == 3 ? 10 : 3 * kk + 2) : (kk == 0 ? 0 : 3 * kk);
          sfor<o0, o1>([&](auto O) { constexpr int o = O;
            if constexpr (o < 4) { if constexpr (xn > 2) { if constexpr (o == 0) setm0i<(xn % 3) * SLT>(w4); dmao<o * 1024>(sx, vx[o], so_x + chunk((xn + 5) & 7) * 128); } }
            else { constexpr int u = o - 4; if constexpr (u % 3 == 0) setm0i<(3 + xn % 3) * SLT + u * 1024>(ld_w);
              dmao<(u % 3) * 1024>(sw, vw[u % 3], so_w + chunk((xn + 5) & 7) * (64 * SF * 2) + (u / 3) * 16 * (SF * 2)); } }); }
        if constexpr (K == 31 && i == 1) { if (lane < 32) for (int k = 0; k < 3; ++k) *(LAS float*)(size_t)(M_CS + (w * 3 + k) * 128 + lane * 4) = csg[k][0]; }
        if constexpr (K == 31 && i == 4) { wait_lgkm0();
          if (lane == 0) __hip_atomic_fetch_add((LAS unsigned*)(size_t)M_CNT, 1u, __ATOMIC_RELAXED, __HIP_MEMORY_SCOPE_WORKGROUP); }
        sb();
      });
      if constexpr (!(s & 1)) { for (int k = 0; k < 3; ++k) csg[k] = mfma16(sel, W(s & 1, k), csg[k]); sb(); }
    });
    STAMP(8)
    f32x2 st[2], sd[2];
    { const unsigned ia = (64 * pi + 32 * ((lane >> 2) & 1) + 4 * ((lane >> 3) & 3) + (lane & 3)) * 8;
      for (int rb = 0; rb < 2; ++rb) { st[rb] = *(LAS f32x2*)(size_t)(M_ST + ia + rb * 128); sd[rb] = *(LAS f32x2*)(size_t)(M_SD + ia + rb * 128); } }
    wait_cnt();
    for (int k = 0; k < 3; ++k) {
      float cs = csg[k][0] + *(LAS float*)(size_t)(M_CS + ((w ^ 2) * 3 + k) * 128 + l5 * 4);
      cs = h ? 0.f : cs;
      const float c0 = rbf(cs), r1 = cs - c0, c1 = rbf(r1), c2 = r1 - c1;
      const u32x4 cf = {pk2(c0, c1), pk2(c2, 0.f), 0u, 0u};
      for (int rb = 0; rb < 2; ++rb) { const unsigned nm = __float_as_uint(sd[rb][1]); acc[k][rb] = mfma(cf, (u32x4){nm, nm, 0u, 0u}, acc[k][rb]); }
    }
    for (int k = 0; k < 3; ++k) { pin(acc[k][0]); pin(acc[k][1]); }
    asm volatile("s_nop 11" ::: "memory");
    for (int k = 0; k < 3; ++k) { pin(acc[k][0]); pin(acc[k][1]); }
    __builtin_amdgcn_s_setprio(CPE);
    STAMP(7)
    const int fb = get_fb();
    sfor<0, 3>([&](auto K) { constexpr int k = K;
      const unsigned vst = (gi < 7 || 3 * sb_ + k == 0) ? a2pb + lane * 16 + (64 + fb + 3 * sb_ + k) * 1024 : OOB;
      sfor<0, 2>([&](auto R_) { constexpr int rb = R_;
        const float rs = st[rb][0], k1 = -2.302208198f * rs, k3 = -0.1029432397f * rs * rs * rs, s2 = sd[rb][0] * sd[rb][0];
        unsigned o[4];
        for (int i = 0; i < 4; ++i) {
          const f32x2 gv = {acc[k][rb][2 * i], acc[k][rb][2 * i + 1]}, uv = {acc[k][rb][8 + 2 * i], acc[k][rb][9 + 2 * i]};
          const f32x2 z = gv * pfma(gv * gv, sp(k3), sp(k1));
          const f32x2 d = pfma((f32x2){__builtin_amdgcn_exp2f(z[0]), __builtin_amdgcn_exp2f(z[1])}, sp(s2), sp(s2));
          const f32x2 r = (gv * uv) * (f32x2){__builtin_amdgcn_rcpf(d[0]), __builtin_amdgcn_rcpf(d[1])};
          o[i] = pk2(r[0], r[1]); }
        __builtin_amdgcn_raw_buffer_store_b128((u32x4){o[0], o[1], o[2], o[3]}, rws, vst + rb * NKS * 1024, 0, 16);
      });
    });
    sb();
    STAMP(9)
    wait_vm<0>();
    set_flag(4 + w);
    STAMP(10)
    const int t = ftid(), ch = t & 15, r4 = t >> 4, ct = get_ct();
    const int trow = 64 * rsel + 32 * ((r4 >> 2) & 1) + 8 * (r4 & 1) + 2 * (r4 >> 3) + ((r4 >> 1) & 1);
    const int irow = 64 * rsel + 32 * ((r4 >> 2) & 1) + 4 * (r4 >> 3) + (r4 & 3);
    u32x2 xv[4], dk;
    for (int it = 0; it < 4; ++it) ld64(xv[it], sx, trow * 1024 + ch * 8, c * 131072 + ct * 128 + (4 * (it & 1) + 16 * (it >> 1)) * 1024);
    ld64(dk, mk_srd(dskip, SD * 2), ch * 8, ct * 128);
    wait_vm<0>(); for (int it = 0; it < 4; ++it) pin(xv[it]); pin(dk);
    f32x4 base[4]; unsigned ph[4];
    for (int it = 0; it < 4; ++it) { const f32x2 s2 = *(LAS f32x2*)(size_t)(M_ST + (irow + 8 * (it & 1) + 16 * (it >> 1)) * 8);
      for (int e = 0; e < 4; ++e) { const unsigned xu = xv[it][e >> 1], du = dk[e >> 1];
        const float xe = (e & 1) ? hi2f(xu) : lo2f(xu), de = (e & 1) ? hi2f(du) : lo2f(du);
        base[it][e] = xe + rbf(rbf(__builtin_fmaf(xe, s2[0], s2[1])) * de); }
      const int e = 2 * it + (r4 >> 3), d = (e * 11) >> 5;
      ph[it] = (3 + d) * SLT + (e - 3 * d) * 2048 + (r4 & 7) * 256 + (ch ^ (r4 & 7)) * 16;
      pin(base[it]); pin(ph[it]); }
    float* op = out + (c * 128 + trow) * SD + ct * 64 + ch * 4;
    bar();
    STAMP(11)
    for (int it = 0; it < 4; ++it) {
      f32x4 p[4];
      for (int ww = 0; ww < 4; ++ww) p[ww] = *(LAS f32x4*)(size_t)(ph[it] + ww * 6144);
      *(f32x4*)(op + (4 * (it & 1) + 16 * (it >> 1)) * SD) = base[it] + ((p[0] + p[1]) + (p[2] + p[3]));
    }
    if (g == 0) __builtin_amdgcn_raw_buffer_store_b64((u32x2){(unsigned)tag2, (unsigned)(tag2 >> 32)}, rws, t == 0 ? WS_E2 + c * 128 : OOB, 0, 16);
  } else {
    const int lane = ftid() & 63, l5 = lane & 31, h = lane >> 5, blk = (lane >> 4) & 1, q = (lane & 15) >> 2, pp = lane & 3;
    const unsigned va = lane * 16;
    const unsigned sw0 = ((lane & 7) ^ (lane >> 4)) * 16;
    unsigned vb[2], vw[3], vd[2];
    for (int r = 0; r < 2; ++r) vb[r] = ((lane >> 3) + 7 * r) * 1024 + (sw0 ^ (r * 64));
    const i32x4 sbm = mk_srd((w >> 1) ? Bim : Bre, SP * SD * 2);
    const unsigned so_b = (g * 32 + (w & 1) * 16) * 1024;
    auto ldB = [&](auto S) { constexpr int s = S;
      const unsigned so = so_b + chunk(s) * 128;
      setm0i<((s + 1) % 6) * SLT + OFF_B>(w2); dmao<0>(sbm, vb[0], so); dmao<1024>(sbm, vb[1], so); };
    u32x2 ep2, ep1; unsigned plr, pli, pdt;
    ld64_sc1(ep2, sws, 0, WS_E2 + c * 128);
    ld64_sc1(ep1, sws, 0, WS_E1 + g * 128);
    const int ps = (lane & 3) | ((lane & 4) << 1) | ((lane & 8) >> 1);
    { const unsigned po = (g * 32 + sb_ * 16 + ps) * 2;
      ld16(plr, mk_srd(lam_re, SP * 2), po); ld16(pli, mk_srd(lam_im, SP * 2), po); ld16(pdt, mk_srd(logdt, SP * 2), po); }
    { u64 pc; unsigned len, back;
      asm volatile("s_getpc_b64 %0\n.Lpf_here:\n\ts_mov_b32 %1, .Lpf_end-.Lpf_beg\n\ts_mov_b32 %2, .Lpf_here-.Lpf_beg" : "=s"(pc), "=s"(len), "=s"(back));
      for (unsigned o = (idx * 4 + w) * 128; o + 128 < len; o += 16384) sline((pc - back) & ~127ull, o + 128); }
    const i32x4 sw = mk_srd((w >> 1) ? wu : wg, SD * SF * 2);
    const unsigned so_w = (w & 1) * 32 * (SF * 2) + get_fb() * 32;
    const unsigned ld_w = ((w >> 1) ? OFF_WU : 0) + (w & 1) * 6144;
    auto opW = [&](auto N, auto I) { constexpr int n = N, i = I;
      if constexpr (i % 3 == 0) setm0i<(3 + n % 3) * SLT + i * 1024>(ld_w);
      dmao<(i % 3) * 1024>(sw, vw[i % 3], so_w + chunk((n + 5) & 7) * (64 * SF * 2) + (i / 3) * 16 * (SF * 2)); };
    unsigned ra[4], wa[4];
    for (int s = 0; s < 4; ++s) { ra[s] = pi * 8192 + l5 * 128 + (((2 * s + h) ^ ((l5 >> 1) & 7)) * 16);
      const int row = sb_ * 16 + ps; wa[s] = OFF_B + blk * 4096 + row * 128 + (((2 * s + h) ^ ((row >> 1) & 7)) * 16); }
    const unsigned selv = (((lane & 15) == 0 && blk == 0) || ((lane & 15) == 4 && blk == 1)) ? 0x3f803f80u : 0u;
    const u32x4 sel = {selv, selv, selv, selv};
    for (int s = 0; s < 4; ++s) { pin(ra[s]); pin(wa[s]); }
    unsigned t1l = 0, t1h = 0;
    const int cc = 2 * c + pi;
    float ar, ai, nai, kr, nkr, nki, a8r, a8i, a16r, a16i, a32r, a32i, a64r, a64i, pwr = 1.f, pwi = 0.f, a256r, a256i, mr = 1.f, mi = 0.f;
    u32x4 oh[2];
    float lr, li;
    auto params = [&](auto V) { constexpr int v = V;
      if constexpr (v == 1) { const int q12 = (lane * 43) >> 9, r12 = lane - 12 * q12;
        for (int i = 0; i < 3; ++i) { const int r = r12 + 4 * i, cy = r >= 12, col = r - 12 * cy;
          vw[i] = (gi == 7 && col >= 2) ? OOB : (5 * i + q12 + cy) * (SF * 2) + col * 16 - i * 1024; pin(vw[i]); } }
      if constexpr (v == 2) {
        pin(ep2); pin(ep1); pin(plr); pin(pli); pin(pdt);
        tag2 = utag(ep2);
        const u64 tag1 = utag(ep1); t1l = (unsigned)tag1; t1h = (unsigned)(tag1 >> 32); }
      if constexpr (v == 3) {
        lr = lo2f(plr); li = lo2f(pli);
        const float dt = rbf(__expf(lo2f(pdt)));
        const float er = __expf(dt * lr);
        const float rev = dt * li * 0.15915494309189535f;
        const float fr = rev - __builtin_rintf(rev);
        ar = er * __builtin_amdgcn_cosf(fr); ai = er * __builtin_amdgcn_sinf(fr);
        pin(ar); pin(ai); }
      if constexpr (v == 4) { a8r = ar; a8i = ai;
        for (int i = 0; i < 3; ++i) csq(a8r, a8i, a8r, a8i);
        csq(a16r, a16i, a8r, a8i); pin(a16r); pin(a16i); }
      if constexpr (v == 6) {
        const float nr = ar - 1.f, ni = ai, den = 2.f * __builtin_amdgcn_rcpf(vfma(lr, lr, li * li));
        kr = vfma(nr, lr, ni * li) * den; nki = vfma(nr, li, -(ni * lr)) * den; nkr = -kr; nai = -ai;
        pin(nai); pin(kr); pin(nkr); pin(nki); }
      if constexpr (v == 10) { csq(a32r, a32i, a16r, a16i); csq(a64r, a64i, a32r, a32i); if (blk) { pwr = a16r; pwi = a16i; mr = a64r; mi = a64i; } }
      if constexpr (v == 11) { float tr = pwr, ti = pwi; cf4(tr, ti, a32r, a32i, 0.f, 0.f); if (h) { pwr = tr; pwi = ti; } csq(a32r, a32i, a64r, a64i); }
      if constexpr (v == 12) { float ur = mr, ui = mi; cf4(ur, ui, a32r, a32i, 0.f, 0.f); if (h) { mr = ur; mi = ui; } csq(a256r, a256i, a32r, a32i); }
      if constexpr (v == 13 || v == 14) { constexpr int u = v - 13;
        const unsigned val = (((lane >> 2) & 1) == h && blk == u) ? ((lane & 1) ? 0x3f800000u : 0x3f80u) : 0u; const int d0 = ((lane >> 1) & 1) | ((lane >> 2) & 2);
        for (int d = 0; d < 4; ++d) oh[u][d] = d0 == d ? val : 0u;
        pin(oh[u]); }
      sb();
    };
    f32x16 sa[2] = {};
    f32x4 csa = {};
    { u32x4 af[2][4][2], wf[2][4];
      auto rdA = [&](int p, int s, unsigned slot) { wf[p][s] = lds_r128(slot + wa[s]); af[p][s][0] = lds_r128(slot + ra[s]); af[p][s][1] = lds_r128(slot + ra[s] + 4096); };
      auto mmA = [&](int p, int s) { sa[0] = mfma(af[p][s][0], wf[p][s], sa[0]); sa[1] = mfma(af[p][s][1], wf[p][s], sa[1]); pin(sa[0]); pin(sa[1]); };
      auto csA = [&](int p) { for (int s = 0; s < 4; ++s) csa = mfma16(sel, wf[p][s], csa); pin(csa); sb(); };
      auto W3 = [&](auto N, auto H_) { sfor<0, 3>([&](auto I) { opW(N, IC(3 * H_ + I)); }); };
      unsigned rh[4], wh[4];
      for (int s = 0; s < 4; ++s) { rh[s] = ra[s] + 3 * SLT; wh[s] = wa[s] + 3 * SLT - 8192; pin(rh[s]); pin(wh[s]); wh[s] += 8192; wa[s] -= 8192; pin(wa[s]); wa[s] += 8192; }
      ldB(IC(2));
      __builtin_amdgcn_s_setprio(HPA);
      wait_vm<10>(); STA(46) bar(); STA(47) for (int s = 0; s < 4; ++s) rdA(0, s, SLT); sb(); ldB(IC(3));
      sfor<1, 8>([&](auto V) { constexpr int v = V, p = v & 1, sl = (v + 1) % 6;
        wait_vm<(v == 1 ? 9 : WEARLY ? (v < 4 ? 4 : v == 4 ? 7 : v == 5 ? 10 : v == 6 ? 11 : 6) : (v < 5 ? 4 : v == 5 ? 7 : v == 6 ? 11 : 6))>(); wait_lgkm0();
        if constexpr (v == 7) __builtin_amdgcn_s_setprio(3);
        STA(46 + 2 * v) bar(); STA(47 + 2 * v)
        sfor<0, 8>([&](auto I) { constexpr int i = I;
          sa[i & 1] = mfma(af[p ^ 1][i >> 1][i & 1], wf[p ^ 1][i >> 1], sa[i & 1]); pin(sa[i & 1]);
          sb();
          if constexpr (i < 6) sfor<2 * i, 2 * i + 2>([&](auto R) { constexpr int r = R, s = r / 3, k = r % 3, o = (sl % 3) * SLT;
            if constexpr (k == 0) wf[p][s] = lds_r128((sl < 3 ? wa[s] : wh[s]) + o); else af[p][s][k - 1] = lds_r128((sl < 3 ? ra[s] : rh[s]) + o + (k - 1) * 4096); });
          if constexpr (i == 6 && v < 5) ldB(IC(v + 3));
          if constexpr (i == 7 && v == 4) W3(IC(0), IC(0));
          if constexpr (i == 6 && v == 5) W3(IC(0), IC(1));
          if constexpr (i == 7 && v == 5) W3(IC(1), IC(0));
          if constexpr (i == 6 && v == 6) W3(IC(1), IC(1));
          sb(); });
        csA(p ^ 1);
        params(V);
      });
      STAMP(2)
      for (int s = 0; s < 4; ++s) mmA(1, s);
      csA(1);
      __builtin_amdgcn_s_setprio(HPB);
    }
    const int kb = 22 * w - (w == 3), cnt = w < 2 ? 22 : 21, ct = get_ct();
    const i32x4 swd = mk_srd(wd + kb * 8192, cnt * 16384);
    const unsigned so_c = ct * 65536 + w * 256;
    const unsigned r2 = (xcd * 11) >> 2;
    auto kofs = [&](int p, unsigned sc_) { const unsigned r = lnd(r2 * sc_), a = r + p * sc_, b2 = r + (p - 22) * sc_; return a < b2 ? a : b2; };
    for (int r = 0; r < 2; ++r) vd[r] = ((lane >> 3) + 7 * r) * 1024 + (((lane & 7) ^ (((lane >> 4) & 1) << 2)) * 16) + ct * 128;
    unsigned so_t = 0; i32x4 sc = swd;
    auto wd_op = [&](auto M, auto E, auto R) { constexpr int m = M, e = E, r = R;
      if constexpr (r == 0) { so_t = kofs(m < 18 ? (m + 14) % 18 : m, 16384); setm0i<cell_c(e)>(cell_k(e) == 0 ? w6 : cell_k(e) == 1 ? w4 : w2); }
      dmao<r * 1024>(swd, vd[r], so_t);
    };
    auto dma_wd = [&](auto M, auto E) { wd_op(M, E, IC(0)); wd_op(M, E, IC(1)); };
    auto c_op = [&](auto N, auto I) { constexpr int n = N, i = I;
      if constexpr (i == 0) { const int kg = ((n >> 1) + lnd(xcd)) & 3; so_t = so_c + (n & 1) * 32768 + (kg & 1) * 128; sc = mk_srd((kg >> 1) ? Cim : Cre, SD * SP * 2); }
      if constexpr (!(i & 1)) setm0i<(n & 1) ? ((i >> 1) ? M_T : cell_c(15)) : cell_c(16 + (i >> 1))>(w2);
      dmao<(i & 1) * 1024>(sc, vb[i & 1], so_t + (i >> 1) * 16384);
    };
    unsigned rcx[4];
    for (int s = 0; s < 4; ++s) rcx[s] = OFF_B + w * 2048 + blk * SLT + (lane & 15) * 128 + (((2 * s + h) ^ ((l5 >> 1) & 7)) * 16);
    const unsigned rcd = 2 * SLT + blk * (M_T - cell_c(15) - SLT);
    auto rd_c = [&](int n, u32x4* d) { for (int s = 0; s < 4; ++s) d[s] = lds_r128(rcx[s] + ((n & 1) ? rcd : 0u)); };
    const unsigned granb = WS_GRAN + (g * 2 + sb_) * 8192;
    const i32x4 sgr = mk_srd(ws + granb, 8192);
    const unsigned vg = (32 - cc) * 256 + va;
    float R[16], I_[16], csr, csi, er[2], ei[2], sr[4], si[4], prer, prei, cr, ci;
    f32x4 tb[8];
    u32x4 ge[8], fr4[4];
    f32x16 td[2];
    unsigned hp[2][8];
    unsigned nbad = 0, fo_g = 0, pa[2], tdl = 0;
    auto side = [&](auto P_) { constexpr int P = P_;
      if constexpr (P == 0) { for (int k = 0; k < 8; ++k) tb[k] = *(LAS f32x4*)(size_t)(M_ST + pi * 512 + (lane >> 4) * 128 + k * 16);
        csr = csa[0]; float t = csr; swap32(csr, t); csi = csr; swap16(csr, csi); }
      if constexpr (P >= 1 && P < 9) { constexpr int k = P - 1;
        for (int r = 0; r < 2; ++r) { R[2 * k + r] = sa[0][2 * k + r]; I_[2 * k + r] = sa[1][2 * k + r]; swap16(R[2 * k + r], I_[2 * k + r]); }
        for (int r = 0; r < 2; ++r) sc4(R[2 * k + r], I_[2 * k + r], tb[k][2 * r], tb[k][2 * r + 1], csr, csi); }
      if constexpr (P == 9) { for (int r = 0; r < 2; ++r) { er[r] = R[r]; ei[r] = I_[r]; cf4(er[r], ei[r], ar, ai, R[2 + r], I_[2 + r]); } }
      if constexpr (P >= 16 && P < 22) { constexpr int k = P - 14; for (int r = 0; r < 2; ++r) cf4(er[r], ei[r], ar, ai, R[2 * k + r], I_[2 * k + r]); }
      if constexpr (P == 22) { sr[0] = er[0]; si[0] = ei[0]; cf4(sr[0], si[0], a8r, a8i, er[1], ei[1]); sr[1] = sr[0]; swap16(sr[0], sr[1]); si[1] = si[0]; swap16(si[0], si[1]); }
      if constexpr (P == 23) { sr[2] = sr[0]; swap32(sr[0], sr[2]); si[2] = si[0]; swap32(si[0], si[2]); sr[3] = sr[1]; swap32(sr[1], sr[3]); si[3] = si[1]; swap32(si[1], si[3]); }
      if constexpr (P == 24) { cr = sr[0]; ci = si[0]; cf4(cr, ci, a16r, a16i, sr[1], si[1]); sr[1] = cr; si[1] = ci; cf4(cr, ci, a16r, a16i, sr[2], si[2]); sr[2] = cr; si[2] = ci; }
      if constexpr (P == PPUB) { cf4(cr, ci, a16r, a16i, sr[3], si[3]);
        __builtin_amdgcn_raw_buffer_store_b128((u32x4){__float_as_uint(cr), t1l, __float_as_uint(ci), t1h}, rws, lane < 16 ? granb + (31 - cc) * 256 + va : OOB, 0, 16); }
      if constexpr (P == 32) { prer = h ? (blk ? sr[2] : sr[1]) : (blk ? sr[0] : 0.f); prei = h ? (blk ? si[2] : si[1]) : (blk ? si[0] : 0.f); }
      sfor<0, 16>([&](auto K) { constexpr int k = K; if constexpr (P == nxt(33, k < 4 ? k / 2 : k - 2)) kc4(R[k], I_[k], kr, nkr, nki); });
      if constexpr (P >= 53 && P < 58) params(IC(P - 43));
      if constexpr (P == PG) sfor<0, 8>([&](auto K) { constexpr int k = K; ld128_sc1<(k & 3) * 1024>(ge[k], sgr, vg, (k >> 2) * 4096); });
      if constexpr (P == PC) { STB(23) wait_vm<hb_cnt(PC, false) - hb_cnt(PG, true)>(); for (int i = 0; i < 8; ++i) pin(ge[i]); STB(24) }
      if constexpr (P == PC + 3) STB(25) if constexpr (P == PC + 6) STB(26) if constexpr (P == PC + 9) STB(27)
      if constexpr (P == PC || P == PC + 1) { constexpr int i = 4 * (P - PC); miss2(nbad, ge[i], ge[i + 1], t1l, t1h); miss2(nbad, ge[i + 2], ge[i + 3], t1l, t1h); }
      if constexpr (P == PC + 2) { const unsigned nout = 8 - ((cc + 3 - (lane >> 4)) >> 2);
#pragma unroll 1
        for (unsigned spins = 0; spins < SPIN_MAX; ++spins) {
          if (__all(nbad == nout)) break;
          STAMP(28)
          __builtin_amdgcn_s_sleep(SLP);
          sfor<0, 8>([&](auto I) { constexpr int i = I; ld128_sc1_rw<(i & 3) * 1024>(ge[i], sgr, vg, (i >> 2) * 4096); });
          wait_vm<0>(); for (int i = 0; i < 8; ++i) pin(ge[i]);
          nbad = 0;
          for (int i = 0; i < 8; i += 2) miss2(nbad, ge[i], ge[i + 1], t1l, t1h);
        }
        cr = __uint_as_float(ge[7][0]); ci = __uint_as_float(ge[7][2]); cf4(cr, ci, a256r, a256i, __uint_as_float(ge[6][0]), __uint_as_float(ge[6][2])); }
      if constexpr (P >= PC + 3 && P < PC + 6) { constexpr int i = 5 - 2 * (P - PC - 3);
        cf4(cr, ci, a256r, a256i, __uint_as_float(ge[i][0]), __uint_as_float(ge[i][2])); cf4(cr, ci, a256r, a256i, __uint_as_float(ge[i - 1][0]), __uint_as_float(ge[i - 1][2])); }
      if constexpr (P == PC + 6) { cf4(cr, ci, mr, mi, 0.f, 0.f); float t = cr, u = ci; swap16(cr, t); swap16(ci, u); cr = vadd(cr, t); ci = vadd(ci, u); }
      if constexpr (P == PC + 7) { float t = cr, u = ci; swap32(cr, t); swap32(ci, u); cr = vadd(cr, t); ci = vadd(ci, u); cf4(cr, ci, pwr, pwi, prer, prei); }
      if constexpr (P == PC + 8) { sr[0] = cr; si[0] = ci; cf4(cr, ci, a8r, a8i, er[0], ei[0]); er[0] = sr[0]; ei[0] = si[0]; er[1] = cr; ei[1] = ci; }
      if constexpr (P == PC + 9) { kc4(er[0], ei[0], kr, nkr, nki); kc4(er[1], ei[1], kr, nkr, nki); }
      if constexpr (P % 16 == 4 && P / 16 >= PFW_S && P / 16 < 6) sfor<0, PFW>([&](auto U) { constexpr int k = (P / 16 - PFW_S) * PFW + U + PFW_O, m = k < 4 ? 18 + k : k < 18 ? k : k - 18;
        if constexpr (k < 22) { const unsigned o = kofs(m < 18 ? (m + 14) % 18 : m, 16384); if (o < (unsigned)cnt * 16384) for (int r = 0; r < 4; ++r) sline((u64)(wd + kb * 8192), o + ct * 128 + (4 * (c & 3) + r) * 1024); } });
      sfor<0, 8>([&](auto K) { constexpr int k = K; if constexpr (P == nxt(P2, k)) {
        for (int r = 0; r < 2; ++r) cf4c(k ? R[2 * k - 2 + r] : er[r], k ? I_[2 * k - 2 + r] : ei[r], ar, ai, nai, R[2 * k + r], I_[2 * k + r]);
        hp[0][k] = pk2(R[2 * k], R[2 * k + 1]); hp[1][k] = pk2(I_[2 * k], I_[2 * k + 1]); } });
      static_assert(PT > nxt(P2, 7) && PS > PT && PF > PS && PC % 16 == 0, "scan schedule");
      if constexpr (P == PT) {
        __builtin_amdgcn_s_setprio(3);
        for (int part = 0; part < 2; ++part) {
          td[part] = mfma((u32x4){hp[part][0], hp[part][1], hp[part][2], hp[part][3]}, oh[0], (f32x16){});
          td[part] = mfma((u32x4){hp[part][4], hp[part][5], hp[part][6], hp[part][7]}, oh[1], td[part]); pin(td[part]); }
        __builtin_amdgcn_s_setprio(HPB); }
      if constexpr (P == PS) { for (int k = 0; k < 4; ++k) for (int d = 0; d < 4; ++d) fr4[k][d] = pk2(td[k >> 1][8 * (k & 1) + 2 * d], td[k >> 1][8 * (k & 1) + 2 * d + 1]); }
      if constexpr (P == PS) { for (int k = 0; k < 4; ++k)
          __builtin_amdgcn_raw_buffer_store_b128(fr4[k], rws, a2pb + (2 * g + sb_) * 1024 + va + ((k & 1) * NKS + (k >> 1) * 32) * 1024, 0, 16); }
      if constexpr (P == 100) { const int ks = kb + lane, p6 = (ks * 43) >> 8, pg = ks < 84 ? p6 + (p6 >= 7) : 7 + 8 * (ks - 84), pb = ks < 84 ? ks - 6 * p6 : 0;
        fo_g = lane < cnt ? (pg * 8 + 4 + 2 * rsel + (pb >= 3)) * 128 : OOB; pin(fo_g); }
      if constexpr (P == 104) { for (int rb = 0; rb < 2; ++rb) { const int e = 4 * rb + (l5 >> 3), d = (e * 11) >> 5;
          pa[rb] = (3 + d) * SLT + (e - 3 * d) * 2048 + w6 + (lane & 7) * 256 + ((h ^ (lane & 7)) * 16); pin(pa[rb]); } }
      if constexpr (P == 106) { tdl = (8 * h + q) * 128 + (((blk * 2 + (pp >> 1)) ^ ((q >> 1) << 2)) * 16) + (pp & 1) * 8; pin(tdl); }
      if constexpr (P == PF) { wait_vm<hb_cnt(PF, false) - hb_cnt(PS, true)>(); set_flag(w);
        if (cc == 31) __builtin_amdgcn_raw_buffer_store_b64((u32x2){t1l, t1h}, rws, (lane == 0 && sb_ == 0) ? WS_E1 + g * 128 : OOB, 0, 16); }
    };
    u32x4 cq[8][4];
    u32x2 fs;
    const unsigned fo_s = lane < 8 ? ((4 * w + (lane >> 1)) * 8 + 2 * rsel + (lane & 1)) * 128 : OOB;
    sfor<0, 8>([&](auto N) { constexpr int n = N, g0 = hb_g0(n), done = n ? hb_cnt(16 * n - 1, true) : 0;
      if constexpr (n > 0) { if constexpr (n == 1) wait_vm<done>(); STB(30 + 2 * n) bar(); STB(31 + 2 * n) } else STB(31)
      if constexpr (n > 1) { wait_vm<done - hb_cnt(16 * (n > 1 ? n - 2 : 0) + hb_g0(n - 2) + 3, false)>(); rd_c(n, cq[n - 2]); sb(); }
      sfor<0, hb_ops(n)>([&](auto I) { constexpr int i = I, io = i < g0 ? i : i - 4;
        if constexpr (i >= g0 && i < g0 + 4) { if constexpr (i == g0 && n > 1) { for (int s = 0; s < 4; ++s) use(cq[n - 2][s]); } c_op(N, IC(i - g0)); }
        else if constexpr (n >= 6) { constexpr int e = 5 * (n - 6) + (io >> 1); wd_op(IC(e < 4 ? 18 + e : e), IC(e), IC(io & 1)); }
        if constexpr (16 * n + i == PPOLL) ld64_sc1(fs, sws, fo_s, flagb);
        side(IC(16 * n + i));
        sb();
      });
    });
    __builtin_amdgcn_s_setprio(HPC);
    STAMP(3)
    auto poll = [&](unsigned fo, u32x2 fv) {
#pragma unroll 1
      for (unsigned spins = 0; spins < SPIN_MAX; ++spins) {
        const bool ok = fo == OOB || (fv[0] == (unsigned)tag2 && fv[1] == (unsigned)(tag2 >> 32));
        if (__all(ok)) break;
        __builtin_amdgcn_s_sleep(SLP);
        ld64_sc1(fv, sws, fo, flagb); wait_vm<0>(); pin(fv);
      }
    };
    wait_vm<hb_cnt(127, true) - hb_cnt(16 * 6 + 7, false)>();
    rd_c(0, cq[6]);
    const unsigned a2c = WS_A2 + (unsigned)(c * 4 + 2 * rsel) * (NKS * 1024);
    const i32x4 sg0 = mk_srd(ws + a2c + (64 + kb) * 1024, cnt * 1024), sg1 = mk_srd(ws + a2c + (NKS + 64 + kb) * 1024, cnt * 1024);
    const unsigned vs0 = va + a2c + w * 8192, vs1 = vs0 + NKS * 1024;
    wait_lgkm0();
    dma_wd(IC(16), IC(16)); dma_wd(IC(17), IC(17));
    wait_cnt();
    ld64_sc1(fs, sws, fo_s, flagb);
    sfor<10, 13>([&](auto E) { dma_wd(E, E); });
    wait_vm<6>(); pin(fs);
    STAMP(7)
    poll(fo_s, fs);
    STAMP(4)
    unsigned tb2[4][2];
    auto rd_half = [&](int e, int cb) { const int k = cell_k(e) == 0 ? (cell_c(e) >= 5 * SLT) : cell_k(e) + 1;
      return lds_tr2(tb2[k][cb] + (cell_c(e) - (k == 0 ? 3 * SLT : k == 1 ? 5 * SLT : k == 3 ? OFF_B : 0)), 512); };
    f32x16 acc2[2][2];
    u32x4 L[76], wq[4][2], bf[2][2];
    unsigned so_l = 0;
    auto soL = [&](auto M) { constexpr int m = M;
      if constexpr (m < 16) { if constexpr (!(m & 3)) { const int kg = ((m >> 2) + lnd(xcd)) & 3; so_l = ((kg >> 1) * 32 + (kg & 1) * 4) * 1024; } }
      else so_l = kofs(m - 16, 1024); };
    auto ldH = [&](auto M, auto R) { constexpr int m = M, r = R;
      if constexpr (m < 16) ld128_sc1<(m & 3) * 1024>(L[2 * m + r], sws, r ? vs1 : vs0, so_l); else ld128_sc1<0>(L[2 * m + r], r ? sg1 : sg0, va, so_l); };
    auto ldL = [&](auto M) { soL(M); ldH(M, IC(0)); ldH(M, IC(1)); };
    sfor<0, p2_l0(0)>(ldL);
    sfor<0, 38>([&](auto M) { constexpr int m = M, mm = m - 16;
      constexpr bool one = p2_l0(m + 1) == p2_l0(m) + 1;
      wait_vm<p2_after(m)>(); pin(L[2 * m]); pin(L[2 * m + 1]);
      sfor<0, 4>([&](auto G) { constexpr int gp = G, cb = gp >> 1, rb = gp & 1;
        if constexpr (m < 16) acc2[cb][rb] = mfma(cq[2 * (m >> 2) + cb][m & 3], L[2 * m + rb], m == 0 ? (f32x16){} : acc2[cb][rb]);
        else acc2[cb][rb] = mfma(mm < 18 ? bf[mm & 1][cb] : wq[mm < 18 ? 0 : mm - 18][cb], L[2 * m + rb], acc2[cb][rb]);
        sb();
        if constexpr (gp < 2) {
          if constexpr (m >= 15 && mm + 1 < 18) bf[(mm + 1) & 1][gp] = rd_half((mm + 5) % 18, gp);
          if constexpr (m == 0 || m == 1) wd_op(IC(13 + m), IC(13 + m), G);
          if constexpr (m == 2 || m == 3) { constexpr int k = 2 * (m - 2) + gp;
            for (int u = 0; u < 2; ++u) { tb2[k][u] = (tdl ^ (u * 64)) + (k == 0 ? 3 * SLT + w6 : k == 1 ? 5 * SLT + w6 : k == 2 ? w4 : OFF_B + w2); pin(tb2[k][u]); } }
          if constexpr (m == 4 && gp == 0) rd_c(1, cq[7]);
          if constexpr (m == 5) { if constexpr (gp == 0) { for (int s = 0; s < 4; ++s) use(cq[7][s]); } wd_op(IC(15), IC(15), G); }
          if constexpr (m >= 7 && m <= 10) { if constexpr (gp == 0) { use(wq[m - 7][0]); use(wq[m - 7][1]); } wd_op(IC(m - 7), IC(m - 7), G); }
          if constexpr (m >= 6 && m <= 9) wq[m - 6][gp] = rd_half(m - 6, gp);
          if constexpr (gp == 1 && m == PCK - 1) ld64_sc1(fs, sws, fo_g, flagb);
          if constexpr (gp == 1 && one) soL(IC(p2_l0(m)));
        } else {
          if constexpr (gp == 2 && m == PCK) { STAMP(5) wait_vm<p2_extra(PCK) + 2 * (16 - p2_l0(PCK - 1))>(); pin(fs); poll(fo_g, fs); STAMP(6) }
          if constexpr (one) ldH(IC(p2_l0(m)), IC(gp - 2));
          else if constexpr (gp == 2) sfor<p2_l0(m), p2_l0(m + 1)>(ldL);
        }
        sb();
      });
    });
    STAMP(9)
    for (int rb = 0; rb < 2; ++rb) for (int rq = 0; rq < 4; ++rq) for (int cb = 0; cb < 2; ++cb)
      *(LAS f32x4*)(size_t)((pa[rb] ^ (rq * 32)) + cb * 128) =
          (f32x4){acc2[cb][rb][4 * rq], acc2[cb][rb][4 * rq + 1], acc2[cb][rb][4 * rq + 2], acc2[cb][rb][4 * rq + 3]};
    wait_lgkm0(); bar();
    STAMP(11)
  }
  STAMP(12)
  STAMP_OUT
  asm volatile(".Lpf_end:");
}

extern "C" void kernel_launch(void* const* d_in, const int* in_sizes, int n_in, void* d_out, int out_size, void* d_ws, size_t ws_size,
                              hipStream_t stream) {
  (void)hipFuncSetAttribute((const void*)fused, hipFuncAttributeMaxDynamicSharedMemorySize, LDS_BYTES);
  fused<<<256, 512, LDS_BYTES, stream>>>((const u16*)d_in[0], (const u16*)d_in[1], (const u16*)d_in[2], (const u16*)d_in[3], (const u16*)d_in[4],
                                         (const u16*)d_in[5], (const u16*)d_in[6], (const u16*)d_in[7], (const u16*)d_in[8], (const u16*)d_in[10],
                                         (const u16*)d_in[11], (const u16*)d_in[12], (char*)d_ws, (float*)d_out);
}
```

```cpp
#define DIAG 0
#include <hip/hip_runtime.h>
#include <hip/hip_bf16.h>
#include <type_traits>

typedef unsigned short u16;
typedef unsigned long long u64;
typedef short s16x4 __attribute__((ext_vector_type(4)));
typedef __bf16 bf16x2 __attribute__((ext_vector_type(2)));
typedef __bf16 bf16x8 __attribute__((ext_vector_type(8)));
typedef float f32x2 __attribute__((ext_vector_type(2)));
typedef float f32x4 __attribute__((ext_vector_type(4)));
typedef float f32x16 __attribute__((ext_vector_type(16)));
typedef unsigned u32x2 __attribute__((ext_vector_type(2)));
typedef unsigned u32x4 __attribute__((ext_vector_type(4)));
typedef int i32x4 __attribute__((ext_vector_type(4)));
#define LAS __attribute__((address_space(3)))
#define DEVI __device__ __forceinline__

constexpr int SL = 2048, SD = 512, SP = 512, SF = 1376;
constexpr int NKS = 150;
constexpr unsigned WS_E1 = 0;
constexpr unsigned WS_E2 = 2048;
constexpr unsigned WS_GRAN = 262144;
constexpr unsigned WS_FLAG = 524288;
constexpr unsigned WS_A2 = 786432;
constexpr unsigned A2_BYTES = 64u * NKS * 1024u;
constexpr unsigned WS_BYTES = WS_A2 + A2_BYTES;
constexpr unsigned OOB = 0x40000000u;
constexpr int SLT = 24832;
constexpr int OFF_B = 16384, OFF_WU = 12288 + 32;
constexpr int MISC = 6 * SLT;
constexpr int M_ST = MISC, M_SD = MISC + 1024, M_CNT = MISC + 2048, M_CS = MISC + 2304, M_T = MISC + 3840;
constexpr int LDS_BYTES = 163840;
constexpr unsigned SPIN_MAX = 1u << 16;
constexpr int cell_k(int e) { return e >= 15 ? 2 : (e % 5) < 3 ? 0 : 1; }
constexpr int cell_c(int e) { return e >= 15 ? ((e - 13) % 3) * SLT + OFF_B : (e % 5) < 3 ? (3 + (e / 5 + 2) % 3) * SLT + (e % 5) * 2048 : ((e / 5 + 2) % 3) * SLT + (e % 5 - 3) * 2048; }

#define STAMP_DECL
#define STAMP(i) {}
#define STAMP_OUT
#define STB(i) {}
template <int B, int E, class Fn> DEVI void sfor(Fn&& f) {
  if constexpr (B < E) { f(std::integral_constant<int, B>{}); sfor<B + 1, E>(f); }
}
DEVI i32x4 mk_srd(const void* p, unsigned bytes) {
  u64 a = (u64)p; return (i32x4){(int)(unsigned)a, (int)((unsigned)(a >> 32) & 0xffffu), (int)bytes, 0x00020000};
}
DEVI void dma(i32x4 srd, unsigned voff, unsigned soff, unsigned lds) {
  asm volatile("s_mov_b32 m0, %0\n\ts_nop 0\n\tbuffer_load_dwordx4 %1, %2, %3 offen lds" ::"s"(lds), "v"(voff), "s"(srd), "s"(soff) : "memory");
}
template <int OFF> DEVI void ld128_sc1(u32x4& d, i32x4 srd, unsigned voff, unsigned soff) {
  asm volatile("buffer_load_dwordx4 %0, %1, %2, %3 offen offset:%4 sc1" : "=v"(d) : "v"(voff), "s"(srd), "s"(soff), "n"(OFF) : "memory");
}
template <int OFF> DEVI void ld128_sc1_rw(u32x4& d, i32x4 srd, unsigned voff, unsigned soff) {
  asm volatile("buffer_load_dwordx4 %0, %1, %2, %3 offen offset:%4 sc1" : "+v"(d) : "v"(voff), "s"(srd), "s"(soff), "n"(OFF) : "memory");
}
DEVI void ld64_sc1(u32x2& d, i32x4 srd, unsigned voff, unsigned soff) {
  asm volatile("buffer_load_dwordx2 %0, %1, %2, %3 offen sc1" : "=v"(d) : "v"(voff), "s"(srd), "s"(soff) : "memory");
}
DEVI void ld64(u32x2& d, i32x4 srd, unsigned voff, unsigned soff) {
  asm volatile("buffer_load_dwordx2 %0, %1, %2, %3 offen" : "=v"(d) : "v"(voff), "s"(srd), "s"(soff) : "memory");
}
DEVI void ld16(unsigned& d, i32x4 srd, unsigned voff) {
  asm volatile("buffer_load_ushort %0, %1, %2, 0 offen" : "=v"(d) : "v"(voff), "s"(srd) : "memory");
}
template <int N> DEVI void wait_vm() { asm volatile("s_waitcnt vmcnt(%0)" ::"n"(N) : "memory"); }
template <class T> DEVI void pin(T& a) { asm volatile("" : "+v"(a)::"memory"); }
template <class T> DEVI void use(const T& a) { asm volatile("" ::"v"(a) : "memory"); }
DEVI int lnd(int a) { asm volatile("" : "+s"(a)); return a; }
DEVI int ftid() { int t = threadIdx.x; asm volatile("" : "+v"(t)); return t; }
DEVI void wait_lgkm0() { asm volatile("s_waitcnt lgkmcnt(0)" ::: "memory"); }
DEVI void bar() { asm volatile("s_barrier" ::: "memory"); }
DEVI void sb() { __builtin_amdgcn_sched_barrier(0); }
DEVI float lo2f(unsigned u) { return __uint_as_float(u << 16); }
DEVI float hi2f(unsigned u) { return __uint_as_float(u & 0xffff0000u); }
DEVI unsigned pk2(float a, float b) { f32x2 v = {a, b}; return __builtin_bit_cast(unsigned, __builtin_convertvector(v, bf16x2)); }
DEVI float rbf(float a) { return lo2f(pk2(a, a)); }
DEVI u32x4 lds_r128(unsigned a) { return *(LAS u32x4*)(size_t)a; }
DEVI u32x2 lds_tr(unsigned a) { return __builtin_bit_cast(u32x2, __builtin_amdgcn_ds_read_tr16_b64_v4i16((LAS s16x4*)(size_t)a)); }
DEVI u32x4 lds_tr2(unsigned a, unsigned d) { u32x2 x = lds_tr(a), y = lds_tr(a + d); return (u32x4){x[0], x[1], y[0], y[1]}; }
DEVI f32x16 mfma(u32x4 a, u32x4 b, f32x16 c) {
  return __builtin_amdgcn_mfma_f32_32x32x16_bf16(__builtin_bit_cast(bf16x8, a), __builtin_bit_cast(bf16x8, b), c, 0, 0, 0);
}
DEVI float dot2(unsigned a, unsigned b, float c) {
  return __builtin_amdgcn_fdot2_f32_bf16(__builtin_bit_cast(bf16x2, a), __builtin_bit_cast(bf16x2, b), c, false);
}
DEVI void swap16(float& a, float& b) { auto r = __builtin_amdgcn_permlane16_swap(__float_as_uint(a), __float_as_uint(b), false, false); a = __uint_as_float(r[0]); b = __uint_as_float(r[1]); }
DEVI void swap32(float& a, float& b) { auto r = __builtin_amdgcn_permlane32_swap(__float_as_uint(a), __float_as_uint(b), false, false); a = __uint_as_float(r[0]); b = __uint_as_float(r[1]); }
DEVI float sum32(float v) { float a = v, b = v; swap32(a, b); return a + b; }
DEVI float sum16(float v) { float a = v, b = v; swap16(a, b); return a + b; }
DEVI void cmul(float& dr, float& di, float ar, float ai, float br, float bi) { float t = ar * br - ai * bi; di = ar * bi + ai * br; dr = t; }
DEVI void cfma(float& hr, float& hi, float ar, float ai, float br, float bi) {
  float t = __builtin_fmaf(ar, hr, __builtin_fmaf(-ai, hi, br)); hi = __builtin_fmaf(ar, hi, __builtin_fmaf(ai, hr, bi)); hr = t;
}

template <int OFF> DEVI void dmao(i32x4 srd, unsigned voff, unsigned soff) {
  asm volatile("buffer_load_dwordx4 %0, %1, %2 offen offset:%3 lds" ::"v"(voff), "s"(srd), "s"(soff), "n"(OFF) : "memory");
}
DEVI void setm0(unsigned lds) { asm volatile("s_mov_b32 m0, %0\n\ts_nop 0" ::"s"(lds) : "memory"); }
DEVI void touch(i32x4 srd, unsigned voff, unsigned soff) {
  asm volatile("buffer_load_dword %0, %1, %2 offen lds" ::"v"(voff), "s"(srd), "s"(soff) : "memory");
}
DEVI f32x4 mfma16(u32x4 a, u32x4 b, f32x4 c) {
  return __builtin_amdgcn_mfma_f32_16x16x32_bf16(__builtin_bit_cast(bf16x8, a), __builtin_bit_cast(bf16x8, b), c, 0, 0, 0);
}
DEVI float vadd(float a, float b) { float r; asm("v_add_f32 %0, %1, %2" : "=v"(r) : "v"(a), "v"(b)); return r; }
DEVI float vfma(float a, float b, float c) { float r; asm("v_fma_f32 %0, %1, %2, %3" : "=v"(r) : "v"(a), "v"(b), "v"(c)); return r; }
DEVI f32x2 pfma(f32x2 a, f32x2 b, f32x2 c) { return __builtin_elementwise_fma(a, b, c); }
DEVI f32x2 sp(float a) { return (f32x2){a, a}; }
DEVI void cfma2(f32x2& hr, f32x2& hi, f32x2 a, f32x2 br, f32x2 bi) {
  f32x2 t1, t2;
  asm("v_pk_fma_f32 %2, %4, %1, %5 op_sel:[1,0,0] op_sel_hi:[1,1,1] neg_lo:[1,0,0] neg_hi:[1,0,0]\n\t"
      "v_pk_fma_f32 %3, %4, %0, %6 op_sel:[1,0,0] op_sel_hi:[1,1,1]\n\t"
      "v_pk_fma_f32 %0, %4, %0, %2 op_sel:[0,0,0] op_sel_hi:[0,1,1]\n\t"
      "v_pk_fma_f32 %1, %4, %1, %3 op_sel:[0,0,0] op_sel_hi:[0,1,1]"
      : "+v"(hr), "+v"(hi), "=&v"(t1), "=&v"(t2) : "v"(a), "v"(br), "v"(bi));
}
DEVI void cout2(unsigned& o0, unsigned& o1, f32x2 k, f32x2 gr, f32x2 gi) {
  f32x2 t1, t2;
  asm("v_pk_mul_f32 %0, %2, %4 op_sel:[1,0] op_sel_hi:[1,1]\n\t"
      "v_pk_mul_f32 %1, %2, %3 op_sel:[1,0] op_sel_hi:[1,1]\n\t"
      "v_pk_fma_f32 %0, %2, %3, %0 op_sel:[0,0,0] op_sel_hi:[0,1,1] neg_lo:[0,0,1] neg_hi:[0,0,1]\n\t"
      "v_pk_fma_f32 %1, %2, %4, %1 op_sel:[0,0,0] op_sel_hi:[0,1,1] neg_lo:[1,0,1] neg_hi:[1,0,1]"
      : "=&v"(t1), "=&v"(t2) : "v"(k), "v"(gr), "v"(gi));
  o0 = pk2(t1[0], t1[1]); o1 = pk2(t2[0], t2[1]);
}
#define SLP 4
#define WN 10
#define PGV 50
#define PCV 64
#define P2D 16
#define PTV 88
#define PSV 89
#define PFV 114
#define PFW 1
#define PFW_S 2
#define PFW_O 0
#define PFX 0
#define ROTX (2 * (xcd >> 1) + 4 * (xcd & 1))
#define PLATE 0
#define AVAR 0
#define STA(i) {}
#define HPA 0
#define WEARLY 0
#define PFW0 0
#define HPB 0
#define HPC 0
#define PFG 0
#define PFG_O 0
#define PCK 13
#define WG 6
#define EPK 0
#define CPE 0
#define PFT 1
#define LEADX (c + g)
#define LEAD 1
#define IC(x) std::integral_constant<int, (x)>{}
constexpr int p2_extra(int m) { return ((m == 0 || m == 1 || m == 5 || (m >= 7 && m <= 10)) ? 2 : 0) + (m == PCK - 1 ? 1 : 0); }
constexpr int p2_l0(int m) { return m <= PCK ? (m + WN / 2 < 16 ? m + WN / 2 : 16) : (m + WG < 38 ? m + WG : 38); }
constexpr int p2_after(int m) {
  int pos = 0, posL[40] = {};
  for (int k = 0; k < p2_l0(0); ++k) { pos += 2; posL[k] = pos; }
  for (int s = 0; s < m; ++s) { pos += p2_extra(s); for (int k = p2_l0(s); k < p2_l0(s + 1); ++k) { pos += 2; posL[k] = pos; } }
  return pos - posL[m];
}
constexpr int PPUB = 25, PG = PGV, PC = PCV, P2 = PC + P2D, PT = PTV, PS = PSV, PF = PFV, PPOLL = -1;
constexpr int hb_ops(int n) { return n < 6 ? 10 : 14; }
constexpr int hb_g0(int n) { return n < 6 ? 3 : 4; }
constexpr int hb_extra(int P) { return P == PPUB ? 1 : P == PG ? 8 : P == PS ? 4 : P == PF ? 1 : P == PPOLL ? 1 : 0; }
constexpr int hb_cnt(int P, bool with_extra) {
  int t = 0;
  for (int n = 0; n < 8; ++n) for (int i = 0; i < hb_ops(n); ++i) { const int p = 16 * n + i; if (p > P) return t; t += (n >= 6 || (i >= hb_g0(n) && i < hb_g0(n) + 4)); if (p < P || with_extra) t += hb_extra(p); }
  return t;
}
constexpr int nxt(int P, int d) { for (; d > 0; --d) { ++P; if (P % 16 >= hb_ops(P / 16)) P = (P / 16 + 1) * 16; } return P; }
DEVI u64 utag(u32x2 e) { const u64 t = (((u64)(unsigned)__builtin_amdgcn_readfirstlane(e[1]) << 32) | (unsigned)__builtin_amdgcn_readfirstlane(e[0])) + 1; return t ? t : 1; }
template <int CTRL> DEVI float dppf(float v) { return __int_as_float(__builtin_amdgcn_update_dpp(0, __float_as_int(v), CTRL, 0xf, 0xf, true)); }
DEVI void sline(u64 base, unsigned off) {
  asm volatile("s_load_dwordx2 s[98:99], %0, %1" ::"s"(base), "s"(off + 60) : "memory", "s98", "s99");
}
template <int IMM> DEVI void setm0i(unsigned base) { asm volatile("s_add_i32 m0, %0, %1\n\ts_nop 0" ::"s"(base), "n"(IMM) : "memory", "scc"); }
DEVI void miss2(unsigned& n, u32x4 a, u32x4 b, unsigned tl, unsigned th) {
  unsigned t, u;
  asm("v_xor_b32 %1, %7, %3\n\tv_xor_b32 %2, %7, %5\n\tv_bitop3_b32 %1, %4, %8, %1 bitop3:0xbe\n\tv_bitop3_b32 %2, %6, %8, %2 bitop3:0xbe\n\t"
      "v_min_u32 %1, 1, %1\n\tv_min_u32 %2, 1, %2\n\tv_add3_u32 %0, %0, %1, %2"
      : "+v"(n), "=&v"(t), "=&v"(u) : "v"(a[1]), "v"(a[3]), "v"(b[1]), "v"(b[3]), "s"(tl), "s"(th));
}
DEVI unsigned geglu2(float g0, float g1, float u0, float u1, float k1, float k3, float s2) {
  float a, b;
  asm("v_mul_f32 %0, %4, %4\n\tv_mul_f32 %1, %5, %5\n\tv_fma_f32 %0, %0, %7, %6\n\tv_fma_f32 %1, %1, %7, %6\n\t"
      "v_mul_f32 %0, %0, %4\n\tv_mul_f32 %1, %1, %5\n\tv_exp_f32 %0, %0\n\tv_exp_f32 %1, %1\n\tv_mul_f32 %2, %2, %4\n\t"
      "v_fma_f32 %0, %0, %8, %8\n\tv_fma_f32 %1, %1, %8, %8\n\tv_rcp_f32 %0, %0\n\tv_rcp_f32 %1, %1\n\tv_mul_f32 %3, %3, %5\n\t"
      "v_mul_f32 %0, %0, %2\n\tv_mul_f32 %1, %1, %3\n\tv_cvt_pk_bf16_f32 %0, %0, %1"
      : "=&v"(a), "=&v"(b), "+v"(u0), "+v"(u1) : "v"(g0), "v"(g1), "v"(k1), "v"(k3), "v"(s2));
  return __float_as_uint(a);
}
DEVI void cf4(float& hr, float& hi, float ar, float ai, float br, float bi) {
  float t, u;
  asm("v_fma_f32 %0, -%5, %3, %6\n\tv_fma_f32 %1, %5, %2, %7\n\tv_fmac_f32 %0, %4, %2\n\tv_fmac_f32 %1, %4, %3"
      : "=&v"(t), "=&v"(u) : "v"(hr), "v"(hi), "v"(ar), "v"(ai), "v"(br), "v"(bi));
  hr = t; hi = u;
}
DEVI void csq(float& dr, float& di, float r, float i) {
  float t, u;
  asm("v_mul_f32 %0, %2, %2\n\tv_mul_f32 %1, %2, %3\n\tv_fma_f32 %0, -%3, %3, %0\n\tv_add_f32 %1, %1, %1" : "=&v"(t), "=&v"(u) : "v"(r), "v"(i));
  dr = t; di = u;
}
DEVI void cf4c(float hr, float hi, float ar, float ai, float nai, float& br, float& bi) {
  asm("v_fmac_f32 %0, %5, %3\n\tv_fmac_f32 %1, %6, %2\n\tv_fmac_f32 %0, %4, %2\n\tv_fmac_f32 %1, %4, %3"
      : "+v"(br), "+v"(bi) : "v"(hr), "v"(hi), "v"(ar), "v"(ai), "v"(nai));
}
DEVI void sc4(float& r, float& i, float rs, float rc, float csr, float csi) {
  asm("v_mul_f32 %0, %2, %0\n\tv_mul_f32 %1, %2, %1\n\tv_fmac_f32 %0, %3, %4\n\tv_fmac_f32 %1, %3, %5"
      : "+v"(r), "+v"(i) : "v"(rs), "v"(rc), "v"(csr), "v"(csi));
}
DEVI void kc4(float& r, float& i, float kr, float nkr, float nki) {
  float t, u;
  asm("v_mul_f32 %0, %4, %2\n\tv_mul_f32 %1, %5, %3\n\tv_fmac_f32 %0, %6, %3\n\tv_fmac_f32 %1, %6, %2"
      : "=&v"(t), "=&v"(u) : "v"(r), "v"(i), "v"(kr), "v"(nkr), "v"(nki));
  r = t; i = u;
}

__global__ __launch_bounds__(512, 2) void fused(const u16* __restrict__ x, const u16* __restrict__ lam_re, const u16* __restrict__ lam_im,
                                                const u16* __restrict__ Bre, const u16* __restrict__ Bim, const u16* __restrict__ Cre,
                                                const u16* __restrict__ Cim, const u16* __restrict__ logdt, const u16* __restrict__ dskip,
                                                const u16* __restrict__ wg, const u16* __restrict__ wu, const u16* __restrict__ wd,
                                                char* __restrict__ ws, float* __restrict__ out) {
  extern __shared__ __attribute__((aligned(16))) char shm[];
  asm volatile(".Lpf_beg:");
  u64 dg0_, dgn_ = 0; asm volatile("s_memrealtime %0" : "=s"(dg0_));
#define DG(n) if (DIAG == n) { asm volatile("s_memrealtime %0\n\ts_waitcnt lgkmcnt(0)" : "=s"(dgn_) :: "memory"); }
  STAMP_DECL STAMP(0)
  unsigned perm;
  const i32x4 sx = mk_srd(x, SL * SD * 2);
  {
    const int b0 = blockIdx.x, t = threadIdx.x, lead = ((b0 >> 6) + (b0 >> 3)) & 3, rot = (b0 * 5) & 6;
    perm = (0x76543210u - (15u << (4 * lead)) * (1u + ((((lead + 1) >> 2) ^ 1) << 16)) + rot * 0x11111111u) & 0x77777777u;
    const unsigned v0 = (((t >> 2) & 64) + ((t >> 3) & 16) + ((t >> 4) & 5) + (t & 40)) * 1024 + (((t & 7) ^ ((t >> 4) & 3)) * 16);
    const unsigned so = (((b0 & 6) * 2 + (b0 >> 6)) << 17) + (perm & 7) * 128;
    asm volatile("" ::"s"(lam_re), "s"(lam_im), "s"(Bre), "s"(Bim), "s"(Cre), "s"(Cim), "s"(logdt), "s"(dskip), "s"(wg), "s"(wu), "s"(wd), "s"(ws), "s"(out));
    asm volatile("s_waitcnt lgkmcnt(0)" : "+s"(dg0_) :: "memory");
    DG(1)
    const int j0 = __builtin_amdgcn_readfirstlane(t >> 6);
    const unsigned v1 = (v0 ^ 64) + 1024;
    setm0i<SLT>(j0 * 2048); dmao<0>(sx, v0, so); dmao<1024>(sx, v1, so);
    const i32x4 sb0 = mk_srd((j0 >> 2) ? Bim : Bre, SP * SD * 2);
    const unsigned vb0 = ((t >> 3) & 31) * 1024 + (((t & 7) ^ ((t >> 4) & 7)) * 16), sob = (((b0 & 1) * 8 + ((b0 >> 3) & 7)) << 15);
    setm0i<SLT + OFF_B>(j0 * 1024); dmao<0>(sb0, vb0, sob + (perm & 7) * 128);
    const unsigned k1 = ((perm >> 4) & 7) * 128;
    setm0i<2 * SLT>(j0 * 2048); dmao<0>(sx, v0, (so & ~1023u) + k1); dmao<1024>(sx, v1, (so & ~1023u) + k1);
    setm0i<2 * SLT + OFF_B>(j0 * 1024); dmao<0>(sb0, vb0, sob + k1);
    { const unsigned ix = b0 >> 3, q4 = (b0 >> 1) & 3;
      const char* p0 = (j0 & 1) ? (const char*)wu : (const char*)wg;
      const char* p1 = (j0 & 1) ? ((ix & 16) ? (const char*)Cim : (const char*)Cre) : (const char*)wd;
      const char* p2 = (j0 & 1) ? (const char*)ws : (const char*)out + q4 * 1048576;
      const char* p3 = (const char*)ws + WS_A2 + (q4 * 2 + (j0 & 1)) * (8 * NKS * 1024);
      const char* pb = (j0 & 4) ? ((j0 & 2) ? p3 : p2) : ((j0 & 2) ? p1 : p0);
      const unsigned off = j0 == 3 ? (ix & 15) * 32768 : ix * (j0 < 3 ? 44032 : j0 == 4 ? 32768 : j0 == 5 ? 24576 : 38400);
      sline((u64)pb, off); }
  }
  auto chunk = [&](int v) { return (perm >> (4 * v)) & 7; };
  const int j = __builtin_amdgcn_readfirstlane(ftid() >> 6), w = j & 3, sb_ = w & 1, pi = w >> 1;
  const int b = lnd(blockIdx.x), xcd = b & 7, idx = b >> 3;
  const int c = (xcd >> 1) * 4 + (idx >> 3), rsel = xcd & 1, gi = idx & 7, g = rsel * 8 + gi;
  auto get_fb = [&]() { const int l = (lnd(gi) + 1) >> 3; return (6 * (g - rsel)) * (1 - l) + (84 + rsel) * l; };
  auto get_ct = [&]() { const int slow = (3 - lnd((int)((u64)wd >> 7))) & 7; return lnd(gi == 7 ? slow : gi == slow ? 7 : gi); };
  const i32x4 sws = mk_srd(ws, WS_BYTES);
  const __amdgpu_buffer_rsrc_t rws = __builtin_amdgcn_make_buffer_rsrc(ws, 0, WS_BYTES, 0x00020000);
  const unsigned flagb = WS_FLAG + c * 16384;
  const unsigned a2pb = WS_A2 + (unsigned)(c * 4 + 2 * pi) * (NKS * 1024);
  const unsigned w4 = w * 4096, w2 = w * 2048, w6 = w * 6144;
  const unsigned so_x = (c * 128 + 64 * (w >> 1) + 16 * (w & 1)) * 1024;
  u64 tag2 = 0;
  auto set_flag = [&](int fi) {
    __builtin_amdgcn_raw_buffer_store_b64((u32x2){(unsigned)tag2, (unsigned)(tag2 >> 32)}, rws, (ftid() & 63) == 0 ? flagb + (g * 8 + fi) * 128 : OOB, 0, 16);
  };
  auto wait_cnt = [&]() {
#pragma unroll 1
    for (unsigned spins = 0; spins < SPIN_MAX; ++spins)
      if (__builtin_amdgcn_readfirstlane(__hip_atomic_load((LAS unsigned*)(size_t)M_CNT, __ATOMIC_RELAXED, __HIP_MEMORY_SCOPE_WORKGROUP)) == 4u) break;
    asm volatile("" ::: "memory");
  };

  if (j < 4) {
    const int lane = ftid() & 63, l5 = lane & 31, h = lane >> 5, blk = (lane >> 4) & 1, q = (lane & 15) >> 2, pp = lane & 3;
    unsigned vx[4], vw[3], ra[4], wt[2];
    const i32x4 sw = mk_srd((w >> 1) ? wu : wg, SD * SF * 2);
    const unsigned ld_w = ((w >> 1) ? OFF_WU : 0) + (w & 1) * 6144;
    unsigned so_w = 0;
    u32x4 sel;
    {
      const unsigned sw0 = ((lane & 7) ^ (lane >> 4)) * 16;
      for (int r = 0; r < 4; ++r) vx[r] = (32 * (lane >> 5) + 8 * ((lane >> 3) & 1) + ((lane >> 4) & 1) + r) * 1024 + (sw0 ^ ((r & 1) * 64));
      auto ldX = [&](auto S) { constexpr int s = S;
        const unsigned so = so_x + chunk(s) * 128;
        setm0i<((s + 1) % 6) * SLT>(w4);
        dmao<0>(sx, vx[0], so); dmao<1024>(sx, vx[1], so); dmao<2048>(sx, vx[2], so); dmao<3072>(sx, vx[3], so);
      };
      u32x2 ep2;
      STAMP(1)
      if (w == 0 && lane == 0) *(LAS unsigned*)(size_t)M_CNT = 0;
      const unsigned xa = w * 4096 + lane * 16;
      unsigned ti;
      auto setup = [&]() {
        for (int s = 0; s < 4; ++s) { ra[s] = pi * 8192 + l5 * 128 + (((2 * (s ^ pi) + h) ^ ((l5 >> 1) & 7)) * 16); pin(ra[s]); }
        for (int p = 0; p < 2; ++p) { wt[p] = 3 * SLT + (blk ? OFF_WU : 0) + (8 * h + q) * 192 + (((pp & 1) << 1) | (pp >> 1)) * 8 + sb_ * 96 + (p ? -pi * 3072 : pi * 3072); pin(wt[p]); }
        const unsigned selv = (((lane & 15) == 0 && blk == 0) || ((lane & 15) == 4 && blk == 1)) ? 0x3f803f80u : 0u; sel = (u32x4){selv, selv, selv, selv}; pin(sel);
        ti = (lane & 4) ? M_CNT + 64 - M_ST : (64 * (w >> 1) + 32 * (lane >> 5) + 16 * (w & 1) + 4 * (lane & 3) + ((lane >> 3) & 3)) * 8;
        pin(ti);
      };
      auto setup2 = [&]() { const int q12 = (lane * 43) >> 9, r12 = lane - 12 * q12;
        for (int i = 0; i < 3; ++i) { const int r = r12 + 4 * i, cy = r >= 12, col = r - 12 * cy;
          vw[i] = (gi == 7 && col >= 2) ? OOB : (5 * i + q12 + cy) * (SF * 2) + col * 16 - i * 1024; pin(vw[i]); }
        so_w = (w & 1) * 32 * (SF * 2) + get_fb() * 32; };
      float s1[4] = {}, s2[4] = {};
      auto stats = [&](auto S, auto&& mid) { constexpr int s = S;
        u32x4 xr[4];
        for (int k = 0; k < 4; ++k) xr[k] = lds_r128(((s + 1) % 6) * SLT + k * 1024 + xa);
        sb(); mid(); sb();
        for (int e = 0; e < 4; ++e) for (int i = 0; i < 4; ++i) { const unsigned u = xr[i][e]; s1[i] = dot2(u, 0x3f803f80u, s1[i]); s2[i] = dot2(u, u, s2[i]); }
        for (int i = 0; i < 4; ++i) { pin(s1[i]); pin(s2[i]); }
        sb(); };
      wait_vm<3>(); wait_lgkm0(); STA(46) bar(); STA(47)
      DG(2)
      stats(IC(0), [&] { ldX(IC(2)); ld64_sc1(ep2, sws, 0, WS_E2 + c * 128); });
      wait_vm<5>(); STA(48) bar(); STA(49)
      stats(IC(1), [&] { ldX(IC(3)); setup(); });
      sfor<2, 8>([&](auto S) { constexpr int s = S;
        wait_vm<(s < 7 ? 4 : 0)>();
        stats(S, [&] { if constexpr (s < 6) ldX(IC(s + 2)); if constexpr (s == 6) setup2(); });
        if constexpr (s < 7) { STA(46 + 2 * s) bar(); STA(47 + 2 * s) }
      });
      pin(ep2); tag2 = utag(ep2);
      float t1[2], t2[2];
      for (int k = 0; k < 2; ++k) { const bool o = lane & 1;
        t1[k] = (o ? s1[2 * k + 1] : s1[2 * k]) + dppf<0xB1>(o ? s1[2 * k] : s1[2 * k + 1]); t2[k] = (o ? s2[2 * k + 1] : s2[2 * k]) + dppf<0xB1>(o ? s2[2 * k] : s2[2 * k + 1]); }
      const bool o = lane & 2;
      float sm = (o ? t1[1] : t1[0]) + dppf<0x4E>(o ? t1[0] : t1[1]), sq = (o ? t2[1] : t2[0]) + dppf<0x4E>(o ? t2[0] : t2[1]);
      sm += dppf<0x104>(sm); sq += dppf<0x104>(sq);
      const float mean = sm * (1.f / SD);
      const float var = fmaxf(sq * (1.f / SD) - mean * mean, 0.f);
      const float sd = rbf(__builtin_amdgcn_sqrtf(rbf(rbf(var) + 1e-5f)));
      const float r_ = __builtin_amdgcn_rcpf(sd); const unsigned nm = pk2(-mean, -mean);
      *(LAS f32x2*)(size_t)(M_ST + ti) = (f32x2){r_, lo2f(nm) * r_}; *(LAS f32x2*)(size_t)(M_SD + ti) = (f32x2){sd, __uint_as_float(nm)};
    }
    f32x16 acc[3][2];
    f32x4 csg[3] = {};
    u32x4 af[2][2]; u32x2 wf[2][3][2];
    auto rd1 = [&](auto N, auto S, auto I) { constexpr int n = N, s = S, i = I;
      const unsigned a_ = ra[s] + (n % 3) * SLT, t_ = wt[s & 1] + (n % 3) * SLT + s * 3072;
      if constexpr (i == 0) { wf[s & 1][0][0] = lds_tr(t_); wf[s & 1][0][1] = lds_tr(t_ + 768); }
      if constexpr (i == 1) { af[s & 1][0] = lds_r128(a_); af[s & 1][1] = lds_r128(a_ + 4096); }
      if constexpr (i == 2) { wf[s & 1][1][0] = lds_tr(t_ + 32); wf[s & 1][1][1] = lds_tr(t_ + 32 + 768); }
      if constexpr (i == 3) { wf[s & 1][2][0] = lds_tr(t_ + 64); wf[s & 1][2][1] = lds_tr(t_ + 64 + 768); }
    };
    auto W = [&](int p, int k) { return (u32x4){wf[p][k][0][0], wf[p][k][0][1], wf[p][k][1][0], wf[p][k][1][1]}; };
    STAMP(22)
    wait_vm<0>(); wait_lgkm0(); bar();
    DG(3)
    STAMP(2)
    sfor<0, 4>([&](auto I) { rd1(IC(0), IC(0), I); });
    sfor<0, 32>([&](auto K) { constexpr int n = K / 4, s = K % 4, nn = (K + 1) / 4, sn = (K + 1) % 4;
      sfor<0, 6>([&](auto I) { constexpr int i = I;
        acc[i >> 1][i & 1] = mfma(W(s & 1, i >> 1), af[s & 1][i & 1], K == 0 ? (f32x16){} : acc[i >> 1][i & 1]);
        sb();
        if constexpr (s == 3 && n < 7) { if constexpr (i == 0) { if constexpr (n >= 1) wait_vm<(n < 6 ? 10 : 0)>(); wait_lgkm0(); STB(32 + 2 * n) bar(); STB(33 + 2 * n) } if constexpr (i >= 1 && i < 5) rd1(IC(nn), IC(sn), IC(i - 1)); }
        else if constexpr (K < 31 && i < 4) rd1(IC(nn), IC(sn), I);
        if constexpr (i >= 4 && K < 23) {
          constexpr int xn = (K + 1) / 4 + 2, kk = (K + 1) % 4, o0 = i == 5 ? (kk == 0 ? 0 : 3 * kk) : (kk == 0 ? 0 : 3 * kk - 1), o1 = i == 5 ? (kk == 3 ? 10 : 3 * kk + 2) : (kk == 0 ? 0 : 3 * kk);
          sfor<o0, o1>([&](auto O) { constexpr int o = O;
            if constexpr (o < 4) { if constexpr (xn > 2) { if constexpr (o == 0) setm0i<(xn % 3) * SLT>(w4); dmao<o * 1024>(sx, vx[o], so_x + chunk((xn + 5) & 7) * 128); } }
            else { constexpr int u = o - 4; if constexpr (u % 3 == 0) setm0i<(3 + xn % 3) * SLT + u * 1024>(ld_w);
              dmao<(u % 3) * 1024>(sw, vw[u % 3], so_w + chunk((xn + 5) & 7) * (64 * SF * 2) + (u / 3) * 16 * (SF * 2)); } }); }
        if constexpr (K == 31 && i == 1) { if (lane < 32) for (int k = 0; k < 3; ++k) *(LAS float*)(size_t)(M_CS + (w * 3 + k) * 128 + lane * 4) = csg[k][0]; }
        if constexpr (K == 31 && i == 4) { wait_lgkm0();
          if (lane == 0) __hip_atomic_fetch_add((LAS unsigned*)(size_t)M_CNT, 1u, __ATOMIC_RELAXED, __HIP_MEMORY_SCOPE_WORKGROUP); }
        sb();
      });
      if constexpr (!(s & 1)) { for (int k = 0; k < 3; ++k) csg[k] = mfma16(sel, W(s & 1, k), csg[k]); sb(); }
    });
    DG(4)
    f32x2 st[2], sd[2];
    { const unsigned ia = (64 * pi + 32 * ((lane >> 2) & 1) + 4 * ((lane >> 3) & 3) + (lane & 3)) * 8;
      for (int rb = 0; rb < 2; ++rb) { st[rb] = *(LAS f32x2*)(size_t)(M_ST + ia + rb * 128); sd[rb] = *(LAS f32x2*)(size_t)(M_SD + ia + rb * 128); } }
    wait_cnt();
    for (int k = 0; k < 3; ++k) {
      float cs = csg[k][0] + *(LAS float*)(size_t)(M_CS + ((w ^ 2) * 3 + k) * 128 + l5 * 4);
      cs = h ? 0.f : cs;
      const float c0 = rbf(cs), r1 = cs - c0, c1 = rbf(r1), c2 = r1 - c1;
      const u32x4 cf = {pk2(c0, c1), pk2(c2, 0.f), 0u, 0u};
      for (int rb = 0; rb < 2; ++rb) { const unsigned nm = __float_as_uint(sd[rb][1]); acc[k][rb] = mfma(cf, (u32x4){nm, nm, 0u, 0u}, acc[k][rb]); }
    }
    for (int k = 0; k < 3; ++k) { pin(acc[k][0]); pin(acc[k][1]); }
    asm volatile("s_nop 11" ::: "memory");
    for (int k = 0; k < 3; ++k) { pin(acc[k][0]); pin(acc[k][1]); }
    __builtin_amdgcn_s_setprio(CPE);
    STAMP(7)
    const int fb = get_fb();
    sfor<0, 3>([&](auto K) { constexpr int k = K;
      const unsigned vst = (gi < 7 || 3 * sb_ + k == 0) ? a2pb + lane * 16 + (64 + fb + 3 * sb_ + k) * 1024 : OOB;
      sfor<0, 2>([&](auto R_) { constexpr int rb = R_;
        const float rs = st[rb][0], k1 = -2.302208198f * rs, k3 = -0.1029432397f * rs * rs * rs, s2 = sd[rb][0] * sd[rb][0];
        unsigned o[4];
        for (int i = 0; i < 4; ++i) o[i] = geglu2(acc[k][rb][2 * i], acc[k][rb][2 * i + 1], acc[k][rb][8 + 2 * i], acc[k][rb][9 + 2 * i], k1, k3, s2);
        __builtin_amdgcn_raw_buffer_store_b128((u32x4){o[0], o[1], o[2], o[3]}, rws, vst + rb * NKS * 1024, 0, 16);
      });
    });
    sb();
    STAMP(9)
    wait_vm<0>();
    set_flag(4 + w);
    STAMP(10)
    const int t = ftid(), ch = t & 15, r4 = t >> 4, ct = get_ct();
    const int trow = 64 * rsel + 32 * ((r4 >> 2) & 1) + 8 * (r4 & 1) + 2 * (r4 >> 3) + ((r4 >> 1) & 1);
    const int irow = 64 * rsel + 32 * ((r4 >> 2) & 1) + 4 * (r4 >> 3) + (r4 & 3);
    u32x2 xv[4], dk;
    for (int it = 0; it < 4; ++it) ld64(xv[it], sx, trow * 1024 + ch * 8, c * 131072 + ct * 128 + (4 * (it & 1) + 16 * (it >> 1)) * 1024);
    ld64(dk, mk_srd(dskip, SD * 2), ch * 8, ct * 128);
    wait_vm<0>(); for (int it = 0; it < 4; ++it) pin(xv[it]); pin(dk);
    f32x4 base[4]; unsigned ph[4];
    for (int it = 0; it < 4; ++it) { const f32x2 s2 = *(LAS f32x2*)(size_t)(M_ST + (irow + 8 * (it & 1) + 16 * (it >> 1)) * 8);
      for (int e = 0; e < 4; ++e) { const unsigned xu = xv[it][e >> 1], du = dk[e >> 1];
        const float xe = (e & 1) ? hi2f(xu) : lo2f(xu), de = (e & 1) ? hi2f(du) : lo2f(du);
        base[it][e] = xe + rbf(rbf(__builtin_fmaf(xe, s2[0], s2[1])) * de); }
      const int e = 2 * it + (r4 >> 3), d = (e * 11) >> 5;
      ph[it] = (3 + d) * SLT + (e - 3 * d) * 2048 + (r4 & 7) * 256 + (ch ^ (r4 & 7)) * 16;
      pin(base[it]); pin(ph[it]); }
    float* op = out + (c * 128 + trow) * SD + ct * 64 + ch * 4;
    bar();
    STAMP(11)
    for (int it = 0; it < 4; ++it) {
      f32x4 p[4];
      for (int ww = 0; ww < 4; ++ww) p[ww] = *(LAS f32x4*)(size_t)(ph[it] + ww * 6144);
      *(f32x4*)(op + (4 * (it & 1) + 16 * (it >> 1)) * SD) = base[it] + ((p[0] + p[1]) + (p[2] + p[3]));
    }
    if (g == 0) __builtin_amdgcn_raw_buffer_store_b64((u32x2){(unsigned)tag2, (unsigned)(tag2 >> 32)}, rws, t == 0 ? WS_E2 + c * 128 : OOB, 0, 16);
    DG(5)
    if (DIAG) { const u64 e_ = __builtin_amdgcn_s_memrealtime() + 4 * (dgn_ - dg0_); while (__builtin_amdgcn_s_memrealtime() < e_) __builtin_amdgcn_s_sleep(1); }
  } else {
    const int lane = ftid() & 63, l5 = lane & 31, h = lane >> 5, blk = (lane >> 4) & 1, q = (lane & 15) >> 2, pp = lane & 3;
    const unsigned va = lane * 16;
    const unsigned sw0 = ((lane & 7) ^ (lane >> 4)) * 16;
    unsigned vb[2], vw[3], vd[2];
    for (int r = 0; r < 2; ++r) vb[r] = ((lane >> 3) + 7 * r) * 1024 + (sw0 ^ (r * 64));
    const i32x4 sbm = mk_srd((w >> 1) ? Bim : Bre, SP * SD * 2);
    const unsigned so_b = (g * 32 + (w & 1) * 16) * 1024;
    auto ldB = [&](auto S) { constexpr int s = S;
      const unsigned so = so_b + chunk(s) * 128;
      setm0i<((s + 1) % 6) * SLT + OFF_B>(w2); dmao<0>(sbm, vb[0], so); dmao<1024>(sbm, vb[1], so); };
    u32x2 ep2, ep1; unsigned plr, pli, pdt;
    ld64_sc1(ep2, sws, 0, WS_E2 + c * 128);
    ld64_sc1(ep1, sws, 0, WS_E1 + g * 128);
    const int ps = (lane & 3) | ((lane & 4) << 1) | ((lane & 8) >> 1);
    { const unsigned po = (g * 32 + sb_ * 16 + ps) * 2;
      ld16(plr, mk_srd(lam_re, SP * 2), po); ld16(pli, mk_srd(lam_im, SP * 2), po); ld16(pdt, mk_srd(logdt, SP * 2), po); }
    { u64 pc; unsigned len, back;
      asm volatile("s_getpc_b64 %0\n.Lpf_here:\n\ts_mov_b32 %1, .Lpf_end-.Lpf_beg\n\ts_mov_b32 %2, .Lpf_here-.Lpf_beg" : "=s"(pc), "=s"(len), "=s"(back));
      for (unsigned o = (idx * 4 + w) * 128; o + 128 < len; o += 16384) sline((pc - back) & ~127ull, o + 128); }
    const i32x4 sw = mk_srd((w >> 1) ? wu : wg, SD * SF * 2);
    const unsigned so_w = (w & 1) * 32 * (SF * 2) + get_fb() * 32;
    const unsigned ld_w = ((w >> 1) ? OFF_WU : 0) + (w & 1) * 6144;
    auto opW = [&](auto N, auto I) { constexpr int n = N, i = I;
      if constexpr (i % 3 == 0) setm0i<(3 + n % 3) * SLT + i * 1024>(ld_w);
      dmao<(i % 3) * 1024>(sw, vw[i % 3], so_w + chunk((n + 5) & 7) * (64 * SF * 2) + (i / 3) * 16 * (SF * 2)); };
    unsigned ra[4], wa[4];
    for (int s = 0; s < 4; ++s) { ra[s] = pi * 8192 + l5 * 128 + (((2 * s + h) ^ ((l5 >> 1) & 7)) * 16);
      const int row = sb_ * 16 + ps; wa[s] = OFF_B + blk * 4096 + row * 128 + (((2 * s + h) ^ ((row >> 1) & 7)) * 16); }
    const unsigned selv = (((lane & 15) == 0 && blk == 0) || ((lane & 15) == 4 && blk == 1)) ? 0x3f803f80u : 0u;
    const u32x4 sel = {selv, selv, selv, selv};
    for (int s = 0; s < 4; ++s) { pin(ra[s]); pin(wa[s]); }
    unsigned t1l = 0, t1h = 0;
    const int cc = 2 * c + pi;
    float ar, ai, nai, kr, nkr, nki, a8r, a8i, a16r, a16i, a32r, a32i, a64r, a64i, pwr = 1.f, pwi = 0.f, a256r, a256i, mr = 1.f, mi = 0.f;
    u32x4 oh[2];
    float lr, li;
    auto params = [&](auto V) { constexpr int v = V;
      if constexpr (v == 1) { const int q12 = (lane * 43) >> 9, r12 = lane - 12 * q12;
        for (int i = 0; i < 3; ++i) { const int r = r12 + 4 * i, cy = r >= 12, col = r - 12 * cy;
          vw[i] = (gi == 7 && col >= 2) ? OOB : (5 * i + q12 + cy) * (SF * 2) + col * 16 - i * 1024; pin(vw[i]); } }
      if constexpr (v == 2) {
        pin(ep2); pin(ep1); pin(plr); pin(pli); pin(pdt);
        tag2 = utag(ep2);
        const u64 tag1 = utag(ep1); t1l = (unsigned)tag1; t1h = (unsigned)(tag1 >> 32); }
      if constexpr (v == 3) {
        lr = lo2f(plr); li = lo2f(pli);
        const float dt = rbf(__expf(lo2f(pdt)));
        const float er = __expf(dt * lr);
        const float rev = dt * li * 0.15915494309189535f;
        const float fr = rev - __builtin_rintf(rev);
        ar = er * __builtin_amdgcn_cosf(fr); ai = er * __builtin_amdgcn_sinf(fr);
        pin(ar); pin(ai); }
      if constexpr (v == 4) { a8r = ar; a8i = ai;
        for (int i = 0; i < 3; ++i) csq(a8r, a8i, a8r, a8i);
        csq(a16r, a16i, a8r, a8i); pin(a16r); pin(a16i); }
      if constexpr (v == 6) {
        const float nr = ar - 1.f, ni = ai, den = 2.f * __builtin_amdgcn_rcpf(vfma(lr, lr, li * li));
        kr = vfma(nr, lr, ni * li) * den; nki = vfma(nr, li, -(ni * lr)) * den; nkr = -kr; nai = -ai;
        pin(nai); pin(kr); pin(nkr); pin(nki); }
      if constexpr (v == 10) { csq(a32r, a32i, a16r, a16i); csq(a64r, a64i, a32r, a32i); if (blk) { pwr = a16r; pwi = a16i; mr = a64r; mi = a64i; } }
      if constexpr (v == 11) { float tr = pwr, ti = pwi; cf4(tr, ti, a32r, a32i, 0.f, 0.f); if (h) { pwr = tr; pwi = ti; } csq(a32r, a32i, a64r, a64i); }
      if constexpr (v == 12) { float ur = mr, ui = mi; cf4(ur, ui, a32r, a32i, 0.f, 0.f); if (h) { mr = ur; mi = ui; } csq(a256r, a256i, a32r, a32i); }
      if constexpr (v == 13 || v == 14) { constexpr int u = v - 13;
        const unsigned val = (((lane >> 2) & 1) == h && blk == u) ? ((lane & 1) ? 0x3f800000u : 0x3f80u) : 0u; const int d0 = ((lane >> 1) & 1) | ((lane >> 2) & 2);
        for (int d = 0; d < 4; ++d) oh[u][d] = d0 == d ? val : 0u;
        pin(oh[u]); }
      sb();
    };
    f32x16 sa[2] = {};
    f32x4 csa = {};
    { u32x4 af[2][4][2], wf[2][4];
      auto rdA = [&](int p, int s, unsigned slot) { wf[p][s] = lds_r128(slot + wa[s]); af[p][s][0] = lds_r128(slot + ra[s]); af[p][s][1] = lds_r128(slot + ra[s] + 4096); };
      auto mmA = [&](int p, int s) { sa[0] = mfma(af[p][s][0], wf[p][s], sa[0]); sa[1] = mfma(af[p][s][1], wf[p][s], sa[1]); pin(sa[0]); pin(sa[1]); };
      auto csA = [&](int p) { for (int s = 0; s < 4; ++s) csa = mfma16(sel, wf[p][s], csa); pin(csa); sb(); };
      auto W3 = [&](auto N, auto H_) { sfor<0, 3>([&](auto I) { opW(N, IC(3 * H_ + I)); }); };
      unsigned rh[4], wh[4];
      for (int s = 0; s < 4; ++s) { rh[s] = ra[s] + 3 * SLT; wh[s] = wa[s] + 3 * SLT - 8192; pin(rh[s]); pin(wh[s]); wh[s] += 8192; wa[s] -= 8192; pin(wa[s]); wa[s] += 8192; }
      ldB(IC(2));
      __builtin_amdgcn_s_setprio(HPA);
      wait_vm<10>(); STA(46) bar(); STA(47) for (int s = 0; s < 4; ++s) rdA(0, s, SLT); sb(); ldB(IC(3));
      sfor<1, 8>([&](auto V) { constexpr int v = V, p = v & 1, sl = (v + 1) % 6;
        wait_vm<(v == 1 ? 9 : WEARLY ? (v < 4 ? 4 : v == 4 ? 7 : v == 5 ? 10 : v == 6 ? 11 : 6) : (v < 5 ? 4 : v == 5 ? 7 : v == 6 ? 11 : 6))>(); wait_lgkm0();
        if constexpr (v == 7) __builtin_amdgcn_s_setprio(3);
        STA(46 + 2 * v) bar(); STA(47 + 2 * v)
        sfor<0, 8>([&](auto I) { constexpr int i = I;
          sa[i & 1] = mfma(af[p ^ 1][i >> 1][i & 1], wf[p ^ 1][i >> 1], sa[i & 1]); pin(sa[i & 1]);
          sb();
          if constexpr (i < 6) sfor<2 * i, 2 * i + 2>([&](auto R) { constexpr int r = R, s = r / 3, k = r % 3, o = (sl % 3) * SLT;
            if constexpr (k == 0) wf[p][s] = lds_r128((sl < 3 ? wa[s] : wh[s]) + o); else af[p][s][k - 1] = lds_r128((sl < 3 ? ra[s] : rh[s]) + o + (k - 1) * 4096); });
          if constexpr (i == 6 && v < 5) ldB(IC(v + 3));
          if constexpr (i == 7 && v == 4) W3(IC(0), IC(0));
          if constexpr (i == 6 && v == 5) W3(IC(0), IC(1));
          if constexpr (i == 7 && v == 5) W3(IC(1), IC(0));
          if constexpr (i == 6 && v == 6) W3(IC(1), IC(1));
          sb(); });
        csA(p ^ 1);
        params(V);
      });
      STAMP(2)
      for (int s = 0; s < 4; ++s) mmA(1, s);
      csA(1);
      __builtin_amdgcn_s_setprio(HPB);
    }
    const int kb = 22 * w - (w == 3), cnt = w < 2 ? 22 : 21, ct = get_ct();
    const i32x4 swd = mk_srd(wd + kb * 8192, cnt * 16384);
    const unsigned so_c = ct * 65536 + w * 256;
    const unsigned r2 = (xcd * 11) >> 2;
    auto kofs = [&](int p, unsigned sc_) { const unsigned r = lnd(r2 * sc_), a = r + p * sc_, b2 = r + (p - 22) * sc_; return a < b2 ? a : b2; };
    for (int r = 0; r < 2; ++r) vd[r] = ((lane >> 3) + 7 * r) * 1024 + (((lane & 7) ^ (((lane >> 4) & 1) << 2)) * 16) + ct * 128;
    unsigned so_t = 0; i32x4 sc = swd;
    auto wd_op = [&](auto M, auto E, auto R) { constexpr int m = M, e = E, r = R;
      if constexpr (r == 0) { so_t = kofs(m < 18 ? (m + 14) % 18 : m, 16384); setm0i<cell_c(e)>(cell_k(e) == 0 ? w6 : cell_k(e) == 1 ? w4 : w2); }
      dmao<r * 1024>(swd, vd[r], so_t);
    };
    auto dma_wd = [&](auto M, auto E) { wd_op(M, E, IC(0)); wd_op(M, E, IC(1)); };
    auto c_op = [&](auto N, auto I) { constexpr int n = N, i = I;
      if constexpr (i == 0) { const int kg = ((n >> 1) + lnd(xcd)) & 3; so_t = so_c + (n & 1) * 32768 + (kg & 1) * 128; sc = mk_srd((kg >> 1) ? Cim : Cre, SD * SP * 2); }
      if constexpr (!(i & 1)) setm0i<(n & 1) ? ((i >> 1) ? M_T : cell_c(15)) : cell_c(16 + (i >> 1))>(w2);
      dmao<(i & 1) * 1024>(sc, vb[i & 1], so_t + (i >> 1) * 16384);
    };
    unsigned rcx[4];
    for (int s = 0; s < 4; ++s) rcx[s] = OFF_B + w * 2048 + blk * SLT + (lane & 15) * 128 + (((2 * s + h) ^ ((l5 >> 1) & 7)) * 16);
    const unsigned rcd = 2 * SLT + blk * (M_T - cell_c(15) - SLT);
    auto rd_c = [&](int n, u32x4* d) { for (int s = 0; s < 4; ++s) d[s] = lds_r128(rcx[s] + ((n & 1) ? rcd : 0u)); };
    const unsigned granb = WS_GRAN + (g * 2 + sb_) * 8192;
    const i32x4 sgr = mk_srd(ws + granb, 8192);
    const unsigned vg = (32 - cc) * 256 + va;
    float R[16], I_[16], csr, csi, er[2], ei[2], sr[4], si[4], prer, prei, cr, ci;
    f32x4 tb[8];
    u32x4 ge[8], fr4[4];
    f32x16 td[2];
    unsigned hp[2][8];
    unsigned nbad = 0, fo_g = 0, pa[2], tdl = 0;
    auto side = [&](auto P_) { constexpr int P = P_;
      if constexpr (P == 0) { for (int k = 0; k < 8; ++k) tb[k] = *(LAS f32x4*)(size_t)(M_ST + pi * 512 + (lane >> 4) * 128 + k * 16);
        csr = csa[0]; float t = csr; swap32(csr, t); csi = csr; swap16(csr, csi); }
      if constexpr (P >= 1 && P < 9) { constexpr int k = P - 1;
        for (int r = 0; r < 2; ++r) { R[2 * k + r] = sa[0][2 * k + r]; I_[2 * k + r] = sa[1][2 * k + r]; swap16(R[2 * k + r], I_[2 * k + r]); }
        for (int r = 0; r < 2; ++r) sc4(R[2 * k + r], I_[2 * k + r], tb[k][2 * r], tb[k][2 * r + 1], csr, csi); }
      if constexpr (P == 9) { for (int r = 0; r < 2; ++r) { er[r] = R[r]; ei[r] = I_[r]; cf4(er[r], ei[r], ar, ai, R[2 + r], I_[2 + r]); } }
      if constexpr (P >= 16 && P < 22) { constexpr int k = P - 14; for (int r = 0; r < 2; ++r) cf4(er[r], ei[r], ar, ai, R[2 * k + r], I_[2 * k + r]); }
      if constexpr (P == 22) { sr[0] = er[0]; si[0] = ei[0]; cf4(sr[0], si[0], a8r, a8i, er[1], ei[1]); sr[1] = sr[0]; swap16(sr[0], sr[1]); si[1] = si[0]; swap16(si[0], si[1]); }
      if constexpr (P == 23) { sr[2] = sr[0]; swap32(sr[0], sr[2]); si[2] = si[0]; swap32(si[0], si[2]); sr[3] = sr[1]; swap32(sr[1], sr[3]); si[3] = si[1]; swap32(si[1], si[3]); }
      if constexpr (P == 24) { cr = sr[0]; ci = si[0]; cf4(cr, ci, a16r, a16i, sr[1], si[1]); sr[1] = cr; si[1] = ci; cf4(cr, ci, a16r, a16i, sr[2], si[2]); sr[2] = cr; si[2] = ci; }
      if constexpr (P == PPUB) { cf4(cr, ci, a16r, a16i, sr[3], si[3]);
        __builtin_amdgcn_raw_buffer_store_b128((u32x4){__float_as_uint(cr), t1l, __float_as_uint(ci), t1h}, rws, lane < 16 ? granb + (31 - cc) * 256 + va : OOB, 0, 16); }
      if constexpr (P == 32) { prer = h ? (blk ? sr[2] : sr[1]) : (blk ? sr[0] : 0.f); prei = h ? (blk ? si[2] : si[1]) : (blk ? si[0] : 0.f); }
      sfor<0, 16>([&](auto K) { constexpr int k = K; if constexpr (P == nxt(33, k < 4 ? k / 2 : k - 2)) kc4(R[k], I_[k], kr, nkr, nki); });
      if constexpr (P >= 53 && P < 58) params(IC(P - 43));
      if constexpr (P == PG) sfor<0, 8>([&](auto K) { constexpr int k = K; ld128_sc1<(k & 3) * 1024>(ge[k], sgr, vg, (k >> 2) * 4096); });
      if constexpr (P == PC) { STB(23) wait_vm<hb_cnt(PC, false) - hb_cnt(PG, true)>(); for (int i = 0; i < 8; ++i) pin(ge[i]); STB(24) }
      if constexpr (P == PC + 3) STB(25) if constexpr (P == PC + 6) STB(26) if constexpr (P == PC + 9) STB(27)
      if constexpr (P == PC || P == PC + 1) { constexpr int i = 4 * (P - PC); miss2(nbad, ge[i], ge[i + 1], t1l, t1h); miss2(nbad, ge[i + 2], ge[i + 3], t1l, t1h); }
      if constexpr (P == PC + 2) { const unsigned nout = 8 - ((cc + 3 - (lane >> 4)) >> 2);
#pragma unroll 1
        for (unsigned spins = 0; spins < SPIN_MAX; ++spins) {
          if (__all(nbad == nout)) break;
          STAMP(28)
          __builtin_amdgcn_s_sleep(SLP);
          sfor<0, 8>([&](auto I) { constexpr int i = I; ld128_sc1_rw<(i & 3) * 1024>(ge[i], sgr, vg, (i >> 2) * 4096); });
          wait_vm<0>(); for (int i = 0; i < 8; ++i) pin(ge[i]);
          nbad = 0;
          for (int i = 0; i < 8; i += 2) miss2(nbad, ge[i], ge[i + 1], t1l, t1h);
        }
        cr = __uint_as_float(ge[7][0]); ci = __uint_as_float(ge[7][2]); cf4(cr, ci, a256r, a256i, __uint_as_float(ge[6][0]), __uint_as_float(ge[6][2])); }
      if constexpr (P >= PC + 3 && P < PC + 6) { constexpr int i = 5 - 2 * (P - PC - 3);
        cf4(cr, ci, a256r, a256i, __uint_as_float(ge[i][0]), __uint_as_float(ge[i][2])); cf4(cr, ci, a256r, a256i, __uint_as_float(ge[i - 1][0]), __uint_as_float(ge[i - 1][2])); }
      if constexpr (P == PC + 6) { cf4(cr, ci, mr, mi, 0.f, 0.f); float t = cr, u = ci; swap16(cr, t); swap16(ci, u); cr = vadd(cr, t); ci = vadd(ci, u); }
      if constexpr (P == PC + 7) { float t = cr, u = ci; swap32(cr, t); swap32(ci, u); cr = vadd(cr, t); ci = vadd(ci, u); cf4(cr, ci, pwr, pwi, prer, prei); }
      if constexpr (P == PC + 8) { sr[0] = cr; si[0] = ci; cf4(cr, ci, a8r, a8i, er[0], ei[0]); er[0] = sr[0]; ei[0] = si[0]; er[1] = cr; ei[1] = ci; }
      if constexpr (P == PC + 9) { kc4(er[0], ei[0], kr, nkr, nki); kc4(er[1], ei[1], kr, nkr, nki); }
      if constexpr (P % 16 == 4 && P / 16 >= PFW_S && P / 16 < 6) sfor<0, PFW>([&](auto U) { constexpr int k = (P / 16 - PFW_S) * PFW + U + PFW_O, m = k < 4 ? 18 + k : k < 18 ? k : k - 18;
        if constexpr (k < 22) { const unsigned o = kofs(m < 18 ? (m + 14) % 18 : m, 16384); if (o < (unsigned)cnt * 16384) for (int r = 0; r < 4; ++r) sline((u64)(wd + kb * 8192), o + ct * 128 + (4 * (c & 3) + r) * 1024); } });
      sfor<0, 8>([&](auto K) { constexpr int k = K; if constexpr (P == nxt(P2, k)) {
        for (int r = 0; r < 2; ++r) cf4c(k ? R[2 * k - 2 + r] : er[r], k ? I_[2 * k - 2 + r] : ei[r], ar, ai, nai, R[2 * k + r], I_[2 * k + r]);
        hp[0][k] = pk2(R[2 * k], R[2 * k + 1]); hp[1][k] = pk2(I_[2 * k], I_[2 * k + 1]); } });
      static_assert(PT > nxt(P2, 7) && PS > PT && PF > PS && PC % 16 == 0, "scan schedule");
      if constexpr (P == PT) {
        __builtin_amdgcn_s_setprio(3);
        for (int part = 0; part < 2; ++part) {
          td[part] = mfma((u32x4){hp[part][0], hp[part][1], hp[part][2], hp[part][3]}, oh[0], (f32x16){});
          td[part] = mfma((u32x4){hp[part][4], hp[part][5], hp[part][6], hp[part][7]}, oh[1], td[part]); pin(td[part]); }
        __builtin_amdgcn_s_setprio(HPB); }
      if constexpr (P == PS) { for (int k = 0; k < 4; ++k) for (int d = 0; d < 4; ++d) fr4[k][d] = pk2(td[k >> 1][8 * (k & 1) + 2 * d], td[k >> 1][8 * (k & 1) + 2 * d + 1]); }
      if constexpr (P == PS) { for (int k = 0; k < 4; ++k)
          __builtin_amdgcn_raw_buffer_store_b128(fr4[k], rws, a2pb + (2 * g + sb_) * 1024 + va + ((k & 1) * NKS + (k >> 1) * 32) * 1024, 0, 16); }
      if constexpr (P == 100) { const int ks = kb + lane, p6 = (ks * 43) >> 8, pg = ks < 84 ? p6 + (p6 >= 7) : 7 + 8 * (ks - 84), pb = ks < 84 ? ks - 6 * p6 : 0;
        fo_g = lane < cnt ? (pg * 8 + 4 + 2 * rsel + (pb >= 3)) * 128 : OOB; pin(fo_g); }
      if constexpr (P == 104) { for (int rb = 0; rb < 2; ++rb) { const int e = 4 * rb + (l5 >> 3), d = (e * 11) >> 5;
          pa[rb] = (3 + d) * SLT + (e - 3 * d) * 2048 + w6 + (lane & 7) * 256 + ((h ^ (lane & 7)) * 16); pin(pa[rb]); } }
      if constexpr (P == 106) { tdl = (8 * h + q) * 128 + (((blk * 2 + (pp >> 1)) ^ ((q >> 1) << 2)) * 16) + (pp & 1) * 8; pin(tdl); }
      if constexpr (P == PF) { wait_vm<hb_cnt(PF, false) - hb_cnt(PS, true)>(); set_flag(w);
        if (cc == 31) __builtin_amdgcn_raw_buffer_store_b64((u32x2){t1l, t1h}, rws, (lane == 0 && sb_ == 0) ? WS_E1 + g * 128 : OOB, 0, 16); }
    };
    u32x4 cq[8][4];
    u32x2 fs;
    const unsigned fo_s = lane < 8 ? ((4 * w + (lane >> 1)) * 8 + 2 * rsel + (lane & 1)) * 128 : OOB;
    sfor<0, 8>([&](auto N) { constexpr int n = N, g0 = hb_g0(n), done = n ? hb_cnt(16 * n - 1, true) : 0;
      if constexpr (n > 0) { if constexpr (n == 1) wait_vm<done>(); STB(30 + 2 * n) bar(); STB(31 + 2 * n) } else STB(31)
      if constexpr (n > 1) { wait_vm<done - hb_cnt(16 * (n > 1 ? n - 2 : 0) + hb_g0(n - 2) + 3, false)>(); rd_c(n, cq[n - 2]); sb(); }
      sfor<0, hb_ops(n)>([&](auto I) { constexpr int i = I, io = i < g0 ? i : i - 4;
        if constexpr (i >= g0 && i < g0 + 4) { if constexpr (i == g0 && n > 1) { for (int s = 0; s < 4; ++s) use(cq[n - 2][s]); } c_op(N, IC(i - g0)); }
        else if constexpr (n >= 6) { constexpr int e = 5 * (n - 6) + (io >> 1); wd_op(IC(e < 4 ? 18 + e : e), IC(e), IC(io & 1)); }
        if constexpr (16 * n + i == PPOLL) ld64_sc1(fs, sws, fo_s, flagb);
        side(IC(16 * n + i));
        sb();
      });
    });
    __builtin_amdgcn_s_setprio(HPC);
    STAMP(3)
    auto poll = [&](unsigned fo, u32x2 fv) {
#pragma unroll 1
      for (unsigned spins = 0; spins < SPIN_MAX; ++spins) {
        const bool ok = fo == OOB || (fv[0] == (unsigned)tag2 && fv[1] == (unsigned)(tag2 >> 32));
        if (__all(ok)) break;
        __builtin_amdgcn_s_sleep(SLP);
        ld64_sc1(fv, sws, fo, flagb); wait_vm<0>(); pin(fv);
      }
    };
    wait_vm<hb_cnt(127, true) - hb_cnt(16 * 6 + 7, false)>();
    rd_c(0, cq[6]);
    const unsigned a2c = WS_A2 + (unsigned)(c * 4 + 2 * rsel) * (NKS * 1024);
    const i32x4 sg0 = mk_srd(ws + a2c + (64 + kb) * 1024, cnt * 1024), sg1 = mk_srd(ws + a2c + (NKS + 64 + kb) * 1024, cnt * 1024);
    const unsigned vs0 = va + a2c + w * 8192, vs1 = vs0 + NKS * 1024;
    wait_lgkm0();
    dma_wd(IC(16), IC(16)); dma_wd(IC(17), IC(17));
    wait_cnt();
    ld64_sc1(fs, sws, fo_s, flagb);
    sfor<10, 13>([&](auto E) { dma_wd(E, E); });
    wait_vm<6>(); pin(fs);
    STAMP(7)
    poll(fo_s, fs);
    STAMP(4)
    unsigned tb2[4][2];
    auto rd_half = [&](int e, int cb) { const int k = cell_k(e) == 0 ? (cell_c(e) >= 5 * SLT) : cell_k(e) + 1;
      return lds_tr2(tb2[k][cb] + (cell_c(e) - (k == 0 ? 3 * SLT : k == 1 ? 5 * SLT : k == 3 ? OFF_B : 0)), 512); };
    f32x16 acc2[2][2];
    u32x4 L[76], wq[4][2], bf[2][2];
    unsigned so_l = 0;
    auto soL = [&](auto M) { constexpr int m = M;
      if constexpr (m < 16) { if constexpr (!(m & 3)) { const int kg = ((m >> 2) + lnd(xcd)) & 3; so_l = ((kg >> 1) * 32 + (kg & 1) * 4) * 1024; } }
      else so_l = kofs(m - 16, 1024); };
    auto ldH = [&](auto M, auto R) { constexpr int m = M, r = R;
      if constexpr (m < 16) ld128_sc1<(m & 3) * 1024>(L[2 * m + r], sws, r ? vs1 : vs0, so_l); else ld128_sc1<0>(L[2 * m + r], r ? sg1 : sg0, va, so_l); };
    auto ldL = [&](auto M) { soL(M); ldH(M, IC(0)); ldH(M, IC(1)); };
    sfor<0, p2_l0(0)>(ldL);
    sfor<0, 38>([&](auto M) { constexpr int m = M, mm = m - 16;
      constexpr bool one = p2_l0(m + 1) == p2_l0(m) + 1;
      wait_vm<p2_after(m)>(); pin(L[2 * m]); pin(L[2 * m + 1]);
      sfor<0, 4>([&](auto G) { constexpr int gp = G, cb = gp >> 1, rb = gp & 1;
        if constexpr (m < 16) acc2[cb][rb] = mfma(cq[2 * (m >> 2) + cb][m & 3], L[2 * m + rb], m == 0 ? (f32x16){} : acc2[cb][rb]);
        else acc2[cb][rb] = mfma(mm < 18 ? bf[mm & 1][cb] : wq[mm < 18 ? 0 : mm - 18][cb], L[2 * m + rb], acc2[cb][rb]);
        sb();
        if constexpr (gp < 2) {
          if constexpr (m >= 15 && mm + 1 < 18) bf[(mm + 1) & 1][gp] = rd_half((mm + 5) % 18, gp);
          if constexpr (m == 0 || m == 1) wd_op(IC(13 + m), IC(13 + m), G);
          if constexpr (m == 2 || m == 3) { constexpr int k = 2 * (m - 2) + gp;
            for (int u = 0; u < 2; ++u) { tb2[k][u] = (tdl ^ (u * 64)) + (k == 0 ? 3 * SLT + w6 : k == 1 ? 5 * SLT + w6 : k == 2 ? w4 : OFF_B + w2); pin(tb2[k][u]); } }
          if constexpr (m == 4 && gp == 0) rd_c(1, cq[7]);
          if constexpr (m == 5) { if constexpr (gp == 0) { for (int s = 0; s < 4; ++s) use(cq[7][s]); } wd_op(IC(15), IC(15), G); }
          if constexpr (m >= 7 && m <= 10) { if constexpr (gp == 0) { use(wq[m - 7][0]); use(wq[m - 7][1]); } wd_op(IC(m - 7), IC(m - 7), G); }
          if constexpr (m >= 6 && m <= 9) wq[m - 6][gp] = rd_half(m - 6, gp);
          if constexpr (gp == 1 && m == PCK - 1) ld64_sc1(fs, sws, fo_g, flagb);
          if constexpr (gp == 1 && one) soL(IC(p2_l0(m)));
        } else {
          if constexpr (gp == 2 && m == PCK) { STAMP(5) wait_vm<p2_extra(PCK) + 2 * (16 - p2_l0(PCK - 1))>(); pin(fs); poll(fo_g, fs); STAMP(6) }
          if constexpr (one) ldH(IC(p2_l0(m)), IC(gp - 2));
          else if constexpr (gp == 2) sfor<p2_l0(m), p2_l0(m + 1)>(ldL);
        }
        sb();
      });
    });
    STAMP(9)
    for (int rb = 0; rb < 2; ++rb) for (int rq = 0; rq < 4; ++rq) for (int cb = 0; cb < 2; ++cb)
      *(LAS f32x4*)(size_t)((pa[rb] ^ (rq * 32)) + cb * 128) =
          (f32x4){acc2[cb][rb][4 * rq], acc2[cb][rb][4 * rq + 1], acc2[cb][rb][4 * rq + 2], acc2[cb][rb][4 * rq + 3]};
    wait_lgkm0(); bar();
    STAMP(11)
  }
  STAMP(12)
  STAMP_OUT
  asm volatile(".Lpf_end:");
}

extern "C" void kernel_launch(void* const* d_in, const int* in_sizes, int n_in, void* d_out, int out_size, void* d_ws, size_t ws_size,
                              hipStream_t stream) {
  (void)hipFuncSetAttribute((const void*)fused, hipFuncAttributeMaxDynamicSharedMemorySize, LDS_BYTES);
  fused<<<256, 512, LDS_BYTES, stream>>>((const u16*)d_in[0], (const u16*)d_in[1], (const u16*)d_in[2], (const u16*)d_in[3], (const u16*)d_in[4],
                                         (const u16*)d_in[5], (const u16*)d_in[6], (const u16*)d_in[7], (const u16*)d_in[8], (const u16*)d_in[10],
                                         (const u16*)d_in[11], (const u16*)d_in[12], (char*)d_ws, (float*)d_out);
}
```

```cpp
#define OAUX 19
#include <hip/hip_runtime.h>
#include <hip/hip_bf16.h>
#include <type_traits>

typedef unsigned short u16;
typedef unsigned long long u64;
typedef short s16x4 __attribute__((ext_vector_type(4)));
typedef __bf16 bf16x2 __attribute__((ext_vector_type(2)));
typedef __bf16 bf16x8 __attribute__((ext_vector_type(8)));
typedef float f32x2 __attribute__((ext_vector_type(2)));
typedef float f32x4 __attribute__((ext_vector_type(4)));
typedef float f32x16 __attribute__((ext_vector_type(16)));
typedef unsigned u32x2 __attribute__((ext_vector_type(2)));
typedef unsigned u32x4 __attribute__((ext_vector_type(4)));
typedef int i32x4 __attribute__((ext_vector_type(4)));
#define LAS __attribute__((address_space(3)))
#define DEVI __device__ __forceinline__

constexpr int SL = 2048, SD = 512, SP = 512, SF = 1376;
constexpr int NKS = 150;
constexpr unsigned WS_E1 = 0;
constexpr unsigned WS_E2 = 2048;
constexpr unsigned WS_GRAN = 262144;
constexpr unsigned WS_FLAG = 524288;
constexpr unsigned WS_A2 = 786432;
constexpr unsigned A2_BYTES = 64u * NKS * 1024u;
constexpr unsigned WS_BYTES = WS_A2 + A2_BYTES;
constexpr unsigned OOB = 0x40000000u;
constexpr int SLT = 24832;
constexpr int OFF_B = 16384, OFF_WU = 12288 + 32;
constexpr int MISC = 6 * SLT;
constexpr int M_ST = MISC, M_SD = MISC + 1024, M_CNT = MISC + 2048, M_CS = MISC + 2304, M_T = MISC + 3840;
constexpr int LDS_BYTES = 163840;
constexpr unsigned SPIN_MAX = 1u << 16;
constexpr int cell_k(int e) { return e >= 15 ? 2 : (e % 5) < 3 ? 0 : 1; }
constexpr int cell_c(int e) { return e >= 15 ? ((e - 13) % 3) * SLT + OFF_B : (e % 5) < 3 ? (3 + (e / 5 + 2) % 3) * SLT + (e % 5) * 2048 : ((e / 5 + 2) % 3) * SLT + (e % 5 - 3) * 2048; }

#define STAMP_DECL
#define STAMP(i) {}
#define STAMP_OUT
#define STB(i) {}
template <int B, int E, class Fn> DEVI void sfor(Fn&& f) {
  if constexpr (B < E) { f(std::integral_constant<int, B>{}); sfor<B + 1, E>(f); }
}
DEVI i32x4 mk_srd(const void* p, unsigned bytes) {
  u64 a = (u64)p; return (i32x4){(int)(unsigned)a, (int)((unsigned)(a >> 32) & 0xffffu), (int)bytes, 0x00020000};
}
DEVI void dma(i32x4 srd, unsigned voff, unsigned soff, unsigned lds) {
  asm volatile("s_mov_b32 m0, %0\n\ts_nop 0\n\tbuffer_load_dwordx4 %1, %2, %3 offen lds" ::"s"(lds), "v"(voff), "s"(srd), "s"(soff) : "memory");
}
template <int OFF> DEVI void ld128_sc1(u32x4& d, i32x4 srd, unsigned voff, unsigned soff) {
  asm volatile("buffer_load_dwordx4 %0, %1, %2, %3 offen offset:%4 sc1" : "=v"(d) : "v"(voff), "s"(srd), "s"(soff), "n"(OFF) : "memory");
}
template <int OFF> DEVI void ld128_sc1_rw(u32x4& d, i32x4 srd, unsigned voff, unsigned soff) {
  asm volatile("buffer_load_dwordx4 %0, %1, %2, %3 offen offset:%4 sc1" : "+v"(d) : "v"(voff), "s"(srd), "s"(soff), "n"(OFF) : "memory");
}
DEVI void ld64_sc1(u32x2& d, i32x4 srd, unsigned voff, unsigned soff) {
  asm volatile("buffer_load_dwordx2 %0, %1, %2, %3 offen sc1" : "=v"(d) : "v"(voff), "s"(srd), "s"(soff) : "memory");
}
DEVI void ld64(u32x2& d, i32x4 srd, unsigned voff, unsigned soff) {
  asm volatile("buffer_load_dwordx2 %0, %1, %2, %3 offen" : "=v"(d) : "v"(voff), "s"(srd), "s"(soff) : "memory");
}
DEVI void ld16(unsigned& d, i32x4 srd, unsigned voff) {
  asm volatile("buffer_load_ushort %0, %1, %2, 0 offen" : "=v"(d) : "v"(voff), "s"(srd) : "memory");
}
template <int N> DEVI void wait_vm() { asm volatile("s_waitcnt vmcnt(%0)" ::"n"(N) : "memory"); }
template <class T> DEVI void pin(T& a) { asm volatile("" : "+v"(a)::"memory"); }
template <class T> DEVI void use(const T& a) { asm volatile("" ::"v"(a) : "memory"); }
DEVI int lnd(int a) { asm volatile("" : "+s"(a)); return a; }
DEVI int ftid() { int t = threadIdx.x; asm volatile("" : "+v"(t)); return t; }
DEVI void wait_lgkm0() { asm volatile("s_waitcnt lgkmcnt(0)" ::: "memory"); }
DEVI void bar() { asm volatile("s_barrier" ::: "memory"); }
DEVI void sb() { __builtin_amdgcn_sched_barrier(0); }
DEVI float lo2f(unsigned u) { return __uint_as_float(u << 16); }
DEVI float hi2f(unsigned u) { return __uint_as_float(u & 0xffff0000u); }
DEVI unsigned pk2(float a, float b) { f32x2 v = {a, b}; return __builtin_bit_cast(unsigned, __builtin_convertvector(v, bf16x2)); }
DEVI float rbf(float a) { return lo2f(pk2(a, a)); }
DEVI u32x4 lds_r128(unsigned a) { return *(LAS u32x4*)(size_t)a; }
DEVI u32x2 lds_tr(unsigned a) { return __builtin_bit_cast(u32x2, __builtin_amdgcn_ds_read_tr16_b64_v4i16((LAS s16x4*)(size_t)a)); }
DEVI u32x4 lds_tr2(unsigned a, unsigned d) { u32x2 x = lds_tr(a), y = lds_tr(a + d); return (u32x4){x[0], x[1], y[0], y[1]}; }
DEVI f32x16 mfma(u32x4 a, u32x4 b, f32x16 c) {
  return __builtin_amdgcn_mfma_f32_32x32x16_bf16(__builtin_bit_cast(bf16x8, a), __builtin_bit_cast(bf16x8, b), c, 0, 0, 0);
}
DEVI float dot2(unsigned a, unsigned b, float c) {
  return __builtin_amdgcn_fdot2_f32_bf16(__builtin_bit_cast(bf16x2, a), __builtin_bit_cast(bf16x2, b), c, false);
}
DEVI void swap16(float& a, float& b) { auto r = __builtin_amdgcn_permlane16_swap(__float_as_uint(a), __float_as_uint(b), false, false); a = __uint_as_float(r[0]); b = __uint_as_float(r[1]); }
DEVI void swap32(float& a, float& b) { auto r = __builtin_amdgcn_permlane32_swap(__float_as_uint(a), __float_as_uint(b), false, false); a = __uint_as_float(r[0]); b = __uint_as_float(r[1]); }
DEVI float sum32(float v) { float a = v, b = v; swap32(a, b); return a + b; }
DEVI float sum16(float v) { float a = v, b = v; swap16(a, b); return a + b; }
DEVI void cmul(float& dr, float& di, float ar, float ai, float br, float bi) { float t = ar * br - ai * bi; di = ar * bi + ai * br; dr = t; }
DEVI void cfma(float& hr, float& hi, float ar, float ai, float br, float bi) {
  float t = __builtin_fmaf(ar, hr, __builtin_fmaf(-ai, hi, br)); hi = __builtin_fmaf(ar, hi, __builtin_fmaf(ai, hr, bi)); hr = t;
}

template <int OFF> DEVI void dmao(i32x4 srd, unsigned voff, unsigned soff) {
  asm volatile("buffer_load_dwordx4 %0, %1, %2 offen offset:%3 lds" ::"v"(voff), "s"(srd), "s"(soff), "n"(OFF) : "memory");
}
DEVI void setm0(unsigned lds) { asm volatile("s_mov_b32 m0, %0\n\ts_nop 0" ::"s"(lds) : "memory"); }
DEVI void touch(i32x4 srd, unsigned voff, unsigned soff) {
  asm volatile("buffer_load_dword %0, %1, %2 offen lds" ::"v"(voff), "s"(srd), "s"(soff) : "memory");
}
DEVI f32x4 mfma16(u32x4 a, u32x4 b, f32x4 c) {
  return __builtin_amdgcn_mfma_f32_16x16x32_bf16(__builtin_bit_cast(bf16x8, a), __builtin_bit_cast(bf16x8, b), c, 0, 0, 0);
}
DEVI float vadd(float a, float b) { float r; asm("v_add_f32 %0, %1, %2" : "=v"(r) : "v"(a), "v"(b)); return r; }
DEVI float vfma(float a, float b, float c) { float r; asm("v_fma_f32 %0, %1, %2, %3" : "=v"(r) : "v"(a), "v"(b), "v"(c)); return r; }
DEVI f32x2 pfma(f32x2 a, f32x2 b, f32x2 c) { return __builtin_elementwise_fma(a, b, c); }
DEVI f32x2 sp(float a) { return (f32x2){a, a}; }
DEVI void cfma2(f32x2& hr, f32x2& hi, f32x2 a, f32x2 br, f32x2 bi) {
  f32x2 t1, t2;
  asm("v_pk_fma_f32 %2, %4, %1, %5 op_sel:[1,0,0] op_sel_hi:[1,1,1] neg_lo:[1,0,0] neg_hi:[1,0,0]\n\t"
      "v_pk_fma_f32 %3, %4, %0, %6 op_sel:[1,0,0] op_sel_hi:[1,1,1]\n\t"
      "v_pk_fma_f32 %0, %4, %0, %2 op_sel:[0,0,0] op_sel_hi:[0,1,1]\n\t"
      "v_pk_fma_f32 %1, %4, %1, %3 op_sel:[0,0,0] op_sel_hi:[0,1,1]"
      : "+v"(hr), "+v"(hi), "=&v"(t1), "=&v"(t2) : "v"(a), "v"(br), "v"(bi));
}
DEVI void cout2(unsigned& o0, unsigned& o1, f32x2 k, f32x2 gr, f32x2 gi) {
  f32x2 t1, t2;
  asm("v_pk_mul_f32 %0, %2, %4 op_sel:[1,0] op_sel_hi:[1,1]\n\t"
      "v_pk_mul_f32 %1, %2, %3 op_sel:[1,0] op_sel_hi:[1,1]\n\t"
      "v_pk_fma_f32 %0, %2, %3, %0 op_sel:[0,0,0] op_sel_hi:[0,1,1] neg_lo:[0,0,1] neg_hi:[0,0,1]\n\t"
      "v_pk_fma_f32 %1, %2, %4, %1 op_sel:[0,0,0] op_sel_hi:[0,1,1] neg_lo:[1,0,1] neg_hi:[1,0,1]"
      : "=&v"(t1), "=&v"(t2) : "v"(k), "v"(gr), "v"(gi));
  o0 = pk2(t1[0], t1[1]); o1 = pk2(t2[0], t2[1]);
}
#define SLP 4
#define WN 10
#define PGV 50
#define PCV 64
#define P2D 16
#define PTV 88
#define PSV 89
#define PFV 114
#define PFW 1
#define PFW_S 2
#define PFW_O 0
#define PFX 0
#define ROTX (2 * (xcd >> 1) + 4 * (xcd & 1))
#define PLATE 0
#define AVAR 0
#define STA(i) {}
#define HPA 0
#define WEARLY 0
#define PFW0 0
#define HPB 0
#define HPC 0
#define PFG 0
#define PFG_O 0
#define PCK 13
#define WG 6
#define EPK 0
#define CPE 0
#define PFT 1
#define LEADX (c + g)
#define LEAD 1
#define IC(x) std::integral_constant<int, (x)>{}
constexpr int p2_extra(int m) { return ((m == 0 || m == 1 || m == 5 || (m >= 7 && m <= 10)) ? 2 : 0) + (m == PCK - 1 ? 1 : 0); }
constexpr int p2_l0(int m) { return m <= PCK ? (m + WN / 2 < 16 ? m + WN / 2 : 16) : (m + WG < 38 ? m + WG : 38); }
constexpr int p2_after(int m) {
  int pos = 0, posL[40] = {};
  for (int k = 0; k < p2_l0(0); ++k) { pos += 2; posL[k] = pos; }
  for (int s = 0; s < m; ++s) { pos += p2_extra(s); for (int k = p2_l0(s); k < p2_l0(s + 1); ++k) { pos += 2; posL[k] = pos; } }
  return pos - posL[m];
}
constexpr int PPUB = 25, PG = PGV, PC = PCV, P2 = PC + P2D, PT = PTV, PS = PSV, PF = PFV, PPOLL = -1;
constexpr int hb_ops(int n) { return n < 6 ? 10 : 14; }
constexpr int hb_g0(int n) { return n < 6 ? 3 : 4; }
constexpr int hb_extra(int P) { return P == PPUB ? 1 : P == PG ? 8 : P == PS ? 4 : P == PF ? 1 : P == PPOLL ? 1 : 0; }
constexpr int hb_cnt(int P, bool with_extra) {
  int t = 0;
  for (int n = 0; n < 8; ++n) for (int i = 0; i < hb_ops(n); ++i) { const int p = 16 * n + i; if (p > P) return t; t += (n >= 6 || (i >= hb_g0(n) && i < hb_g0(n) + 4)); if (p < P || with_extra) t += hb_extra(p); }
  return t;
}
constexpr int nxt(int P, int d) { for (; d > 0; --d) { ++P; if (P % 16 >= hb_ops(P / 16)) P = (P / 16 + 1) * 16; } return P; }
DEVI u64 utag(u32x2 e) { const u64 t = (((u64)(unsigned)__builtin_amdgcn_readfirstlane(e[1]) << 32) | (unsigned)__builtin_amdgcn_readfirstlane(e[0])) + 1; return t ? t : 1; }
template <int CTRL> DEVI float dppf(float v) { return __int_as_float(__builtin_amdgcn_update_dpp(0, __float_as_int(v), CTRL, 0xf, 0xf, true)); }
DEVI void sline(u64 base, unsigned off) {
  asm volatile("s_load_dwordx2 s[98:99], %0, %1" ::"s"(base), "s"(off + 60) : "memory", "s98", "s99");
}
template <int IMM> DEVI void setm0i(unsigned base) { asm volatile("s_add_i32 m0, %0, %1\n\ts_nop 0" ::"s"(base), "n"(IMM) : "memory", "scc"); }
DEVI void miss2(unsigned& n, u32x4 a, u32x4 b, unsigned tl, unsigned th) {
  unsigned t, u;
  asm("v_xor_b32 %1, %7, %3\n\tv_xor_b32 %2, %7, %5\n\tv_bitop3_b32 %1, %4, %8, %1 bitop3:0xbe\n\tv_bitop3_b32 %2, %6, %8, %2 bitop3:0xbe\n\t"
      "v_min_u32 %1, 1, %1\n\tv_min_u32 %2, 1, %2\n\tv_add3_u32 %0, %0, %1, %2"
      : "+v"(n), "=&v"(t), "=&v"(u) : "v"(a[1]), "v"(a[3]), "v"(b[1]), "v"(b[3]), "s"(tl), "s"(th));
}
DEVI unsigned geglu2(float g0, float g1, float u0, float u1, float k1, float k3, float s2) {
  float a, b;
  asm("v_mul_f32 %0, %4, %4\n\tv_mul_f32 %1, %5, %5\n\tv_fma_f32 %0, %0, %7, %6\n\tv_fma_f32 %1, %1, %7, %6\n\t"
      "v_mul_f32 %0, %0, %4\n\tv_mul_f32 %1, %1, %5\n\tv_exp_f32 %0, %0\n\tv_exp_f32 %1, %1\n\tv_mul_f32 %2, %2, %4\n\t"
      "v_fma_f32 %0, %0, %8, %8\n\tv_fma_f32 %1, %1, %8, %8\n\tv_rcp_f32 %0, %0\n\tv_rcp_f32 %1, %1\n\tv_mul_f32 %3, %3, %5\n\t"
      "v_mul_f32 %0, %0, %2\n\tv_mul_f32 %1, %1, %3\n\tv_cvt_pk_bf16_f32 %0, %0, %1"
      : "=&v"(a), "=&v"(b), "+v"(u0), "+v"(u1) : "v"(g0), "v"(g1), "v"(k1), "v"(k3), "v"(s2));
  return __float_as_uint(a);
}
DEVI void cf4(float& hr, float& hi, float ar, float ai, float br, float bi) {
  float t, u;
  asm("v_fma_f32 %0, -%5, %3, %6\n\tv_fma_f32 %1, %5, %2, %7\n\tv_fmac_f32 %0, %4, %2\n\tv_fmac_f32 %1, %4, %3"
      : "=&v"(t), "=&v"(u) : "v"(hr), "v"(hi), "v"(ar), "v"(ai), "v"(br), "v"(bi));
  hr = t; hi = u;
}
DEVI void csq(float& dr, float& di, float r, float i) {
  float t, u;
  asm("v_mul_f32 %0, %2, %2\n\tv_mul_f32 %1, %2, %3\n\tv_fma_f32 %0, -%3, %3, %0\n\tv_add_f32 %1, %1, %1" : "=&v"(t), "=&v"(u) : "v"(r), "v"(i));
  dr = t; di = u;
}
DEVI void cf4c(float hr, float hi, float ar, float ai, float nai, float& br, float& bi) {
  asm("v_fmac_f32 %0, %5, %3\n\tv_fmac_f32 %1, %6, %2\n\tv_fmac_f32 %0, %4, %2\n\tv_fmac_f32 %1, %4, %3"
      : "+v"(br), "+v"(bi) : "v"(hr), "v"(hi), "v"(ar), "v"(ai), "v"(nai));
}
DEVI void sc4(float& r, float& i, float rs, float rc, float csr, float csi) {
  asm("v_mul_f32 %0, %2, %0\n\tv_mul_f32 %1, %2, %1\n\tv_fmac_f32 %0, %3, %4\n\tv_fmac_f32 %1, %3, %5"
      : "+v"(r), "+v"(i) : "v"(rs), "v"(rc), "v"(csr), "v"(csi));
}
DEVI void kc4(float& r, float& i, float kr, float nkr, float nki) {
  float t, u;
  asm("v_mul_f32 %0, %4, %2\n\tv_mul_f32 %1, %5, %3\n\tv_fmac_f32 %0, %6, %3\n\tv_fmac_f32 %1, %6, %2"
      : "=&v"(t), "=&v"(u) : "v"(r), "v"(i), "v"(kr), "v"(nkr), "v"(nki));
  r = t; i = u;
}

__global__ __launch_bounds__(512, 2) void fused(const u16* __restrict__ x, const u16* __restrict__ lam_re, const u16* __restrict__ lam_im,
                                                const u16* __restrict__ Bre, const u16* __restrict__ Bim, const u16* __restrict__ Cre,
                                                const u16* __restrict__ Cim, const u16* __restrict__ logdt, const u16* __restrict__ dskip,
                                                const u16* __restrict__ wg, const u16* __restrict__ wu, const u16* __restrict__ wd,
                                                char* __restrict__ ws, float* __restrict__ out) {
  extern __shared__ __attribute__((aligned(16))) char shm[];
  asm volatile(".Lpf_beg:");
  STAMP_DECL STAMP(0)
  unsigned perm;
  const i32x4 sx = mk_srd(x, SL * SD * 2);
  {
    const int b0 = blockIdx.x, t = threadIdx.x, lead = ((b0 >> 6) + (b0 >> 3)) & 3, rot = (b0 * 5) & 6;
    perm = (0x76543210u - (15u << (4 * lead)) * (1u + ((((lead + 1) >> 2) ^ 1) << 16)) + rot * 0x11111111u) & 0x77777777u;
    const unsigned v0 = (((t >> 2) & 64) + ((t >> 3) & 16) + ((t >> 4) & 5) + (t & 40)) * 1024 + (((t & 7) ^ ((t >> 4) & 3)) * 16);
    const unsigned so = (((b0 & 6) * 2 + (b0 >> 6)) << 17) + (perm & 7) * 128;
    asm volatile("" ::"s"(lam_re), "s"(lam_im), "s"(Bre), "s"(Bim), "s"(Cre), "s"(Cim), "s"(logdt), "s"(dskip), "s"(wg), "s"(wu), "s"(wd), "s"(ws), "s"(out));
    const int j0 = __builtin_amdgcn_readfirstlane(t >> 6);
    const unsigned v1 = (v0 ^ 64) + 1024;
    setm0i<SLT>(j0 * 2048); dmao<0>(sx, v0, so); dmao<1024>(sx, v1, so);
    const i32x4 sb0 = mk_srd((j0 >> 2) ? Bim : Bre, SP * SD * 2);
    const unsigned vb0 = ((t >> 3) & 31) * 1024 + (((t & 7) ^ ((t >> 4) & 7)) * 16), sob = (((b0 & 1) * 8 + ((b0 >> 3) & 7)) << 15);
    setm0i<SLT + OFF_B>(j0 * 1024); dmao<0>(sb0, vb0, sob + (perm & 7) * 128);
    const unsigned k1 = ((perm >> 4) & 7) * 128;
    setm0i<2 * SLT>(j0 * 2048); dmao<0>(sx, v0, (so & ~1023u) + k1); dmao<1024>(sx, v1, (so & ~1023u) + k1);
    setm0i<2 * SLT + OFF_B>(j0 * 1024); dmao<0>(sb0, vb0, sob + k1);
    { const unsigned ix = b0 >> 3, q4 = (b0 >> 1) & 3;
      const char* p0 = (j0 & 1) ? (const char*)wu : (const char*)wg;
      const char* p1 = (j0 & 1) ? ((ix & 16) ? (const char*)Cim : (const char*)Cre) : (const char*)wd;
      const char* p2 = (j0 & 1) ? (const char*)ws : (const char*)out + q4 * 1048576;
      const char* p3 = (const char*)ws + WS_A2 + (q4 * 2 + (j0 & 1)) * (8 * NKS * 1024);
      const char* pb = (j0 & 4) ? ((j0 & 2) ? p3 : p2) : ((j0 & 2) ? p1 : p0);
      const unsigned off = j0 == 3 ? (ix & 15) * 32768 : ix * (j0 < 3 ? 44032 : j0 == 4 ? 32768 : j0 == 5 ? 24576 : 38400);
      sline((u64)pb, off); }
  }
  auto chunk = [&](int v) { return (perm >> (4 * v)) & 7; };
  const int j = __builtin_amdgcn_readfirstlane(ftid() >> 6), w = j & 3, sb_ = w & 1, pi = w >> 1;
  const int b = lnd(blockIdx.x), xcd = b & 7, idx = b >> 3;
  const int c = (xcd >> 1) * 4 + (idx >> 3), rsel = xcd & 1, gi = idx & 7, g = rsel * 8 + gi;
  auto get_fb = [&]() { const int l = (lnd(gi) + 1) >> 3; return (6 * (g - rsel)) * (1 - l) + (84 + rsel) * l; };
  auto get_ct = [&]() { const int slow = (3 - lnd((int)((u64)wd >> 7))) & 7; return lnd(gi == 7 ? slow : gi == slow ? 7 : gi); };
  const i32x4 sws = mk_srd(ws, WS_BYTES);
  const __amdgpu_buffer_rsrc_t rws = __builtin_amdgcn_make_buffer_rsrc(ws, 0, WS_BYTES, 0x00020000);
  const unsigned flagb = WS_FLAG + c * 16384;
  const unsigned a2pb = WS_A2 + (unsigned)(c * 4 + 2 * pi) * (NKS * 1024);
  const unsigned w4 = w * 4096, w2 = w * 2048, w6 = w * 6144;
  const unsigned so_x = (c * 128 + 64 * (w >> 1) + 16 * (w & 1)) * 1024;
  u64 tag2 = 0;
  auto set_flag = [&](int fi) {
    __builtin_amdgcn_raw_buffer_store_b64((u32x2){(unsigned)tag2, (unsigned)(tag2 >> 32)}, rws, (ftid() & 63) == 0 ? flagb + (g * 8 + fi) * 128 : OOB, 0, 16);
  };
  auto wait_cnt = [&]() {
#pragma unroll 1
    for (unsigned spins = 0; spins < SPIN_MAX; ++spins)
      if (__builtin_amdgcn_readfirstlane(__hip_atomic_load((LAS unsigned*)(size_t)M_CNT, __ATOMIC_RELAXED, __HIP_MEMORY_SCOPE_WORKGROUP)) == 4u) break;
    asm volatile("" ::: "memory");
  };

  if (j < 4) {
    const int lane = ftid() & 63, l5 = lane & 31, h = lane >> 5, blk = (lane >> 4) & 1, q = (lane & 15) >> 2, pp = lane & 3;
    unsigned vx[4], vw[3], ra[4], wt[2];
    const i32x4 sw = mk_srd((w >> 1) ? wu : wg, SD * SF * 2);
    const unsigned ld_w = ((w >> 1) ? OFF_WU : 0) + (w & 1) * 6144;
    unsigned so_w = 0;
    u32x4 sel;
    {
      const unsigned sw0 = ((lane & 7) ^ (lane >> 4)) * 16;
      for (int r = 0; r < 4; ++r) vx[r] = (32 * (lane >> 5) + 8 * ((lane >> 3) & 1) + ((lane >> 4) & 1) + r) * 1024 + (sw0 ^ ((r & 1) * 64));
      auto ldX = [&](auto S) { constexpr int s = S;
        const unsigned so = so_x + chunk(s) * 128;
        setm0i<((s + 1) % 6) * SLT>(w4);
        dmao<0>(sx, vx[0], so); dmao<1024>(sx, vx[1], so); dmao<2048>(sx, vx[2], so); dmao<3072>(sx, vx[3], so);
      };
      u32x2 ep2;
      STAMP(1)
      if (w == 0 && lane == 0) *(LAS unsigned*)(size_t)M_CNT = 0;
      const unsigned xa = w * 4096 + lane * 16;
      unsigned ti;
      auto setup = [&]() {
        for (int s = 0; s < 4; ++s) { ra[s] = pi * 8192 + l5 * 128 + (((2 * (s ^ pi) + h) ^ ((l5 >> 1) & 7)) * 16); pin(ra[s]); }
        for (int p = 0; p < 2; ++p) { wt[p] = 3 * SLT + (blk ? OFF_WU : 0) + (8 * h + q) * 192 + (((pp & 1) << 1) | (pp >> 1)) * 8 + sb_ * 96 + (p ? -pi * 3072 : pi * 3072); pin(wt[p]); }
        const unsigned selv = (((lane & 15) == 0 && blk == 0) || ((lane & 15) == 4 && blk == 1)) ? 0x3f803f80u : 0u; sel = (u32x4){selv, selv, selv, selv}; pin(sel);
        ti = (lane & 4) ? M_CNT + 64 - M_ST : (64 * (w >> 1) + 32 * (lane >> 5) + 16 * (w & 1) + 4 * (lane & 3) + ((lane >> 3) & 3)) * 8;
        pin(ti);
      };
      auto setup2 = [&]() { const int q12 = (lane * 43) >> 9, r12 = lane - 12 * q12;
        for (int i = 0; i < 3; ++i) { const int r = r12 + 4 * i, cy = r >= 12, col = r - 12 * cy;
          vw[i] = (gi == 7 && col >= 2) ? OOB : (5 * i + q12 + cy) * (SF * 2) + col * 16 - i * 1024; pin(vw[i]); }
        so_w = (w & 1) * 32 * (SF * 2) + get_fb() * 32; };
      float s1[4] = {}, s2[4] = {};
      auto stats = [&](auto S, auto&& mid) { constexpr int s = S;
        u32x4 xr[4];
        for (int k = 0; k < 4; ++k) xr[k] = lds_r128(((s + 1) % 6) * SLT + k * 1024 + xa);
        sb(); mid(); sb();
        for (int e = 0; e < 4; ++e) for (int i = 0; i < 4; ++i) { const unsigned u = xr[i][e]; s1[i] = dot2(u, 0x3f803f80u, s1[i]); s2[i] = dot2(u, u, s2[i]); }
        for (int i = 0; i < 4; ++i) { pin(s1[i]); pin(s2[i]); }
        sb(); };
      wait_vm<3>(); wait_lgkm0(); STA(46) bar(); STA(47)
      stats(IC(0), [&] { ldX(IC(2)); ld64_sc1(ep2, sws, 0, WS_E2 + c * 128); });
      wait_vm<5>(); STA(48) bar(); STA(49)
      stats(IC(1), [&] { ldX(IC(3)); setup(); });
      sfor<2, 8>([&](auto S) { constexpr int s = S;
        wait_vm<(s < 7 ? 4 : 0)>();
        stats(S, [&] { if constexpr (s < 6) ldX(IC(s + 2)); if constexpr (s == 6) setup2(); });
        if constexpr (s < 7) { STA(46 + 2 * s) bar(); STA(47 + 2 * s) }
      });
      pin(ep2); tag2 = utag(ep2);
      float t1[2], t2[2];
      for (int k = 0; k < 2; ++k) { const bool o = lane & 1;
        t1[k] = (o ? s1[2 * k + 1] : s1[2 * k]) + dppf<0xB1>(o ? s1[2 * k] : s1[2 * k + 1]); t2[k] = (o ? s2[2 * k + 1] : s2[2 * k]) + dppf<0xB1>(o ? s2[2 * k] : s2[2 * k + 1]); }
      const bool o = lane & 2;
      float sm = (o ? t1[1] : t1[0]) + dppf<0x4E>(o ? t1[0] : t1[1]), sq = (o ? t2[1] : t2[0]) + dppf<0x4E>(o ? t2[0] : t2[1]);
      sm += dppf<0x104>(sm); sq += dppf<0x104>(sq);
      const float mean = sm * (1.f / SD);
      const float var = fmaxf(sq * (1.f / SD) - mean * mean, 0.f);
      const float sd = rbf(__builtin_amdgcn_sqrtf(rbf(rbf(var) + 1e-5f)));
      const float r_ = __builtin_amdgcn_rcpf(sd); const unsigned nm = pk2(-mean, -mean);
      *(LAS f32x2*)(size_t)(M_ST + ti) = (f32x2){r_, lo2f(nm) * r_}; *(LAS f32x2*)(size_t)(M_SD + ti) = (f32x2){sd, __uint_as_float(nm)};
    }
    f32x16 acc[3][2];
    f32x4 csg[3] = {};
    u32x4 af[2][2]; u32x2 wf[2][3][2];
    auto rd1 = [&](auto N, auto S, auto I) { constexpr int n = N, s = S, i = I;
      const unsigned a_ = ra[s] + (n % 3) * SLT, t_ = wt[s & 1] + (n % 3) * SLT + s * 3072;
      if constexpr (i == 0) { wf[s & 1][0][0] = lds_tr(t_); wf[s & 1][0][1] = lds_tr(t_ + 768); }
      if constexpr (i == 1) { af[s & 1][0] = lds_r128(a_); af[s & 1][1] = lds_r128(a_ + 4096); }
      if constexpr (i == 2) { wf[s & 1][1][0] = lds_tr(t_ + 32); wf[s & 1][1][1] = lds_tr(t_ + 32 + 768); }
      if constexpr (i == 3) { wf[s & 1][2][0] = lds_tr(t_ + 64); wf[s & 1][2][1] = lds_tr(t_ + 64 + 768); }
    };
    auto W = [&](int p, int k) { return (u32x4){wf[p][k][0][0], wf[p][k][0][1], wf[p][k][1][0], wf[p][k][1][1]}; };
    STAMP(22)
    wait_vm<0>(); wait_lgkm0(); bar();
    STAMP(2)
    sfor<0, 4>([&](auto I) { rd1(IC(0), IC(0), I); });
    sfor<0, 32>([&](auto K) { constexpr int n = K / 4, s = K % 4, nn = (K + 1) / 4, sn = (K + 1) % 4;
      sfor<0, 6>([&](auto I) { constexpr int i = I;
        acc[i >> 1][i & 1] = mfma(W(s & 1, i >> 1), af[s & 1][i & 1], K == 0 ? (f32x16){} : acc[i >> 1][i & 1]);
        sb();
        if constexpr (s == 3 && n < 7) { if constexpr (i == 0) { if constexpr (n >= 1) wait_vm<(n < 6 ? 10 : 0)>(); wait_lgkm0(); STB(32 + 2 * n) bar(); STB(33 + 2 * n) } if constexpr (i >= 1 && i < 5) rd1(IC(nn), IC(sn), IC(i - 1)); }
        else if constexpr (K < 31 && i < 4) rd1(IC(nn), IC(sn), I);
        if constexpr (i >= 4 && K < 23) {
          constexpr int xn = (K + 1) / 4 + 2, kk = (K + 1) % 4, o0 = i == 5 ? (kk == 0 ? 0 : 3 * kk) : (kk == 0 ? 0 : 3 * kk - 1), o1 = i == 5 ? (kk == 3 ? 10 : 3 * kk + 2) : (kk == 0 ? 0 : 3 * kk);
          sfor<o0, o1>([&](auto O) { constexpr int o = O;
            if constexpr (o < 4) { if constexpr (xn > 2) { if constexpr (o == 0) setm0i<(xn % 3) * SLT>(w4); dmao<o * 1024>(sx, vx[o], so_x + chunk((xn + 5) & 7) * 128); } }
            else { constexpr int u = o - 4; if constexpr (u % 3 == 0) setm0i<(3 + xn % 3) * SLT + u * 1024>(ld_w);
              dmao<(u % 3) * 1024>(sw, vw[u % 3], so_w + chunk((xn + 5) & 7) * (64 * SF * 2) + (u / 3) * 16 * (SF * 2)); } }); }
        if constexpr (K == 31 && i == 1) { if (lane < 32) for (int k = 0; k < 3; ++k) *(LAS float*)(size_t)(M_CS + (w * 3 + k) * 128 + lane * 4) = csg[k][0]; }
        if constexpr (K == 31 && i == 4) { wait_lgkm0();
          if (lane == 0) __hip_atomic_fetch_add((LAS unsigned*)(size_t)M_CNT, 1u, __ATOMIC_RELAXED, __HIP_MEMORY_SCOPE_WORKGROUP); }
        sb();
      });
      if constexpr (!(s & 1)) { for (int k = 0; k < 3; ++k) csg[k] = mfma16(sel, W(s & 1, k), csg[k]); sb(); }
    });
    STAMP(8)
    f32x2 st[2], sd[2];
    { const unsigned ia = (64 * pi + 32 * ((lane >> 2) & 1) + 4 * ((lane >> 3) & 3) + (lane & 3)) * 8;
      for (int rb = 0; rb < 2; ++rb) { st[rb] = *(LAS f32x2*)(size_t)(M_ST + ia + rb * 128); sd[rb] = *(LAS f32x2*)(size_t)(M_SD + ia + rb * 128); } }
    wait_cnt();
    for (int k = 0; k < 3; ++k) {
      float cs = csg[k][0] + *(LAS float*)(size_t)(M_CS + ((w ^ 2) * 3 + k) * 128 + l5 * 4);
      cs = h ? 0.f : cs;
      const float c0 = rbf(cs), r1 = cs - c0, c1 = rbf(r1), c2 = r1 - c1;
      const u32x4 cf = {pk2(c0, c1), pk2(c2, 0.f), 0u, 0u};
      for (int rb = 0; rb < 2; ++rb) { const unsigned nm = __float_as_uint(sd[rb][1]); acc[k][rb] = mfma(cf, (u32x4){nm, nm, 0u, 0u}, acc[k][rb]); }
    }
    for (int k = 0; k < 3; ++k) { pin(acc[k][0]); pin(acc[k][1]); }
    asm volatile("s_nop 11" ::: "memory");
    for (int k = 0; k < 3; ++k) { pin(acc[k][0]); pin(acc[k][1]); }
    __builtin_amdgcn_s_setprio(CPE);
    STAMP(7)
    const int fb = get_fb();
    sfor<0, 3>([&](auto K) { constexpr int k = K;
      const unsigned vst = (gi < 7 || 3 * sb_ + k == 0) ? a2pb + lane * 16 + (64 + fb + 3 * sb_ + k) * 1024 : OOB;
      sfor<0, 2>([&](auto R_) { constexpr int rb = R_;
        const float rs = st[rb][0], k1 = -2.302208198f * rs, k3 = -0.1029432397f * rs * rs * rs, s2 = sd[rb][0] * sd[rb][0];
        unsigned o[4];
        for (int i = 0; i < 4; ++i) o[i] = geglu2(acc[k][rb][2 * i], acc[k][rb][2 * i + 1], acc[k][rb][8 + 2 * i], acc[k][rb][9 + 2 * i], k1, k3, s2);
        __builtin_amdgcn_raw_buffer_store_b128((u32x4){o[0], o[1], o[2], o[3]}, rws, vst + rb * NKS * 1024, 0, 16);
      });
    });
    sb();
    STAMP(9)
    wait_vm<0>();
    set_flag(4 + w);
    STAMP(10)
    const int t = ftid(), ch = t & 15, r4 = t >> 4, ct = get_ct();
    const int trow = 64 * rsel + 32 * ((r4 >> 2) & 1) + 8 * (r4 & 1) + 2 * (r4 >> 3) + ((r4 >> 1) & 1);
    const int irow = 64 * rsel + 32 * ((r4 >> 2) & 1) + 4 * (r4 >> 3) + (r4 & 3);
    u32x2 xv[4], dk;
    for (int it = 0; it < 4; ++it) ld64(xv[it], sx, trow * 1024 + ch * 8, c * 131072 + ct * 128 + (4 * (it & 1) + 16 * (it >> 1)) * 1024);
    ld64(dk, mk_srd(dskip, SD * 2), ch * 8, ct * 128);
    wait_vm<0>(); for (int it = 0; it < 4; ++it) pin(xv[it]); pin(dk);
    f32x4 base[4]; unsigned ph[4];
    for (int it = 0; it < 4; ++it) { const f32x2 s2 = *(LAS f32x2*)(size_t)(M_ST + (irow + 8 * (it & 1) + 16 * (it >> 1)) * 8);
      for (int e = 0; e < 4; ++e) { const unsigned xu = xv[it][e >> 1], du = dk[e >> 1];
        const float xe = (e & 1) ? hi2f(xu) : lo2f(xu), de = (e & 1) ? hi2f(du) : lo2f(du);
        base[it][e] = xe + rbf(rbf(__builtin_fmaf(xe, s2[0], s2[1])) * de); }
      const int e = 2 * it + (r4 >> 3), d = (e * 11) >> 5;
      ph[it] = (3 + d) * SLT + (e - 3 * d) * 2048 + (r4 & 7) * 256 + (ch ^ (r4 & 7)) * 16;
      pin(base[it]); pin(ph[it]); }
    float* op = out + (c * 128 + trow) * SD + ct * 64 + ch * 4;
    const __amdgpu_buffer_rsrc_t rout = __builtin_amdgcn_make_buffer_rsrc(out, 0, SL * SD * 4, 0x00020000);
    const unsigned oo = ((c * 128 + trow) * SD + ct * 64 + ch * 4) * 4;
    bar();
    STAMP(11)
    for (int it = 0; it < 4; ++it) {
      f32x4 p[4];
      for (int ww = 0; ww < 4; ++ww) p[ww] = *(LAS f32x4*)(size_t)(ph[it] + ww * 6144);
      __builtin_amdgcn_raw_buffer_store_b128(__builtin_bit_cast(u32x4, base[it] + ((p[0] + p[1]) + (p[2] + p[3]))), rout, oo + (4 * (it & 1) + 16 * (it >> 1)) * SD * 4, 0, OAUX);
    }
    if (g == 0) __builtin_amdgcn_raw_buffer_store_b64((u32x2){(unsigned)tag2, (unsigned)(tag2 >> 32)}, rws, t == 0 ? WS_E2 + c * 128 : OOB, 0, 16);
  } else {
    const int lane = ftid() & 63, l5 = lane & 31, h = lane >> 5, blk = (lane >> 4) & 1, q = (lane & 15) >> 2, pp = lane & 3;
    const unsigned va = lane * 16;
    const unsigned sw0 = ((lane & 7) ^ (lane >> 4)) * 16;
    unsigned vb[2], vw[3], vd[2];
    for (int r = 0; r < 2; ++r) vb[r] = ((lane >> 3) + 7 * r) * 1024 + (sw0 ^ (r * 64));
    const i32x4 sbm = mk_srd((w >> 1) ? Bim : Bre, SP * SD * 2);
    const unsigned so_b = (g * 32 + (w & 1) * 16) * 1024;
    auto ldB = [&](auto S) { constexpr int s = S;
      const unsigned so = so_b + chunk(s) * 128;
      setm0i<((s + 1) % 6) * SLT + OFF_B>(w2); dmao<0>(sbm, vb[0], so); dmao<1024>(sbm, vb[1], so); };
    u32x2 ep2, ep1; unsigned plr, pli, pdt;
    ld64_sc1(ep2, sws, 0, WS_E2 + c * 128);
    ld64_sc1(ep1, sws, 0, WS_E1 + g * 128);
    const int ps = (lane & 3) | ((lane & 4) << 1) | ((lane & 8) >> 1);
    { const unsigned po = (g * 32 + sb_ * 16 + ps) * 2;
      ld16(plr, mk_srd(lam_re, SP * 2), po); ld16(pli, mk_srd(lam_im, SP * 2), po); ld16(pdt, mk_srd(logdt, SP * 2), po); }
    { u64 pc; unsigned len, back;
      asm volatile("s_getpc_b64 %0\n.Lpf_here:\n\ts_mov_b32 %1, .Lpf_end-.Lpf_beg\n\ts_mov_b32 %2, .Lpf_here-.Lpf_beg" : "=s"(pc), "=s"(len), "=s"(back));
      for (unsigned o = (idx * 4 + w) * 128; o + 128 < len; o += 16384) sline((pc - back) & ~127ull, o + 128); }
    const i32x4 sw = mk_srd((w >> 1) ? wu : wg, SD * SF * 2);
    const unsigned so_w = (w & 1) * 32 * (SF * 2) + get_fb() * 32;
    const unsigned ld_w = ((w >> 1) ? OFF_WU : 0) + (w & 1) * 6144;
    auto opW = [&](auto N, auto I) { constexpr int n = N, i = I;
      if constexpr (i % 3 == 0) setm0i<(3 + n % 3) * SLT + i * 1024>(ld_w);
      dmao<(i % 3) * 1024>(sw, vw[i % 3], so_w + chunk((n + 5) & 7) * (64 * SF * 2) + (i / 3) * 16 * (SF * 2)); };
    unsigned ra[4], wa[4];
    for (int s = 0; s < 4; ++s) { ra[s] = pi * 8192 + l5 * 128 + (((2 * s + h) ^ ((l5 >> 1) & 7)) * 16);
      const int row = sb_ * 16 + ps; wa[s] = OFF_B + blk * 4096 + row * 128 + (((2 * s + h) ^ ((row >> 1) & 7)) * 16); }
    const unsigned selv = (((lane & 15) == 0 && blk == 0) || ((lane & 15) == 4 && blk == 1)) ? 0x3f803f80u : 0u;
    const u32x4 sel = {selv, selv, selv, selv};
    for (int s = 0; s < 4; ++s) { pin(ra[s]); pin(wa[s]); }
    unsigned t1l = 0, t1h = 0;
    const int cc = 2 * c + pi;
    float ar, ai, nai, kr, nkr, nki, a8r, a8i, a16r, a16i, a32r, a32i, a64r, a64i, pwr = 1.f, pwi = 0.f, a256r, a256i, mr = 1.f, mi = 0.f;
    u32x4 oh[2];
    float lr, li;
    auto params = [&](auto V) { constexpr int v = V;
      if constexpr (v == 1) { const int q12 = (lane * 43) >> 9, r12 = lane - 12 * q12;
        for (int i = 0; i < 3; ++i) { const int r = r12 + 4 * i, cy = r >= 12, col = r - 12 * cy;
          vw[i] = (gi == 7 && col >= 2) ? OOB : (5 * i + q12 + cy) * (SF * 2) + col * 16 - i * 1024; pin(vw[i]); } }
      if constexpr (v == 2) {
        pin(ep2); pin(ep1); pin(plr); pin(pli); pin(pdt);
        tag2 = utag(ep2);
        const u64 tag1 = utag(ep1); t1l = (unsigned)tag1; t1h = (unsigned)(tag1 >> 32); }
      if constexpr (v == 3) {
        lr = lo2f(plr); li = lo2f(pli);
        const float dt = rbf(__expf(lo2f(pdt)));
        const float er = __expf(dt * lr);
        const float rev = dt * li * 0.15915494309189535f;
        const float fr = rev - __builtin_rintf(rev);
        ar = er * __builtin_amdgcn_cosf(fr); ai = er * __builtin_amdgcn_sinf(fr);
        pin(ar); pin(ai); }
      if constexpr (v == 4) { a8r = ar; a8i = ai;
        for (int i = 0; i < 3; ++i) csq(a8r, a8i, a8r, a8i);
        csq(a16r, a16i, a8r, a8i); pin(a16r); pin(a16i); }
      if constexpr (v == 6) {
        const float nr = ar - 1.f, ni = ai, den = 2.f * __builtin_amdgcn_rcpf(vfma(lr, lr, li * li));
        kr = vfma(nr, lr, ni * li) * den; nki = vfma(nr, li, -(ni * lr)) * den; nkr = -kr; nai = -ai;
        pin(nai); pin(kr); pin(nkr); pin(nki); }
      if constexpr (v == 10) { csq(a32r, a32i, a16r, a16i); csq(a64r, a64i, a32r, a32i); if (blk) { pwr = a16r; pwi = a16i; mr = a64r; mi = a64i; } }
      if constexpr (v == 11) { float tr = pwr, ti = pwi; cf4(tr, ti, a32r, a32i, 0.f, 0.f); if (h) { pwr = tr; pwi = ti; } csq(a32r, a32i, a64r, a64i); }
      if constexpr (v == 12) { float ur = mr, ui = mi; cf4(ur, ui, a32r, a32i, 0.f, 0.f); if (h) { mr = ur; mi = ui; } csq(a256r, a256i, a32r, a32i); }
      if constexpr (v == 13 || v == 14) { constexpr int u = v - 13;
        const unsigned val = (((lane >> 2) & 1) == h && blk == u) ? ((lane & 1) ? 0x3f800000u : 0x3f80u) : 0u; const int d0 = ((lane >> 1) & 1) | ((lane >> 2) & 2);
        for (int d = 0; d < 4; ++d) oh[u][d] = d0 == d ? val : 0u;
        pin(oh[u]); }
      sb();
    };
    f32x16 sa[2] = {};
    f32x4 csa = {};
    { u32x4 af[2][4][2], wf[2][4];
      auto rdA = [&](int p, int s, unsigned slot) { wf[p][s] = lds_r128(slot + wa[s]); af[p][s][0] = lds_r128(slot + ra[s]); af[p][s][1] = lds_r128(slot + ra[s] + 4096); };
      auto mmA = [&](int p, int s) { sa[0] = mfma(af[p][s][0], wf[p][s], sa[0]); sa[1] = mfma(af[p][s][1], wf[p][s], sa[1]); pin(sa[0]); pin(sa[1]); };
      auto csA = [&](int p) { for (int s = 0; s < 4; ++s) csa = mfma16(sel, wf[p][s], csa); pin(csa); sb(); };
      auto W3 = [&](auto N, auto H_) { sfor<0, 3>([&](auto I) { opW(N, IC(3 * H_ + I)); }); };
      unsigned rh[4], wh[4];
      for (int s = 0; s < 4; ++s) { rh[s] = ra[s] + 3 * SLT; wh[s] = wa[s] + 3 * SLT - 8192; pin(rh[s]); pin(wh[s]); wh[s] += 8192; wa[s] -= 8192; pin(wa[s]); wa[s] += 8192; }
      ldB(IC(2));
      __builtin_amdgcn_s_setprio(HPA);
      wait_vm<10>(); STA(46) bar(); STA(47) for (int s = 0; s < 4; ++s) rdA(0, s, SLT); sb(); ldB(IC(3));
      sfor<1, 8>([&](auto V) { constexpr int v = V, p = v & 1, sl = (v + 1) % 6;
        wait_vm<(v == 1 ? 9 : WEARLY ? (v < 4 ? 4 : v == 4 ? 7 : v == 5 ? 10 : v == 6 ? 11 : 6) : (v < 5 ? 4 : v == 5 ? 7 : v == 6 ? 11 : 6))>(); wait_lgkm0();
        if constexpr (v == 7) __builtin_amdgcn_s_setprio(3);
        STA(46 + 2 * v) bar(); STA(47 + 2 * v)
        sfor<0, 8>([&](auto I) { constexpr int i = I;
          sa[i & 1] = mfma(af[p ^ 1][i >> 1][i & 1], wf[p ^ 1][i >> 1], sa[i & 1]); pin(sa[i & 1]);
          sb();
          if constexpr (i < 6) sfor<2 * i, 2 * i + 2>([&](auto R) { constexpr int r = R, s = r / 3, k = r % 3, o = (sl % 3) * SLT;
            if constexpr (k == 0) wf[p][s] = lds_r128((sl < 3 ? wa[s] : wh[s]) + o); else af[p][s][k - 1] = lds_r128((sl < 3 ? ra[s] : rh[s]) + o + (k - 1) * 4096); });
          if constexpr (i == 6 && v < 5) ldB(IC(v + 3));
          if constexpr (i == 7 && v == 4) W3(IC(0), IC(0));
          if constexpr (i == 6 && v == 5) W3(IC(0), IC(1));
          if constexpr (i == 7 && v == 5) W3(IC(1), IC(0));
          if constexpr (i == 6 && v == 6) W3(IC(1), IC(1));
          sb(); });
        csA(p ^ 1);
        params(V);
      });
      STAMP(2)
      for (int s = 0; s < 4; ++s) mmA(1, s);
      csA(1);
      __builtin_amdgcn_s_setprio(HPB);
    }
    const int kb = 22 * w - (w == 3), cnt = w < 2 ? 22 : 21, ct = get_ct();
    const i32x4 swd = mk_srd(wd + kb * 8192, cnt * 16384);
    const unsigned so_c = ct * 65536 + w * 256;
    const unsigned r2 = (xcd * 11) >> 2;
    auto kofs = [&](int p, unsigned sc_) { const unsigned r = lnd(r2 * sc_), a = r + p * sc_, b2 = r + (p - 22) * sc_; return a < b2 ? a : b2; };
    for (int r = 0; r < 2; ++r) vd[r] = ((lane >> 3) + 7 * r) * 1024 + (((lane & 7) ^ (((lane >> 4) & 1) << 2)) * 16) + ct * 128;
    unsigned so_t = 0; i32x4 sc = swd;
    auto wd_op = [&](auto M, auto E, auto R) { constexpr int m = M, e = E, r = R;
      if constexpr (r == 0) { so_t = kofs(m < 18 ? (m + 14) % 18 : m, 16384); setm0i<cell_c(e)>(cell_k(e) == 0 ? w6 : cell_k(e) == 1 ? w4 : w2); }
      dmao<r * 1024>(swd, vd[r], so_t);
    };
    auto dma_wd = [&](auto M, auto E) { wd_op(M, E, IC(0)); wd_op(M, E, IC(1)); };
    auto c_op = [&](auto N, auto I) { constexpr int n = N, i = I;
      if constexpr (i == 0) { const int kg = ((n >> 1) + lnd(xcd)) & 3; so_t = so_c + (n & 1) * 32768 + (kg & 1) * 128; sc = mk_srd((kg >> 1) ? Cim : Cre, SD * SP * 2); }
      if constexpr (!(i & 1)) setm0i<(n & 1) ? ((i >> 1) ? M_T : cell_c(15)) : cell_c(16 + (i >> 1))>(w2);
      dmao<(i & 1) * 1024>(sc, vb[i & 1], so_t + (i >> 1) * 16384);
    };
    unsigned rcx[4];
    for (int s = 0; s < 4; ++s) rcx[s] = OFF_B + w * 2048 + blk * SLT + (lane & 15) * 128 + (((2 * s + h) ^ ((l5 >> 1) & 7)) * 16);
    const unsigned rcd = 2 * SLT + blk * (M_T - cell_c(15) - SLT);
    auto rd_c = [&](int n, u32x4* d) { for (int s = 0; s < 4; ++s) d[s] = lds_r128(rcx[s] + ((n & 1) ? rcd : 0u)); };
    const unsigned granb = WS_GRAN + (g * 2 + sb_) * 8192;
    const i32x4 sgr = mk_srd(ws + granb, 8192);
    const unsigned vg = (32 - cc) * 256 + va;
    float R[16], I_[16], csr, csi, er[2], ei[2], sr[4], si[4], prer, prei, cr, ci;
    f32x4 tb[8];
    u32x4 ge[8], fr4[4];
    f32x16 td[2];
    unsigned hp[2][8];
    unsigned nbad = 0, fo_g = 0, pa[2], tdl = 0;
    auto side = [&](auto P_) { constexpr int P = P_;
      if constexpr (P == 0) { for (int k = 0; k < 8; ++k) tb[k] = *(LAS f32x4*)(size_t)(M_ST + pi * 512 + (lane >> 4) * 128 + k * 16);
        csr = csa[0]; float t = csr; swap32(csr, t); csi = csr; swap16(csr, csi); }
      if constexpr (P >= 1 && P < 9) { constexpr int k = P - 1;
        for (int r = 0; r < 2; ++r) { R[2 * k + r] = sa[0][2 * k + r]; I_[2 * k + r] = sa[1][2 * k + r]; swap16(R[2 * k + r], I_[2 * k + r]); }
        for (int r = 0; r < 2; ++r) sc4(R[2 * k + r], I_[2 * k + r], tb[k][2 * r], tb[k][2 * r + 1], csr, csi); }
      if constexpr (P == 9) { for (int r = 0; r < 2; ++r) { er[r] = R[r]; ei[r] = I_[r]; cf4(er[r], ei[r], ar, ai, R[2 + r], I_[2 + r]); } }
      if constexpr (P >= 16 && P < 22) { constexpr int k = P - 14; for (int r = 0; r < 2; ++r) cf4(er[r], ei[r], ar, ai, R[2 * k + r], I_[2 * k + r]); }
      if constexpr (P == 22) { sr[0] = er[0]; si[0] = ei[0]; cf4(sr[0], si[0], a8r, a8i, er[1], ei[1]); sr[1] = sr[0]; swap16(sr[0], sr[1]); si[1] = si[0]; swap16(si[0], si[1]); }
      if constexpr (P == 23) { sr[2] = sr[0]; swap32(sr[0], sr[2]); si[2] = si[0]; swap32(si[0], si[2]); sr[3] = sr[1]; swap32(sr[1], sr[3]); si[3] = si[1]; swap32(si[1], si[3]); }
      if constexpr (P == 24) { cr = sr[0]; ci = si[0]; cf4(cr, ci, a16r, a16i, sr[1], si[1]); sr[1] = cr; si[1] = ci; cf4(cr, ci, a16r, a16i, sr[2], si[2]); sr[2] = cr; si[2] = ci; }
      if constexpr (P == PPUB) { cf4(cr, ci, a16r, a16i, sr[3], si[3]);
        __builtin_amdgcn_raw_buffer_store_b128((u32x4){__float_as_uint(cr), t1l, __float_as_uint(ci), t1h}, rws, lane < 16 ? granb + (31 - cc) * 256 + va : OOB, 0, 16); }
      if constexpr (P == 32) { prer = h ? (blk ? sr[2] : sr[1]) : (blk ? sr[0] : 0.f); prei = h ? (blk ? si[2] : si[1]) : (blk ? si[0] : 0.f); }
      sfor<0, 16>([&](auto K) { constexpr int k = K; if constexpr (P == nxt(33, k < 4 ? k / 2 : k - 2)) kc4(R[k], I_[k], kr, nkr, nki); });
      if constexpr (P >= 53 && P < 58) params(IC(P - 43));
      if constexpr (P == PG) sfor<0, 8>([&](auto K) { constexpr int k = K; ld128_sc1<(k & 3) * 1024>(ge[k], sgr, vg, (k >> 2) * 4096); });
      if constexpr (P == PC) { STB(23) wait_vm<hb_cnt(PC, false) - hb_cnt(PG, true)>(); for (int i = 0; i < 8; ++i) pin(ge[i]); STB(24) }
      if constexpr (P == PC + 3) STB(25) if constexpr (P == PC + 6) STB(26) if constexpr (P == PC + 9) STB(27)
      if constexpr (P == PC || P == PC + 1) { constexpr int i = 4 * (P - PC); miss2(nbad, ge[i], ge[i + 1], t1l, t1h); miss2(nbad, ge[i + 2], ge[i + 3], t1l, t1h); }
      if constexpr (P == PC + 2) { const unsigned nout = 8 - ((cc + 3 - (lane >> 4)) >> 2);
#pragma unroll 1
        for (unsigned spins = 0; spins < SPIN_MAX; ++spins) {
          if (__all(nbad == nout)) break;
          STAMP(28)
          __builtin_amdgcn_s_sleep(SLP);
          sfor<0, 8>([&](auto I) { constexpr int i = I; ld128_sc1_rw<(i & 3) * 1024>(ge[i], sgr, vg, (i >> 2) * 4096); });
          wait_vm<0>(); for (int i = 0; i < 8; ++i) pin(ge[i]);
          nbad = 0;
          for (int i = 0; i < 8; i += 2) miss2(nbad, ge[i], ge[i + 1], t1l, t1h);
        }
        cr = __uint_as_float(ge[7][0]); ci = __uint_as_float(ge[7][2]); cf4(cr, ci, a256r, a256i, __uint_as_float(ge[6][0]), __uint_as_float(ge[6][2])); }
      if constexpr (P >= PC + 3 && P < PC + 6) { constexpr int i = 5 - 2 * (P - PC - 3);
        cf4(cr, ci, a256r, a256i, __uint_as_float(ge[i][0]), __uint_as_float(ge[i][2])); cf4(cr, ci, a256r, a256i, __uint_as_float(ge[i - 1][0]), __uint_as_float(ge[i - 1][2])); }
      if constexpr (P == PC + 6) { cf4(cr, ci, mr, mi, 0.f, 0.f); float t = cr, u = ci; swap16(cr, t); swap16(ci, u); cr = vadd(cr, t); ci = vadd(ci, u); }
      if constexpr (P == PC + 7) { float t = cr, u = ci; swap32(cr, t); swap32(ci, u); cr = vadd(cr, t); ci = vadd(ci, u); cf4(cr, ci, pwr, pwi, prer, prei); }
      if constexpr (P == PC + 8) { sr[0] = cr; si[0] = ci; cf4(cr, ci, a8r, a8i, er[0], ei[0]); er[0] = sr[0]; ei[0] = si[0]; er[1] = cr; ei[1] = ci; }
      if constexpr (P == PC + 9) { kc4(er[0], ei[0], kr, nkr, nki); kc4(er[1], ei[1], kr, nkr, nki); }
      if constexpr (P % 16 == 4 && P / 16 >= PFW_S && P / 16 < 6) sfor<0, PFW>([&](auto U) { constexpr int k = (P / 16 - PFW_S) * PFW + U + PFW_O, m = k < 4 ? 18 + k : k < 18 ? k : k - 18;
        if constexpr (k < 22) { const unsigned o = kofs(m < 18 ? (m + 14) % 18 : m, 16384); if (o < (unsigned)cnt * 16384) for (int r = 0; r < 4; ++r) sline((u64)(wd + kb * 8192), o + ct * 128 + (4 * (c & 3) + r) * 1024); } });
      sfor<0, 8>([&](auto K) { constexpr int k = K; if constexpr (P == nxt(P2, k)) {
        for (int r = 0; r < 2; ++r) cf4c(k ? R[2 * k - 2 + r] : er[r], k ? I_[2 * k - 2 + r] : ei[r], ar, ai, nai, R[2 * k + r], I_[2 * k + r]);
        hp[0][k] = pk2(R[2 * k], R[2 * k + 1]); hp[1][k] = pk2(I_[2 * k], I_[2 * k + 1]); } });
      static_assert(PT > nxt(P2, 7) && PS > PT && PF > PS && PC % 16 == 0, "scan schedule");
      if constexpr (P == PT) {
        __builtin_amdgcn_s_setprio(3);
        for (int part = 0; part < 2; ++part) {
          td[part] = mfma((u32x4){hp[part][0], hp[part][1], hp[part][2], hp[part][3]}, oh[0], (f32x16){});
          td[part] = mfma((u32x4){hp[part][4], hp[part][5], hp[part][6], hp[part][7]}, oh[1], td[part]); pin(td[part]); }
        __builtin_amdgcn_s_setprio(HPB); }
      if constexpr (P == PS) { for (int k = 0; k < 4; ++k) for (int d = 0; d < 4; ++d) fr4[k][d] = pk2(td[k >> 1][8 * (k & 1) + 2 * d], td[k >> 1][8 * (k & 1) + 2 * d + 1]); }
      if constexpr (P == PS) { for (int k = 0; k < 4; ++k)
          __builtin_amdgcn_raw_buffer_store_b128(fr4[k], rws, a2pb + (2 * g + sb_) * 1024 + va + ((k & 1) * NKS + (k >> 1) * 32) * 1024, 0, 16); }
      if constexpr (P == 100) { const int ks = kb + lane, p6 = (ks * 43) >> 8, pg = ks < 84 ? p6 + (p6 >= 7) : 7 + 8 * (ks - 84), pb = ks < 84 ? ks - 6 * p6 : 0;
        fo_g = lane < cnt ? (pg * 8 + 4 + 2 * rsel + (pb >= 3)) * 128 : OOB; pin(fo_g); }
      if constexpr (P == 104) { for (int rb = 0; rb < 2; ++rb) { const int e = 4 * rb + (l5 >> 3), d = (e * 11) >> 5;
          pa[rb] = (3 + d) * SLT + (e - 3 * d) * 2048 + w6 + (lane & 7) * 256 + ((h ^ (lane & 7)) * 16); pin(pa[rb]); } }
      if constexpr (P == 106) { tdl = (8 * h + q) * 128 + (((blk * 2 + (pp >> 1)) ^ ((q >> 1) << 2)) * 16) + (pp & 1) * 8; pin(tdl); }
      if constexpr (P == PF) { wait_vm<hb_cnt(PF, false) - hb_cnt(PS, true)>(); set_flag(w);
        if (cc == 31) __builtin_amdgcn_raw_buffer_store_b64((u32x2){t1l, t1h}, rws, (lane == 0 && sb_ == 0) ? WS_E1 + g * 128 : OOB, 0, 16); }
    };
    u32x4 cq[8][4];
    u32x2 fs;
    const unsigned fo_s = lane < 8 ? ((4 * w + (lane >> 1)) * 8 + 2 * rsel + (lane & 1)) * 128 : OOB;
    sfor<0, 8>([&](auto N) { constexpr int n = N, g0 = hb_g0(n), done = n ? hb_cnt(16 * n - 1, true) : 0;
      if constexpr (n > 0) { if constexpr (n == 1) wait_vm<done>(); STB(30 + 2 * n) bar(); STB(31 + 2 * n) } else STB(31)
      if constexpr (n > 1) { wait_vm<done - hb_cnt(16 * (n > 1 ? n - 2 : 0) + hb_g0(n - 2) + 3, false)>(); rd_c(n, cq[n - 2]); sb(); }
      sfor<0, hb_ops(n)>([&](auto I) { constexpr int i = I, io = i < g0 ? i : i - 4;
        if constexpr (i >= g0 && i < g0 + 4) { if constexpr (i == g0 && n > 1) { for (int s = 0; s < 4; ++s) use(cq[n - 2][s]); } c_op(N, IC(i - g0)); }
        else if constexpr (n >= 6) { constexpr int e = 5 * (n - 6) + (io >> 1); wd_op(IC(e < 4 ? 18 + e : e), IC(e), IC(io & 1)); }
        if constexpr (16 * n + i == PPOLL) ld64_sc1(fs, sws, fo_s, flagb);
        side(IC(16 * n + i));
        sb();
      });
    });
    __builtin_amdgcn_s_setprio(HPC);
    STAMP(3)
    auto poll = [&](unsigned fo, u32x2 fv) {
#pragma unroll 1
      for (unsigned spins = 0; spins < SPIN_MAX; ++spins) {
        const bool ok = fo == OOB || (fv[0] == (unsigned)tag2 && fv[1] == (unsigned)(tag2 >> 32));
        if (__all(ok)) break;
        __builtin_amdgcn_s_sleep(SLP);
        ld64_sc1(fv, sws, fo, flagb); wait_vm<0>(); pin(fv);
      }
    };
    wait_vm<hb_cnt(127, true) - hb_cnt(16 * 6 + 7, false)>();
    rd_c(0, cq[6]);
    const unsigned a2c = WS_A2 + (unsigned)(c * 4 + 2 * rsel) * (NKS * 1024);
    const i32x4 sg0 = mk_srd(ws + a2c + (64 + kb) * 1024, cnt * 1024), sg1 = mk_srd(ws + a2c + (NKS + 64 + kb) * 1024, cnt * 1024);
    const unsigned vs0 = va + a2c + w * 8192, vs1 = vs0 + NKS * 1024;
    wait_lgkm0();
    dma_wd(IC(16), IC(16)); dma_wd(IC(17), IC(17));
    wait_cnt();
    ld64_sc1(fs, sws, fo_s, flagb);
    sfor<10, 13>([&](auto E) { dma_wd(E, E); });
    wait_vm<6>(); pin(fs);
    STAMP(7)
    poll(fo_s, fs);
    STAMP(4)
    unsigned tb2[4][2];
    auto rd_half = [&](int e, int cb) { const int k = cell_k(e) == 0 ? (cell_c(e) >= 5 * SLT) : cell_k(e) + 1;
      return lds_tr2(tb2[k][cb] + (cell_c(e) - (k == 0 ? 3 * SLT : k == 1 ? 5 * SLT : k == 3 ? OFF_B : 0)), 512); };
    f32x16 acc2[2][2];
    u32x4 L[76], wq[4][2], bf[2][2];
    unsigned so_l = 0;
    auto soL = [&](auto M) { constexpr int m = M;
      if constexpr (m < 16) { if constexpr (!(m & 3)) { const int kg = ((m >> 2) + lnd(xcd)) & 3; so_l = ((kg >> 1) * 32 + (kg & 1) * 4) * 1024; } }
      else so_l = kofs(m - 16, 1024); };
    auto ldH = [&](auto M, auto R) { constexpr int m = M, r = R;
      if constexpr (m < 16) ld128_sc1<(m & 3) * 1024>(L[2 * m + r], sws, r ? vs1 : vs0, so_l); else ld128_sc1<0>(L[2 * m + r], r ? sg1 : sg0, va, so_l); };
    auto ldL = [&](auto M) { soL(M); ldH(M, IC(0)); ldH(M, IC(1)); };
    sfor<0, p2_l0(0)>(ldL);
    sfor<0, 38>([&](auto M) { constexpr int m = M, mm = m - 16;
      constexpr bool one = p2_l0(m + 1) == p2_l0(m) + 1;
      wait_vm<p2_after(m)>(); pin(L[2 * m]); pin(L[2 * m + 1]);
      sfor<0, 4>([&](auto G) { constexpr int gp = G, cb = gp >> 1, rb = gp & 1;
        if constexpr (m < 16) acc2[cb][rb] = mfma(cq[2 * (m >> 2) + cb][m & 3], L[2 * m + rb], m == 0 ? (f32x16){} : acc2[cb][rb]);
        else acc2[cb][rb] = mfma(mm < 18 ? bf[mm & 1][cb] : wq[mm < 18 ? 0 : mm - 18][cb], L[2 * m + rb], acc2[cb][rb]);
        sb();
        if constexpr (gp < 2) {
          if constexpr (m >= 15 && mm + 1 < 18) bf[(mm + 1) & 1][gp] = rd_half((mm + 5) % 18, gp);
          if constexpr (m == 0 || m == 1) wd_op(IC(13 + m), IC(13 + m), G);
          if constexpr (m == 2 || m == 3) { constexpr int k = 2 * (m - 2) + gp;
            for (int u = 0; u < 2; ++u) { tb2[k][u] = (tdl ^ (u * 64)) + (k == 0 ? 3 * SLT + w6 : k == 1 ? 5 * SLT + w6 : k == 2 ? w4 : OFF_B + w2); pin(tb2[k][u]); } }
          if constexpr (m == 4 && gp == 0) rd_c(1, cq[7]);
          if constexpr (m == 5) { if constexpr (gp == 0) { for (int s = 0; s < 4; ++s) use(cq[7][s]); } wd_op(IC(15), IC(15), G); }
          if constexpr (m >= 7 && m <= 10) { if constexpr (gp == 0) { use(wq[m - 7][0]); use(wq[m - 7][1]); } wd_op(IC(m - 7), IC(m - 7), G); }
          if constexpr (m >= 6 && m <= 9) wq[m - 6][gp] = rd_half(m - 6, gp);
          if constexpr (gp == 1 && m == PCK - 1) ld64_sc1(fs, sws, fo_g, flagb);
          if constexpr (gp == 1 && one) soL(IC(p2_l0(m)));
        } else {
          if constexpr (gp == 2 && m == PCK) { STAMP(5) wait_vm<p2_extra(PCK) + 2 * (16 - p2_l0(PCK - 1))>(); pin(fs); poll(fo_g, fs); STAMP(6) }
          if constexpr (one) ldH(IC(p2_l0(m)), IC(gp - 2));
          else if constexpr (gp == 2) sfor<p2_l0(m), p2_l0(m + 1)>(ldL);
        }
        sb();
      });
    });
    STAMP(9)
    for (int rb = 0; rb < 2; ++rb) for (int rq = 0; rq < 4; ++rq) for (int cb = 0; cb < 2; ++cb)
      *(LAS f32x4*)(size_t)((pa[rb] ^ (rq * 32)) + cb * 128) =
          (f32x4){acc2[cb][rb][4 * rq], acc2[cb][rb][4 * rq + 1], acc2[cb][rb][4 * rq + 2], acc2[cb][rb][4 * rq + 3]};
    wait_lgkm0(); bar();
    STAMP(11)
  }
  STAMP(12)
  STAMP_OUT
  asm volatile(".Lpf_end:");
}

extern "C" void kernel_launch(void* const* d_in, const int* in_sizes, int n_in, void* d_out, int out_size, void* d_ws, size_t ws_size,
                              hipStream_t stream) {
  (void)hipFuncSetAttribute((const void*)fused, hipFuncAttributeMaxDynamicSharedMemorySize, LDS_BYTES);
  fused<<<256, 512, LDS_BYTES, stream>>>((const u16*)d_in[0], (const u16*)d_in[1], (const u16*)d_in[2], (const u16*)d_in[3], (const u16*)d_in[4],
                                         (const u16*)d_in[5], (const u16*)d_in[6], (const u16*)d_in[7], (const u16*)d_in[8], (const u16*)d_in[10],
                                         (const u16*)d_in[11], (const u16*)d_in[12], (char*)d_ws, (float*)d_out);
}
```

```cpp
#define DIAG 0
#include <hip/hip_runtime.h>
#include <hip/hip_bf16.h>
#include <type_traits>

typedef unsigned short u16;
typedef unsigned long long u64;
typedef short s16x4 __attribute__((ext_vector_type(4)));
typedef __bf16 bf16x2 __attribute__((ext_vector_type(2)));
typedef __bf16 bf16x8 __attribute__((ext_vector_type(8)));
typedef float f32x2 __attribute__((ext_vector_type(2)));
typedef float f32x4 __attribute__((ext_vector_type(4)));
typedef float f32x16 __attribute__((ext_vector_type(16)));
typedef unsigned u32x2 __attribute__((ext_vector_type(2)));
typedef unsigned u32x4 __attribute__((ext_vector_type(4)));
typedef int i32x4 __attribute__((ext_vector_type(4)));
#define LAS __attribute__((address_space(3)))
#define DEVI __device__ __forceinline__

constexpr int SL = 2048, SD = 512, SP = 512, SF = 1376;
constexpr int NKS = 150;
constexpr unsigned WS_E1 = 0;
constexpr unsigned WS_E2 = 2048;
constexpr unsigned WS_GRAN = 262144;
constexpr unsigned WS_FLAG = 524288;
constexpr unsigned WS_A2 = 786432;
constexpr unsigned A2_BYTES = 64u * NKS * 1024u;
constexpr unsigned WS_BYTES = WS_A2 + A2_BYTES;
constexpr unsigned OOB = 0x40000000u;
constexpr int SLT = 24832;
constexpr int OFF_B = 16384, OFF_WU = 12288 + 32;
constexpr int MISC = 6 * SLT;
constexpr int M_ST = MISC, M_SD = MISC + 1024, M_CNT = MISC + 2048, M_CS = MISC + 2304, M_T = MISC + 3840;
constexpr int LDS_BYTES = 163840;
constexpr unsigned SPIN_MAX = 1u << 16;
constexpr int cell_k(int e) { return e >= 15 ? 2 : (e % 5) < 3 ? 0 : 1; }
constexpr int cell_c(int e) { return e >= 15 ? ((e - 13) % 3) * SLT + OFF_B : (e % 5) < 3 ? (3 + (e / 5 + 2) % 3) * SLT + (e % 5) * 2048 : ((e / 5 + 2) % 3) * SLT + (e % 5 - 3) * 2048; }

#define STAMP_DECL
#define STAMP(i) {}
#define STAMP_OUT
#define STB(i) {}
template <int B, int E, class Fn> DEVI void sfor(Fn&& f) {
  if constexpr (B < E) { f(std::integral_constant<int, B>{}); sfor<B + 1, E>(f); }
}
DEVI i32x4 mk_srd(const void* p, unsigned bytes) {
  u64 a = (u64)p; return (i32x4){(int)(unsigned)a, (int)((unsigned)(a >> 32) & 0xffffu), (int)bytes, 0x00020000};
}
DEVI void dma(i32x4 srd, unsigned voff, unsigned soff, unsigned lds) {
  asm volatile("s_mov_b32 m0, %0\n\ts_nop 0\n\tbuffer_load_dwordx4 %1, %2, %3 offen lds" ::"s"(lds), "v"(voff), "s"(srd), "s"(soff) : "memory");
}
template <int OFF> DEVI void ld128_sc1(u32x4& d, i32x4 srd, unsigned voff, unsigned soff) {
  asm volatile("buffer_load_dwordx4 %0, %1, %2, %3 offen offset:%4 sc1" : "=v"(d) : "v"(voff), "s"(srd), "s"(soff), "n"(OFF) : "memory");
}
template <int OFF> DEVI void ld128_sc1_rw(u32x4& d, i32x4 srd, unsigned voff, unsigned soff) {
  asm volatile("buffer_load_dwordx4 %0, %1, %2, %3 offen offset:%4 sc1" : "+v"(d) : "v"(voff), "s"(srd), "s"(soff), "n"(OFF) : "memory");
}
DEVI void ld64_sc1(u32x2& d, i32x4 srd, unsigned voff, unsigned soff) {
  asm volatile("buffer_load_dwordx2 %0, %1, %2, %3 offen sc1" : "=v"(d) : "v"(voff), "s"(srd), "s"(soff) : "memory");
}
DEVI void ld64(u32x2& d, i32x4 srd, unsigned voff, unsigned soff) {
  asm volatile("buffer_load_dwordx2 %0, %1, %2, %3 offen" : "=v"(d) : "v"(voff), "s"(srd), "s"(soff) : "memory");
}
DEVI void ld16(unsigned& d, i32x4 srd, unsigned voff) {
  asm volatile("buffer_load_ushort %0, %1, %2, 0 offen" : "=v"(d) : "v"(voff), "s"(srd) : "memory");
}
template <int N> DEVI void wait_vm() { asm volatile("s_waitcnt vmcnt(%0)" ::"n"(N) : "memory"); }
template <class T> DEVI void pin(T& a) { asm volatile("" : "+v"(a)::"memory"); }
template <class T> DEVI void use(const T& a) { asm volatile("" ::"v"(a) : "memory"); }
DEVI int lnd(int a) { asm volatile("" : "+s"(a)); return a; }
DEVI int ftid() { int t = threadIdx.x; asm volatile("" : "+v"(t)); return t; }
DEVI void wait_lgkm0() { asm volatile("s_waitcnt lgkmcnt(0)" ::: "memory"); }
DEVI void bar() { asm volatile("s_barrier" ::: "memory"); }
DEVI void sb() { __builtin_amdgcn_sched_barrier(0); }
DEVI float lo2f(unsigned u) { return __uint_as_float(u << 16); }
DEVI float hi2f(unsigned u) { return __uint_as_float(u & 0xffff0000u); }
DEVI unsigned pk2(float a, float b) { f32x2 v = {a, b}; return __builtin_bit_cast(unsigned, __builtin_convertvector(v, bf16x2)); }
DEVI float rbf(float a) { return lo2f(pk2(a, a)); }
DEVI u32x4 lds_r128(unsigned a) { return *(LAS u32x4*)(size_t)a; }
DEVI u32x2 lds_tr(unsigned a) { return __builtin_bit_cast(u32x2, __builtin_amdgcn_ds_read_tr16_b64_v4i16((LAS s16x4*)(size_t)a)); }
DEVI u32x4 lds_tr2(unsigned a, unsigned d) { u32x2 x = lds_tr(a), y = lds_tr(a + d); return (u32x4){x[0], x[1], y[0], y[1]}; }
DEVI f32x16 mfma(u32x4 a, u32x4 b, f32x16 c) {
  return __builtin_amdgcn_mfma_f32_32x32x16_bf16(__builtin_bit_cast(bf16x8, a), __builtin_bit_cast(bf16x8, b), c, 0, 0, 0);
}
DEVI float dot2(unsigned a, unsigned b, float c) {
  return __builtin_amdgcn_fdot2_f32_bf16(__builtin_bit_cast(bf16x2, a), __builtin_bit_cast(bf16x2, b), c, false);
}
DEVI void swap16(float& a, float& b) { auto r = __builtin_amdgcn_permlane16_swap(__float_as_uint(a), __float_as_uint(b), false, false); a = __uint_as_float(r[0]); b = __uint_as_float(r[1]); }
DEVI void swap32(float& a, float& b) { auto r = __builtin_amdgcn_permlane32_swap(__float_as_uint(a), __float_as_uint(b), false, false); a = __uint_as_float(r[0]); b = __uint_as_float(r[1]); }
DEVI float sum32(float v) { float a = v, b = v; swap32(a, b); return a + b; }
DEVI float sum16(float v) { float a = v, b = v; swap16(a, b); return a + b; }
DEVI void cmul(float& dr, float& di, float ar, float ai, float br, float bi) { float t = ar * br - ai * bi; di = ar * bi + ai * br; dr = t; }
DEVI void cfma(float& hr, float& hi, float ar, float ai, float br, float bi) {
  float t = __builtin_fmaf(ar, hr, __builtin_fmaf(-ai, hi, br)); hi = __builtin_fmaf(ar, hi, __builtin_fmaf(ai, hr, bi)); hr = t;
}

template <int OFF> DEVI void dmao(i32x4 srd, unsigned voff, unsigned soff) {
  asm volatile("buffer_load_dwordx4 %0, %1, %2 offen offset:%3 lds" ::"v"(voff), "s"(srd), "s"(soff), "n"(OFF) : "memory");
}
DEVI void setm0(unsigned lds) { asm volatile("s_mov_b32 m0, %0\n\ts_nop 0" ::"s"(lds) : "memory"); }
DEVI void touch(i32x4 srd, unsigned voff, unsigned soff) {
  asm volatile("buffer_load_dword %0, %1, %2 offen lds" ::"v"(voff), "s"(srd), "s"(soff) : "memory");
}
DEVI f32x4 mfma16(u32x4 a, u32x4 b, f32x4 c) {
  return __builtin_amdgcn_mfma_f32_16x16x32_bf16(__builtin_bit_cast(bf16x8, a), __builtin_bit_cast(bf16x8, b), c, 0, 0, 0);
}
DEVI float vadd(float a, float b) { float r; asm("v_add_f32 %0, %1, %2" : "=v"(r) : "v"(a), "v"(b)); return r; }
DEVI float vfma(float a, float b, float c) { float r; asm("v_fma_f32 %0, %1, %2, %3" : "=v"(r) : "v"(a), "v"(b), "v"(c)); return r; }
DEVI f32x2 pfma(f32x2 a, f32x2 b, f32x2 c) { return __builtin_elementwise_fma(a, b, c); }
DEVI f32x2 sp(float a) { return (f32x2){a, a}; }
DEVI void cfma2(f32x2& hr, f32x2& hi, f32x2 a, f32x2 br, f32x2 bi) {
  f32x2 t1, t2;
  asm("v_pk_fma_f32 %2, %4, %1, %5 op_sel:[1,0,0] op_sel_hi:[1,1,1] neg_lo:[1,0,0] neg_hi:[1,0,0]\n\t"
      "v_pk_fma_f32 %3, %4, %0, %6 op_sel:[1,0,0] op_sel_hi:[1,1,1]\n\t"
      "v_pk_fma_f32 %0, %4, %0, %2 op_sel:[0,0,0] op_sel_hi:[0,1,1]\n\t"
      "v_pk_fma_f32 %1, %4, %1, %3 op_sel:[0,0,0] op_sel_hi:[0,1,1]"
      : "+v"(hr), "+v"(hi), "=&v"(t1), "=&v"(t2) : "v"(a), "v"(br), "v"(bi));
}
DEVI void cout2(unsigned& o0, unsigned& o1, f32x2 k, f32x2 gr, f32x2 gi) {
  f32x2 t1, t2;
  asm("v_pk_mul_f32 %0, %2, %4 op_sel:[1,0] op_sel_hi:[1,1]\n\t"
      "v_pk_mul_f32 %1, %2, %3 op_sel:[1,0] op_sel_hi:[1,1]\n\t"
      "v_pk_fma_f32 %0, %2, %3, %0 op_sel:[0,0,0] op_sel_hi:[0,1,1] neg_lo:[0,0,1] neg_hi:[0,0,1]\n\t"
      "v_pk_fma_f32 %1, %2, %4, %1 op_sel:[0,0,0] op_sel_hi:[0,1,1] neg_lo:[1,0,1] neg_hi:[1,0,1]"
      : "=&v"(t1), "=&v"(t2) : "v"(k), "v"(gr), "v"(gi));
  o0 = pk2(t1[0], t1[1]); o1 = pk2(t2[0], t2[1]);
}
#define SLP 4
#define WN 10
#define PGV 50
#define PCV 64
#define P2D 16
#define PTV 88
#define PSV 89
#define PFV 114
#define PFW 1
#define PFW_S 2
#define PFW_O 0
#define PFX 0
#define ROTX (2 * (xcd >> 1) + 4 * (xcd & 1))
#define PLATE 0
#define AVAR 0
#define STA(i) {}
#define HPA 0
#define WEARLY 0
#define PFW0 0
#define HPB 0
#define HPC 0
#define PFG 0
#define PFG_O 0
#define PCK 13
#define WG 6
#define EPK 0
#define CPE 0
#define PFT 1
#define OAUX 16
#define LEADX (c + g)
#define LEAD 1
#define IC(x) std::integral_constant<int, (x)>{}
constexpr int p2_extra(int m) { return ((m == 0 || m == 1 || m == 5 || (m >= 7 && m <= 10)) ? 2 : 0) + (m == PCK - 1 ? 1 : 0); }
constexpr int p2_l0(int m) { return m <= PCK ? (m + WN / 2 < 16 ? m + WN / 2 : 16) : (m + WG < 38 ? m + WG : 38); }
constexpr int p2_after(int m) {
  int pos = 0, posL[40] = {};
  for (int k = 0; k < p2_l0(0); ++k) { pos += 2; posL[k] = pos; }
  for (int s = 0; s < m; ++s) { pos += p2_extra(s); for (int k = p2_l0(s); k < p2_l0(s + 1); ++k) { pos += 2; posL[k] = pos; } }
  return pos - posL[m];
}
constexpr int PPUB = 25, PG = PGV, PC = PCV, P2 = PC + P2D, PT = PTV, PS = PSV, PF = PFV, PPOLL = -1;
constexpr int hb_ops(int n) { return n < 6 ? 10 : 14; }
constexpr int hb_g0(int n) { return n < 6 ? 3 : 4; }
constexpr int hb_extra(int P) { return P == PPUB ? 1 : P == PG ? 8 : P == PS ? 4 : P == PF ? 1 : P == PPOLL ? 1 : 0; }
constexpr int hb_cnt(int P, bool with_extra) {
  int t = 0;
  for (int n = 0; n < 8; ++n) for (int i = 0; i < hb_ops(n); ++i) { const int p = 16 * n + i; if (p > P) return t; t += (n >= 6 || (i >= hb_g0(n) && i < hb_g0(n) + 4)); if (p < P || with_extra) t += hb_extra(p); }
  return t;
}
constexpr int nxt(int P, int d) { for (; d > 0; --d) { ++P; if (P % 16 >= hb_ops(P / 16)) P = (P / 16 + 1) * 16; } return P; }
DEVI u64 utag(u32x2 e) { const u64 t = (((u64)(unsigned)__builtin_amdgcn_readfirstlane(e[1]) << 32) | (unsigned)__builtin_amdgcn_readfirstlane(e[0])) + 1; return t ? t : 1; }
template <int CTRL> DEVI float dppf(float v) { return __int_as_float(__builtin_amdgcn_update_dpp(0, __float_as_int(v), CTRL, 0xf, 0xf, true)); }
DEVI void sline(u64 base, unsigned off) {
  asm volatile("s_load_dwordx2 s[98:99], %0, %1" ::"s"(base), "s"(off + 60) : "memory", "s98", "s99");
}
template <int IMM> DEVI void setm0i(unsigned base) { asm volatile("s_add_i32 m0, %0, %1\n\ts_nop 0" ::"s"(base), "n"(IMM) : "memory", "scc"); }
DEVI void miss2(unsigned& n, u32x4 a, u32x4 b, unsigned tl, unsigned th) {
  unsigned t, u;
  asm("v_xor_b32 %1, %7, %3\n\tv_xor_b32 %2, %7, %5\n\tv_bitop3_b32 %1, %4, %8, %1 bitop3:0xbe\n\tv_bitop3_b32 %2, %6, %8, %2 bitop3:0xbe\n\t"
      "v_min_u32 %1, 1, %1\n\tv_min_u32 %2, 1, %2\n\tv_add3_u32 %0, %0, %1, %2"
      : "+v"(n), "=&v"(t), "=&v"(u) : "v"(a[1]), "v"(a[3]), "v"(b[1]), "v"(b[3]), "s"(tl), "s"(th));
}
DEVI unsigned geglu2(float g0, float g1, float u0, float u1, float k1, float k3, float s2) {
  float a, b;
  asm("v_mul_f32 %0, %4, %4\n\tv_mul_f32 %1, %5, %5\n\tv_fma_f32 %0, %0, %7, %6\n\tv_fma_f32 %1, %1, %7, %6\n\t"
      "v_mul_f32 %0, %0, %4\n\tv_mul_f32 %1, %1, %5\n\tv_exp_f32 %0, %0\n\tv_exp_f32 %1, %1\n\tv_mul_f32 %2, %2, %4\n\t"
      "v_fma_f32 %0, %0, %8, %8\n\tv_fma_f32 %1, %1, %8, %8\n\tv_rcp_f32 %0, %0\n\tv_rcp_f32 %1, %1\n\tv_mul_f32 %3, %3, %5\n\t"
      "v_mul_f32 %0, %0, %2\n\tv_mul_f32 %1, %1, %3\n\tv_cvt_pk_bf16_f32 %0, %0, %1"
      : "=&v"(a), "=&v"(b), "+v"(u0), "+v"(u1) : "v"(g0), "v"(g1), "v"(k1), "v"(k3), "v"(s2));
  return __float_as_uint(a);
}
DEVI void cf4(float& hr, float& hi, float ar, float ai, float br, float bi) {
  float t, u;
  asm("v_fma_f32 %0, -%5, %3, %6\n\tv_fma_f32 %1, %5, %2, %7\n\tv_fmac_f32 %0, %4, %2\n\tv_fmac_f32 %1, %4, %3"
      : "=&v"(t), "=&v"(u) : "v"(hr), "v"(hi), "v"(ar), "v"(ai), "v"(br), "v"(bi));
  hr = t; hi = u;
}
DEVI void csq(float& dr, float& di, float r, float i) {
  float t, u;
  asm("v_mul_f32 %0, %2, %2\n\tv_mul_f32 %1, %2, %3\n\tv_fma_f32 %0, -%3, %3, %0\n\tv_add_f32 %1, %1, %1" : "=&v"(t), "=&v"(u) : "v"(r), "v"(i));
  dr = t; di = u;
}
DEVI void cf4c(float hr, float hi, float ar, float ai, float nai, float& br, float& bi) {
  asm("v_fmac_f32 %0, %5, %3\n\tv_fmac_f32 %1, %6, %2\n\tv_fmac_f32 %0, %4, %2\n\tv_fmac_f32 %1, %4, %3"
      : "+v"(br), "+v"(bi) : "v"(hr), "v"(hi), "v"(ar), "v"(ai), "v"(nai));
}
DEVI void sc4(float& r, float& i, float rs, float rc, float csr, float csi) {
  asm("v_mul_f32 %0, %2, %0\n\tv_mul_f32 %1, %2, %1\n\tv_fmac_f32 %0, %3, %4\n\tv_fmac_f32 %1, %3, %5"
      : "+v"(r), "+v"(i) : "v"(rs), "v"(rc), "v"(csr), "v"(csi));
}
DEVI void kc4(float& r, float& i, float kr, float nkr, float nki) {
  float t, u;
  asm("v_mul_f32 %0, %4, %2\n\tv_mul_f32 %1, %5, %3\n\tv_fmac_f32 %0, %6, %3\n\tv_fmac_f32 %1, %6, %2"
      : "=&v"(t), "=&v"(u) : "v"(r), "v"(i), "v"(kr), "v"(nkr), "v"(nki));
  r = t; i = u;
}

__global__ __launch_bounds__(512, 2) void fused(const u16* __restrict__ x, const u16* __restrict__ lam_re, const u16* __restrict__ lam_im,
                                                const u16* __restrict__ Bre, const u16* __restrict__ Bim, const u16* __restrict__ Cre,
                                                const u16* __restrict__ Cim, const u16* __restrict__ logdt, const u16* __restrict__ dskip,
                                                const u16* __restrict__ wg, const u16* __restrict__ wu, const u16* __restrict__ wd,
                                                char* __restrict__ ws, float* __restrict__ out) {
  extern __shared__ __attribute__((aligned(16))) char shm[];
  u64 dg0_, dgn_ = 0; asm volatile("s_memrealtime %0" : "=s"(dg0_)); __builtin_amdgcn_sched_barrier(0);
  asm volatile(".Lpf_beg:");
#define DG(n) if (DIAG == n) { asm volatile("s_memrealtime %0\n\ts_waitcnt lgkmcnt(0)" : "=s"(dgn_) :: "memory"); }
  STAMP_DECL STAMP(0)
  unsigned perm;
  const i32x4 sx = mk_srd(x, SL * SD * 2);
  {
    const int b0 = blockIdx.x, t = threadIdx.x, lead = ((b0 >> 6) + (b0 >> 3)) & 3, rot = (b0 * 5) & 6;
    perm = (0x76543210u - (15u << (4 * lead)) * (1u + ((((lead + 1) >> 2) ^ 1) << 16)) + rot * 0x11111111u) & 0x77777777u;
    const unsigned v0 = (((t >> 2) & 64) + ((t >> 3) & 16) + ((t >> 4) & 5) + (t & 40)) * 1024 + (((t & 7) ^ ((t >> 4) & 3)) * 16);
    const unsigned so = (((b0 & 6) * 2 + (b0 >> 6)) << 17) + (perm & 7) * 128;
    asm volatile("" ::"s"(lam_re), "s"(lam_im), "s"(Bre), "s"(Bim), "s"(Cre), "s"(Cim), "s"(logdt), "s"(dskip), "s"(wg), "s"(wu), "s"(wd), "s"(ws), "s"(out));
    asm volatile("s_waitcnt lgkmcnt(0)" : "+s"(dg0_) :: "memory");
    DG(1)
    const int j0 = __builtin_amdgcn_readfirstlane(t >> 6);
    const unsigned v1 = (v0 ^ 64) + 1024;
    setm0i<SLT>(j0 * 2048); dmao<0>(sx, v0, so); dmao<1024>(sx, v1, so);
    const i32x4 sb0 = mk_srd((j0 >> 2) ? Bim : Bre, SP * SD * 2);
    const unsigned vb0 = ((t >> 3) & 31) * 1024 + (((t & 7) ^ ((t >> 4) & 7)) * 16), sob = (((b0 & 1) * 8 + ((b0 >> 3) & 7)) << 15);
    setm0i<SLT + OFF_B>(j0 * 1024); dmao<0>(sb0, vb0, sob + (perm & 7) * 128);
    const unsigned k1 = ((perm >> 4) & 7) * 128;
    setm0i<2 * SLT>(j0 * 2048); dmao<0>(sx, v0, (so & ~1023u) + k1); dmao<1024>(sx, v1, (so & ~1023u) + k1);
    setm0i<2 * SLT + OFF_B>(j0 * 1024); dmao<0>(sb0, vb0, sob + k1);
    { const unsigned ix = b0 >> 3, q4 = (b0 >> 1) & 3;
      const char* p0 = (j0 & 1) ? (const char*)wu : (const char*)wg;
      const char* p1 = (j0 & 1) ? ((ix & 16) ? (const char*)Cim : (const char*)Cre) : (const char*)wd;
      const char* p2 = (j0 & 1) ? (const char*)ws : (const char*)out + q4 * 1048576;
      const char* p3 = (const char*)ws + WS_A2 + (q4 * 2 + (j0 & 1)) * (8 * NKS * 1024);
      const char* pb = (j0 & 4) ? ((j0 & 2) ? p3 : p2) : ((j0 & 2) ? p1 : p0);
      const unsigned off = j0 == 3 ? (ix & 15) * 32768 : ix * (j0 < 3 ? 44032 : j0 == 4 ? 32768 : j0 == 5 ? 24576 : 38400);
      sline((u64)pb, off); }
  }
  auto chunk = [&](int v) { return (perm >> (4 * v)) & 7; };
  const int j = __builtin_amdgcn_readfirstlane(ftid() >> 6), w = j & 3, sb_ = w & 1, pi = w >> 1;
  const int b = lnd(blockIdx.x), xcd = b & 7, idx = b >> 3;
  const int c = (xcd >> 1) * 4 + (idx >> 3), rsel = xcd & 1, gi = idx & 7, g = rsel * 8 + gi;
  auto get_fb = [&]() { const int l = (lnd(gi) + 1) >> 3; return (6 * (g - rsel)) * (1 - l) + (84 + rsel) * l; };
  auto get_ct = [&]() { const int slow = (3 - lnd((int)((u64)wd >> 7))) & 7; return lnd(gi == 7 ? slow : gi == slow ? 7 : gi); };
  const i32x4 sws = mk_srd(ws, WS_BYTES);
  const __amdgpu_buffer_rsrc_t rws = __builtin_amdgcn_make_buffer_rsrc(ws, 0, WS_BYTES, 0x00020000);
  const unsigned flagb = WS_FLAG + c * 16384;
  const unsigned a2pb = WS_A2 + (unsigned)(c * 4 + 2 * pi) * (NKS * 1024);
  const unsigned w4 = w * 4096, w2 = w * 2048, w6 = w * 6144;
  const unsigned so_x = (c * 128 + 64 * (w >> 1) + 16 * (w & 1)) * 1024;
  u64 tag2 = 0;
  auto set_flag = [&](int fi) {
    __builtin_amdgcn_raw_buffer_store_b64((u32x2){(unsigned)tag2, (unsigned)(tag2 >> 32)}, rws, (ftid() & 63) == 0 ? flagb + (g * 8 + fi) * 128 : OOB, 0, 16);
  };
  auto wait_cnt = [&]() {
#pragma unroll 1
    for (unsigned spins = 0; spins < SPIN_MAX; ++spins)
      if (__builtin_amdgcn_readfirstlane(__hip_atomic_load((LAS unsigned*)(size_t)M_CNT, __ATOMIC_RELAXED, __HIP_MEMORY_SCOPE_WORKGROUP)) == 4u) break;
    asm volatile("" ::: "memory");
  };

  if (j < 4) {
    const int lane = ftid() & 63, l5 = lane & 31, h = lane >> 5, blk = (lane >> 4) & 1, q = (lane & 15) >> 2, pp = lane & 3;
    unsigned vx[4], vw[3], ra[4], wt[2];
    const i32x4 sw = mk_srd((w >> 1) ? wu : wg, SD * SF * 2);
    const unsigned ld_w = ((w >> 1) ? OFF_WU : 0) + (w & 1) * 6144;
    unsigned so_w = 0;
    u32x4 sel;
    {
      const unsigned sw0 = ((lane & 7) ^ (lane >> 4)) * 16;
      for (int r = 0; r < 4; ++r) vx[r] = (32 * (lane >> 5) + 8 * ((lane >> 3) & 1) + ((lane >> 4) & 1) + r) * 1024 + (sw0 ^ ((r & 1) * 64));
      auto ldX = [&](auto S) { constexpr int s = S;
        const unsigned so = so_x + chunk(s) * 128;
        setm0i<((s + 1) % 6) * SLT>(w4);
        dmao<0>(sx, vx[0], so); dmao<1024>(sx, vx[1], so); dmao<2048>(sx, vx[2], so); dmao<3072>(sx, vx[3], so);
      };
      u32x2 ep2;
      STAMP(1)
      if (w == 0 && lane == 0) *(LAS unsigned*)(size_t)M_CNT = 0;
      const unsigned xa = w * 4096 + lane * 16;
      unsigned ti;
      auto setup = [&]() {
        for (int s = 0; s < 4; ++s) { ra[s] = pi * 8192 + l5 * 128 + (((2 * (s ^ pi) + h) ^ ((l5 >> 1) & 7)) * 16); pin(ra[s]); }
        for (int p = 0; p < 2; ++p) { wt[p] = 3 * SLT + (blk ? OFF_WU : 0) + (8 * h + q) * 192 + (((pp & 1) << 1) | (pp >> 1)) * 8 + sb_ * 96 + (p ? -pi * 3072 : pi * 3072); pin(wt[p]); }
        const unsigned selv = (((lane & 15) == 0 && blk == 0) || ((lane & 15) == 4 && blk == 1)) ? 0x3f803f80u : 0u; sel = (u32x4){selv, selv, selv, selv}; pin(sel);
        ti = (lane & 4) ? M_CNT + 64 - M_ST : (64 * (w >> 1) + 32 * (lane >> 5) + 16 * (w & 1) + 4 * (lane & 3) + ((lane >> 3) & 3)) * 8;
        pin(ti);
      };
      auto setup2 = [&]() { const int q12 = (lane * 43) >> 9, r12 = lane - 12 * q12;
        for (int i = 0; i < 3; ++i) { const int r = r12 + 4 * i, cy = r >= 12, col = r - 12 * cy;
          vw[i] = (gi == 7 && col >= 2) ? OOB : (5 * i + q12 + cy) * (SF * 2) + col * 16 - i * 1024; pin(vw[i]); }
        so_w = (w & 1) * 32 * (SF * 2) + get_fb() * 32; };
      float s1[4] = {}, s2[4] = {};
      auto stats = [&](auto S, auto&& mid) { constexpr int s = S;
        u32x4 xr[4];
        for (int k = 0; k < 4; ++k) xr[k] = lds_r128(((s + 1) % 6) * SLT + k * 1024 + xa);
        sb(); mid(); sb();
        for (int e = 0; e < 4; ++e) for (int i = 0; i < 4; ++i) { const unsigned u = xr[i][e]; s1[i] = dot2(u, 0x3f803f80u, s1[i]); s2[i] = dot2(u, u, s2[i]); }
        for (int i = 0; i < 4; ++i) { pin(s1[i]); pin(s2[i]); }
        sb(); };
      wait_vm<3>(); wait_lgkm0(); STA(46) bar(); STA(47)
      stats(IC(0), [&] { ldX(IC(2)); ld64_sc1(ep2, sws, 0, WS_E2 + c * 128); });
      wait_vm<5>(); STA(48) bar(); STA(49)
      stats(IC(1), [&] { ldX(IC(3)); setup(); });
      sfor<2, 8>([&](auto S) { constexpr int s = S;
        wait_vm<(s < 7 ? 4 : 0)>();
        stats(S, [&] { if constexpr (s < 6) ldX(IC(s + 2)); if constexpr (s == 6) setup2(); });
        if constexpr (s < 7) { STA(46 + 2 * s) bar(); STA(47 + 2 * s) }
      });
      pin(ep2); tag2 = utag(ep2);
      float t1[2], t2[2];
      for (int k = 0; k < 2; ++k) { const bool o = lane & 1;
        t1[k] = (o ? s1[2 * k + 1] : s1[2 * k]) + dppf<0xB1>(o ? s1[2 * k] : s1[2 * k + 1]); t2[k] = (o ? s2[2 * k + 1] : s2[2 * k]) + dppf<0xB1>(o ? s2[2 * k] : s2[2 * k + 1]); }
      const bool o = lane & 2;
      float sm = (o ? t1[1] : t1[0]) + dppf<0x4E>(o ? t1[0] : t1[1]), sq = (o ? t2[1] : t2[0]) + dppf<0x4E>(o ? t2[0] : t2[1]);
      sm += dppf<0x104>(sm); sq += dppf<0x104>(sq);
      const float mean = sm * (1.f / SD);
      const float var = fmaxf(sq * (1.f / SD) - mean * mean, 0.f);
      const float sd = rbf(__builtin_amdgcn_sqrtf(rbf(rbf(var) + 1e-5f)));
      const float r_ = __builtin_amdgcn_rcpf(sd); const unsigned nm = pk2(-mean, -mean);
      *(LAS f32x2*)(size_t)(M_ST + ti) = (f32x2){r_, lo2f(nm) * r_}; *(LAS f32x2*)(size_t)(M_SD + ti) = (f32x2){sd, __uint_as_float(nm)};
    }
    f32x16 acc[3][2];
    f32x4 csg[3] = {};
    u32x4 af[2][2]; u32x2 wf[2][3][2];
    auto rd1 = [&](auto N, auto S, auto I) { constexpr int n = N, s = S, i = I;
      const unsigned a_ = ra[s] + (n % 3) * SLT, t_ = wt[s & 1] + (n % 3) * SLT + s * 3072;
      if constexpr (i == 0) { wf[s & 1][0][0] = lds_tr(t_); wf[s & 1][0][1] = lds_tr(t_ + 768); }
      if constexpr (i == 1) { af[s & 1][0] = lds_r128(a_); af[s & 1][1] = lds_r128(a_ + 4096); }
      if constexpr (i == 2) { wf[s & 1][1][0] = lds_tr(t_ + 32); wf[s & 1][1][1] = lds_tr(t_ + 32 + 768); }
      if constexpr (i == 3) { wf[s & 1][2][0] = lds_tr(t_ + 64); wf[s & 1][2][1] = lds_tr(t_ + 64 + 768); }
    };
    auto W = [&](int p, int k) { return (u32x4){wf[p][k][0][0], wf[p][k][0][1], wf[p][k][1][0], wf[p][k][1][1]}; };
    STAMP(22)
    wait_vm<0>(); wait_lgkm0(); bar();
    STAMP(2)
    sfor<0, 4>([&](auto I) { rd1(IC(0), IC(0), I); });
    sfor<0, 32>([&](auto K) { constexpr int n = K / 4, s = K % 4, nn = (K + 1) / 4, sn = (K + 1) % 4;
      sfor<0, 6>([&](auto I) { constexpr int i = I;
        acc[i >> 1][i & 1] = mfma(W(s & 1, i >> 1), af[s & 1][i & 1], K == 0 ? (f32x16){} : acc[i >> 1][i & 1]);
        sb();
        if constexpr (s == 3 && n < 7) { if constexpr (i == 0) { if constexpr (n >= 1) wait_vm<(n < 6 ? 10 : 0)>(); wait_lgkm0(); STB(32 + 2 * n) bar(); STB(33 + 2 * n) } if constexpr (i >= 1 && i < 5) rd1(IC(nn), IC(sn), IC(i - 1)); }
        else if constexpr (K < 31 && i < 4) rd1(IC(nn), IC(sn), I);
        if constexpr (i >= 4 && K < 23) {
          constexpr int xn = (K + 1) / 4 + 2, kk = (K + 1) % 4, o0 = i == 5 ? (kk == 0 ? 0 : 3 * kk) : (kk == 0 ? 0 : 3 * kk - 1), o1 = i == 5 ? (kk == 3 ? 10 : 3 * kk + 2) : (kk == 0 ? 0 : 3 * kk);
          sfor<o0, o1>([&](auto O) { constexpr int o = O;
            if constexpr (o < 4) { if constexpr (xn > 2) { if constexpr (o == 0) setm0i<(xn % 3) * SLT>(w4); dmao<o * 1024>(sx, vx[o], so_x + chunk((xn + 5) & 7) * 128); } }
            else { constexpr int u = o - 4; if constexpr (u % 3 == 0) setm0i<(3 + xn % 3) * SLT + u * 1024>(ld_w);
              dmao<(u % 3) * 1024>(sw, vw[u % 3], so_w + chunk((xn + 5) & 7) * (64 * SF * 2) + (u / 3) * 16 * (SF * 2)); } }); }
        if constexpr (K == 31 && i == 1) { if (lane < 32) for (int k = 0; k < 3; ++k) *(LAS float*)(size_t)(M_CS + (w * 3 + k) * 128 + lane * 4) = csg[k][0]; }
        if constexpr (K == 31 && i == 4) { wait_lgkm0();
          if (lane == 0) __hip_atomic_fetch_add((LAS unsigned*)(size_t)M_CNT, 1u, __ATOMIC_RELAXED, __HIP_MEMORY_SCOPE_WORKGROUP); }
        sb();
      });
      if constexpr (!(s & 1)) { for (int k = 0; k < 3; ++k) csg[k] = mfma16(sel, W(s & 1, k), csg[k]); sb(); }
    });
    STAMP(8)
    f32x2 st[2], sd[2];
    { const unsigned ia = (64 * pi + 32 * ((lane >> 2) & 1) + 4 * ((lane >> 3) & 3) + (lane & 3)) * 8;
      for (int rb = 0; rb < 2; ++rb) { st[rb] = *(LAS f32x2*)(size_t)(M_ST + ia + rb * 128); sd[rb] = *(LAS f32x2*)(size_t)(M_SD + ia + rb * 128); } }
    wait_cnt();
    for (int k = 0; k < 3; ++k) {
      float cs = csg[k][0] + *(LAS float*)(size_t)(M_CS + ((w ^ 2) * 3 + k) * 128 + l5 * 4);
      cs = h ? 0.f : cs;
      const float c0 = rbf(cs), r1 = cs - c0, c1 = rbf(r1), c2 = r1 - c1;
      const u32x4 cf = {pk2(c0, c1), pk2(c2, 0.f), 0u, 0u};
      for (int rb = 0; rb < 2; ++rb) { const unsigned nm = __float_as_uint(sd[rb][1]); acc[k][rb] = mfma(cf, (u32x4){nm, nm, 0u, 0u}, acc[k][rb]); }
    }
    for (int k = 0; k < 3; ++k) { pin(acc[k][0]); pin(acc[k][1]); }
    asm volatile("s_nop 11" ::: "memory");
    for (int k = 0; k < 3; ++k) { pin(acc[k][0]); pin(acc[k][1]); }
    __builtin_amdgcn_s_setprio(CPE);
    STAMP(7)
    const int fb = get_fb();
    sfor<0, 3>([&](auto K) { constexpr int k = K;
      const unsigned vst = (gi < 7 || 3 * sb_ + k == 0) ? a2pb + lane * 16 + (64 + fb + 3 * sb_ + k) * 1024 : OOB;
      sfor<0, 2>([&](auto R_) { constexpr int rb = R_;
        const float rs = st[rb][0], k1 = -2.302208198f * rs, k3 = -0.1029432397f * rs * rs * rs, s2 = sd[rb][0] * sd[rb][0];
        unsigned o[4];
        for (int i = 0; i < 4; ++i) o[i] = geglu2(acc[k][rb][2 * i], acc[k][rb][2 * i + 1], acc[k][rb][8 + 2 * i], acc[k][rb][9 + 2 * i], k1, k3, s2);
        __builtin_amdgcn_raw_buffer_store_b128((u32x4){o[0], o[1], o[2], o[3]}, rws, vst + rb * NKS * 1024, 0, 16);
      });
    });
    sb();
    STAMP(9)
    wait_vm<0>();
    set_flag(4 + w);
    STAMP(10)
    const int t = ftid(), ch = t & 15, r4 = t >> 4, ct = get_ct();
    const int trow = 64 * rsel + 32 * ((r4 >> 2) & 1) + 8 * (r4 & 1) + 2 * (r4 >> 3) + ((r4 >> 1) & 1);
    const int irow = 64 * rsel + 32 * ((r4 >> 2) & 1) + 4 * (r4 >> 3) + (r4 & 3);
    u32x2 xv[4], dk;
    for (int it = 0; it < 4; ++it) ld64(xv[it], sx, trow * 1024 + ch * 8, c * 131072 + ct * 128 + (4 * (it & 1) + 16 * (it >> 1)) * 1024);
    ld64(dk, mk_srd(dskip, SD * 2), ch * 8, ct * 128);
    wait_vm<0>(); for (int it = 0; it < 4; ++it) pin(xv[it]); pin(dk);
    f32x4 base[4]; unsigned ph[4];
    for (int it = 0; it < 4; ++it) { const f32x2 s2 = *(LAS f32x2*)(size_t)(M_ST + (irow + 8 * (it & 1) + 16 * (it >> 1)) * 8);
      for (int e = 0; e < 4; ++e) { const unsigned xu = xv[it][e >> 1], du = dk[e >> 1];
        const float xe = (e & 1) ? hi2f(xu) : lo2f(xu), de = (e & 1) ? hi2f(du) : lo2f(du);
        base[it][e] = xe + rbf(rbf(__builtin_fmaf(xe, s2[0], s2[1])) * de); }
      const int e = 2 * it + (r4 >> 3), d = (e * 11) >> 5;
      ph[it] = (3 + d) * SLT + (e - 3 * d) * 2048 + (r4 & 7) * 256 + (ch ^ (r4 & 7)) * 16;
      pin(base[it]); pin(ph[it]); }
    float* op = out + (c * 128 + trow) * SD + ct * 64 + ch * 4;
    const __amdgpu_buffer_rsrc_t rout = __builtin_amdgcn_make_buffer_rsrc(out, 0, SL * SD * 4, 0x00020000);
    const unsigned oo = ((c * 128 + trow) * SD + ct * 64 + ch * 4) * 4;
    bar();
    STAMP(11)
    for (int it = 0; it < 4; ++it) {
      f32x4 p[4];
      for (int ww = 0; ww < 4; ++ww) p[ww] = *(LAS f32x4*)(size_t)(ph[it] + ww * 6144);
      __builtin_amdgcn_raw_buffer_store_b128(__builtin_bit_cast(u32x4, base[it] + ((p[0] + p[1]) + (p[2] + p[3]))), rout, oo + (4 * (it & 1) + 16 * (it >> 1)) * SD * 4, 0, OAUX);
    }
    if (g == 0) __builtin_amdgcn_raw_buffer_store_b64((u32x2){(unsigned)tag2, (unsigned)(tag2 >> 32)}, rws, t == 0 ? WS_E2 + c * 128 : OOB, 0, 16);
    DG(5)
    if (DIAG == 6) { dgn_ = dg0_ + 250; }
    if (DIAG) { const u64 e_ = __builtin_amdgcn_s_memrealtime() + 4 * (dgn_ - dg0_) + (DIAG == 1 ? 1000 : 0); while (__builtin_amdgcn_s_memrealtime() < e_) __builtin_amdgcn_s_sleep(1); }
  } else {
    const int lane = ftid() & 63, l5 = lane & 31, h = lane >> 5, blk = (lane >> 4) & 1, q = (lane & 15) >> 2, pp = lane & 3;
    const unsigned va = lane * 16;
    const unsigned sw0 = ((lane & 7) ^ (lane >> 4)) * 16;
    unsigned vb[2], vw[3], vd[2];
    for (int r = 0; r < 2; ++r) vb[r] = ((lane >> 3) + 7 * r) * 1024 + (sw0 ^ (r * 64));
    const i32x4 sbm = mk_srd((w >> 1) ? Bim : Bre, SP * SD * 2);
    const unsigned so_b = (g * 32 + (w & 1) * 16) * 1024;
    auto ldB = [&](auto S) { constexpr int s = S;
      const unsigned so = so_b + chunk(s) * 128;
      setm0i<((s + 1) % 6) * SLT + OFF_B>(w2); dmao<0>(sbm, vb[0], so); dmao<1024>(sbm, vb[1], so); };
    u32x2 ep2, ep1; unsigned plr, pli, pdt;
    ld64_sc1(ep2, sws, 0, WS_E2 + c * 128);
    ld64_sc1(ep1, sws, 0, WS_E1 + g * 128);
    const int ps = (lane & 3) | ((lane & 4) << 1) | ((lane & 8) >> 1);
    { const unsigned po = (g * 32 + sb_ * 16 + ps) * 2;
      ld16(plr, mk_srd(lam_re, SP * 2), po); ld16(pli, mk_srd(lam_im, SP * 2), po); ld16(pdt, mk_srd(logdt, SP * 2), po); }
    { u64 pc; unsigned len, back;
      asm volatile("s_getpc_b64 %0\n.Lpf_here:\n\ts_mov_b32 %1, .Lpf_end-.Lpf_beg\n\ts_mov_b32 %2, .Lpf_here-.Lpf_beg" : "=s"(pc), "=s"(len), "=s"(back));
      for (unsigned o = (idx * 4 + w) * 128; o + 128 < len; o += 16384) sline((pc - back) & ~127ull, o + 128); }
    const i32x4 sw = mk_srd((w >> 1) ? wu : wg, SD * SF * 2);
    const unsigned so_w = (w & 1) * 32 * (SF * 2) + get_fb() * 32;
    const unsigned ld_w = ((w >> 1) ? OFF_WU : 0) + (w & 1) * 6144;
    auto opW = [&](auto N, auto I) { constexpr int n = N, i = I;
      if constexpr (i % 3 == 0) setm0i<(3 + n % 3) * SLT + i * 1024>(ld_w);
      dmao<(i % 3) * 1024>(sw, vw[i % 3], so_w + chunk((n + 5) & 7) * (64 * SF * 2) + (i / 3) * 16 * (SF * 2)); };
    unsigned ra[4], wa[4];
    for (int s = 0; s < 4; ++s) { ra[s] = pi * 8192 + l5 * 128 + (((2 * s + h) ^ ((l5 >> 1) & 7)) * 16);
      const int row = sb_ * 16 + ps; wa[s] = OFF_B + blk * 4096 + row * 128 + (((2 * s + h) ^ ((row >> 1) & 7)) * 16); }
    const unsigned selv = (((lane & 15) == 0 && blk == 0) || ((lane & 15) == 4 && blk == 1)) ? 0x3f803f80u : 0u;
    const u32x4 sel = {selv, selv, selv, selv};
    for (int s = 0; s < 4; ++s) { pin(ra[s]); pin(wa[s]); }
    unsigned t1l = 0, t1h = 0;
    const int cc = 2 * c + pi;
    float ar, ai, nai, kr, nkr, nki, a8r, a8i, a16r, a16i, a32r, a32i, a64r, a64i, pwr = 1.f, pwi = 0.f, a256r, a256i, mr = 1.f, mi = 0.f;
    u32x4 oh[2];
    float lr, li;
    auto params = [&](auto V) { constexpr int v = V;
      if constexpr (v == 1) { const int q12 = (lane * 43) >> 9, r12 = lane - 12 * q12;
        for (int i = 0; i < 3; ++i) { const int r = r12 + 4 * i, cy = r >= 12, col = r - 12 * cy;
          vw[i] = (gi == 7 && col >= 2) ? OOB : (5 * i + q12 + cy) * (SF * 2) + col * 16 - i * 1024; pin(vw[i]); } }
      if constexpr (v == 2) {
        pin(ep2); pin(ep1); pin(plr); pin(pli); pin(pdt);
        tag2 = utag(ep2);
        const u64 tag1 = utag(ep1); t1l = (unsigned)tag1; t1h = (unsigned)(tag1 >> 32); }
      if constexpr (v == 3) {
        lr = lo2f(plr); li = lo2f(pli);
        const float dt = rbf(__expf(lo2f(pdt)));
        const float er = __expf(dt * lr);
        const float rev = dt * li * 0.15915494309189535f;
        const float fr = rev - __builtin_rintf(rev);
        ar = er * __builtin_amdgcn_cosf(fr); ai = er * __builtin_amdgcn_sinf(fr);
        pin(ar); pin(ai); }
      if constexpr (v == 4) { a8r = ar; a8i = ai;
        for (int i = 0; i < 3; ++i) csq(a8r, a8i, a8r, a8i);
        csq(a16r, a16i, a8r, a8i); pin(a16r); pin(a16i); }
      if constexpr (v == 6) {
        const float nr = ar - 1.f, ni = ai, den = 2.f * __builtin_amdgcn_rcpf(vfma(lr, lr, li * li));
        kr = vfma(nr, lr, ni * li) * den; nki = vfma(nr, li, -(ni * lr)) * den; nkr = -kr; nai = -ai;
        pin(nai); pin(kr); pin(nkr); pin(nki); }
      if constexpr (v == 10) { csq(a32r, a32i, a16r, a16i); csq(a64r, a64i, a32r, a32i); if (blk) { pwr = a16r; pwi = a16i; mr = a64r; mi = a64i; } }
      if constexpr (v == 11) { float tr = pwr, ti = pwi; cf4(tr, ti, a32r, a32i, 0.f, 0.f); if (h) { pwr = tr; pwi = ti; } csq(a32r, a32i, a64r, a64i); }
      if constexpr (v == 12) { float ur = mr, ui = mi; cf4(ur, ui, a32r, a32i, 0.f, 0.f); if (h) { mr = ur; mi = ui; } csq(a256r, a256i, a32r, a32i); }
      if constexpr (v == 13 || v == 14) { constexpr int u = v - 13;
        const unsigned val = (((lane >> 2) & 1) == h && blk == u) ? ((lane & 1) ? 0x3f800000u : 0x3f80u) : 0u; const int d0 = ((lane >> 1) & 1) | ((lane >> 2) & 2);
        for (int d = 0; d < 4; ++d) oh[u][d] = d0 == d ? val : 0u;
        pin(oh[u]); }
      sb();
    };
    f32x16 sa[2] = {};
    f32x4 csa = {};
    { u32x4 af[2][4][2], wf[2][4];
      auto rdA = [&](int p, int s, unsigned slot) { wf[p][s] = lds_r128(slot + wa[s]); af[p][s][0] = lds_r128(slot + ra[s]); af[p][s][1] = lds_r128(slot + ra[s] + 4096); };
      auto mmA = [&](int p, int s) { sa[0] = mfma(af[p][s][0], wf[p][s], sa[0]); sa[1] = mfma(af[p][s][1], wf[p][s], sa[1]); pin(sa[0]); pin(sa[1]); };
      auto csA = [&](int p) { for (int s = 0; s < 4; ++s) csa = mfma16(sel, wf[p][s], csa); pin(csa); sb(); };
      auto W3 = [&](auto N, auto H_) { sfor<0, 3>([&](auto I) { opW(N, IC(3 * H_ + I)); }); };
      unsigned rh[4], wh[4];
      for (int s = 0; s < 4; ++s) { rh[s] = ra[s] + 3 * SLT; wh[s] = wa[s] + 3 * SLT - 8192; pin(rh[s]); pin(wh[s]); wh[s] += 8192; wa[s] -= 8192; pin(wa[s]); wa[s] += 8192; }
      ldB(IC(2));
      __builtin_amdgcn_s_setprio(HPA);
      wait_vm<10>(); STA(46) bar(); STA(47) for (int s = 0; s < 4; ++s) rdA(0, s, SLT); sb(); ldB(IC(3));
      sfor<1, 8>([&](auto V) { constexpr int v = V, p = v & 1, sl = (v + 1) % 6;
        wait_vm<(v == 1 ? 9 : WEARLY ? (v < 4 ? 4 : v == 4 ? 7 : v == 5 ? 10 : v == 6 ? 11 : 6) : (v < 5 ? 4 : v == 5 ? 7 : v == 6 ? 11 : 6))>(); wait_lgkm0();
        if constexpr (v == 7) __builtin_amdgcn_s_setprio(3);
        STA(46 + 2 * v) bar(); STA(47 + 2 * v)
        sfor<0, 8>([&](auto I) { constexpr int i = I;
          sa[i & 1] = mfma(af[p ^ 1][i >> 1][i & 1], wf[p ^ 1][i >> 1], sa[i & 1]); pin(sa[i & 1]);
          sb();
          if constexpr (i < 6) sfor<2 * i, 2 * i + 2>([&](auto R) { constexpr int r = R, s = r / 3, k = r % 3, o = (sl % 3) * SLT;
            if constexpr (k == 0) wf[p][s] = lds_r128((sl < 3 ? wa[s] : wh[s]) + o); else af[p][s][k - 1] = lds_r128((sl < 3 ? ra[s] : rh[s]) + o + (k - 1) * 4096); });
          if constexpr (i == 6 && v < 5) ldB(IC(v + 3));
          if constexpr (i == 7 && v == 4) W3(IC(0), IC(0));
          if constexpr (i == 6 && v == 5) W3(IC(0), IC(1));
          if constexpr (i == 7 && v == 5) W3(IC(1), IC(0));
          if constexpr (i == 6 && v == 6) W3(IC(1), IC(1));
          sb(); });
        csA(p ^ 1);
        params(V);
      });
      STAMP(2)
      for (int s = 0; s < 4; ++s) mmA(1, s);
      csA(1);
      __builtin_amdgcn_s_setprio(HPB);
    }
    const int kb = 22 * w - (w == 3), cnt = w < 2 ? 22 : 21, ct = get_ct();
    const i32x4 swd = mk_srd(wd + kb * 8192, cnt * 16384);
    const unsigned so_c = ct * 65536 + w * 256;
    const unsigned r2 = (xcd * 11) >> 2;
    auto kofs = [&](int p, unsigned sc_) { const unsigned r = lnd(r2 * sc_), a = r + p * sc_, b2 = r + (p - 22) * sc_; return a < b2 ? a : b2; };
    for (int r = 0; r < 2; ++r) vd[r] = ((lane >> 3) + 7 * r) * 1024 + (((lane & 7) ^ (((lane >> 4) & 1) << 2)) * 16) + ct * 128;
    unsigned so_t = 0; i32x4 sc = swd;
    auto wd_op = [&](auto M, auto E, auto R) { constexpr int m = M, e = E, r = R;
      if constexpr (r == 0) { so_t = kofs(m < 18 ? (m + 14) % 18 : m, 16384); setm0i<cell_c(e)>(cell_k(e) == 0 ? w6 : cell_k(e) == 1 ? w4 : w2); }
      dmao<r * 1024>(swd, vd[r], so_t);
    };
    auto dma_wd = [&](auto M, auto E) { wd_op(M, E, IC(0)); wd_op(M, E, IC(1)); };
    auto c_op = [&](auto N, auto I) { constexpr int n = N, i = I;
      if constexpr (i == 0) { const int kg = ((n >> 1) + lnd(xcd)) & 3; so_t = so_c + (n & 1) * 32768 + (kg & 1) * 128; sc = mk_srd((kg >> 1) ? Cim : Cre, SD * SP * 2); }
      if constexpr (!(i & 1)) setm0i<(n & 1) ? ((i >> 1) ? M_T : cell_c(15)) : cell_c(16 + (i >> 1))>(w2);
      dmao<(i & 1) * 1024>(sc, vb[i & 1], so_t + (i >> 1) * 16384);
    };
    unsigned rcx[4];
    for (int s = 0; s < 4; ++s) rcx[s] = OFF_B + w * 2048 + blk * SLT + (lane & 15) * 128 + (((2 * s + h) ^ ((l5 >> 1) & 7)) * 16);
    const unsigned rcd = 2 * SLT + blk * (M_T - cell_c(15) - SLT);
    auto rd_c = [&](int n, u32x4* d) { for (int s = 0; s < 4; ++s) d[s] = lds_r128(rcx[s] + ((n & 1) ? rcd : 0u)); };
    const unsigned granb = WS_GRAN + (g * 2 + sb_) * 8192;
    const i32x4 sgr = mk_srd(ws + granb, 8192);
    const unsigned vg = (32 - cc) * 256 + va;
    float R[16], I_[16], csr, csi, er[2], ei[2], sr[4], si[4], prer, prei, cr, ci;
    f32x4 tb[8];
    u32x4 ge[8], fr4[4];
    f32x16 td[2];
    unsigned hp[2][8];
    unsigned nbad = 0, fo_g = 0, pa[2], tdl = 0;
    auto side = [&](auto P_) { constexpr int P = P_;
      if constexpr (P == 0) { for (int k = 0; k < 8; ++k) tb[k] = *(LAS f32x4*)(size_t)(M_ST + pi * 512 + (lane >> 4) * 128 + k * 16);
        csr = csa[0]; float t = csr; swap32(csr, t); csi = csr; swap16(csr, csi); }
      if constexpr (P >= 1 && P < 9) { constexpr int k = P - 1;
        for (int r = 0; r < 2; ++r) { R[2 * k + r] = sa[0][2 * k + r]; I_[2 * k + r] = sa[1][2 * k + r]; swap16(R[2 * k + r], I_[2 * k + r]); }
        for (int r = 0; r < 2; ++r) sc4(R[2 * k + r], I_[2 * k + r], tb[k][2 * r], tb[k][2 * r + 1], csr, csi); }
      if constexpr (P == 9) { for (int r = 0; r < 2; ++r) { er[r] = R[r]; ei[r] = I_[r]; cf4(er[r], ei[r], ar, ai, R[2 + r], I_[2 + r]); } }
      if constexpr (P >= 16 && P < 22) { constexpr int k = P - 14; for (int r = 0; r < 2; ++r) cf4(er[r], ei[r], ar, ai, R[2 * k + r], I_[2 * k + r]); }
      if constexpr (P == 22) { sr[0] = er[0]; si[0] = ei[0]; cf4(sr[0], si[0], a8r, a8i, er[1], ei[1]); sr[1] = sr[0]; swap16(sr[0], sr[1]); si[1] = si[0]; swap16(si[0], si[1]); }
      if constexpr (P == 23) { sr[2] = sr[0]; swap32(sr[0], sr[2]); si[2] = si[0]; swap32(si[0], si[2]); sr[3] = sr[1]; swap32(sr[1], sr[3]); si[3] = si[1]; swap32(si[1], si[3]); }
      if constexpr (P == 24) { cr = sr[0]; ci = si[0]; cf4(cr, ci, a16r, a16i, sr[1], si[1]); sr[1] = cr; si[1] = ci; cf4(cr, ci, a16r, a16i, sr[2], si[2]); sr[2] = cr; si[2] = ci; }
      if constexpr (P == PPUB) { cf4(cr, ci, a16r, a16i, sr[3], si[3]);
        __builtin_amdgcn_raw_buffer_store_b128((u32x4){__float_as_uint(cr), t1l, __float_as_uint(ci), t1h}, rws, lane < 16 ? granb + (31 - cc) * 256 + va : OOB, 0, 16); }
      if constexpr (P == 32) { prer = h ? (blk ? sr[2] : sr[1]) : (blk ? sr[0] : 0.f); prei = h ? (blk ? si[2] : si[1]) : (blk ? si[0] : 0.f); }
      sfor<0, 16>([&](auto K) { constexpr int k = K; if constexpr (P == nxt(33, k < 4 ? k / 2 : k - 2)) kc4(R[k], I_[k], kr, nkr, nki); });
      if constexpr (P >= 53 && P < 58) params(IC(P - 43));
      if constexpr (P == PG) sfor<0, 8>([&](auto K) { constexpr int k = K; ld128_sc1<(k & 3) * 1024>(ge[k], sgr, vg, (k >> 2) * 4096); });
      if constexpr (P == PC) { STB(23) wait_vm<hb_cnt(PC, false) - hb_cnt(PG, true)>(); for (int i = 0; i < 8; ++i) pin(ge[i]); STB(24) }
      if constexpr (P == PC + 3) STB(25) if constexpr (P == PC + 6) STB(26) if constexpr (P == PC + 9) STB(27)
      if constexpr (P == PC || P == PC + 1) { constexpr int i = 4 * (P - PC); miss2(nbad, ge[i], ge[i + 1], t1l, t1h); miss2(nbad, ge[i + 2], ge[i + 3], t1l, t1h); }
      if constexpr (P == PC + 2) { const unsigned nout = 8 - ((cc + 3 - (lane >> 4)) >> 2);
#pragma unroll 1
        for (unsigned spins = 0; spins < SPIN_MAX; ++spins) {
          if (__all(nbad == nout)) break;
          STAMP(28)
          __builtin_amdgcn_s_sleep(SLP);
          sfor<0, 8>([&](auto I) { constexpr int i = I; ld128_sc1_rw<(i & 3) * 1024>(ge[i], sgr, vg, (i >> 2) * 4096); });
          wait_vm<0>(); for (int i = 0; i < 8; ++i) pin(ge[i]);
          nbad = 0;
          for (int i = 0; i < 8; i += 2) miss2(nbad, ge[i], ge[i + 1], t1l, t1h);
        }
        cr = __uint_as_float(ge[7][0]); ci = __uint_as_float(ge[7][2]); cf4(cr, ci, a256r, a256i, __uint_as_float(ge[6][0]), __uint_as_float(ge[6][2])); }
      if constexpr (P >= PC + 3 && P < PC + 6) { constexpr int i = 5 - 2 * (P - PC - 3);
        cf4(cr, ci, a256r, a256i, __uint_as_float(ge[i][0]), __uint_as_float(ge[i][2])); cf4(cr, ci, a256r, a256i, __uint_as_float(ge[i - 1][0]), __uint_as_float(ge[i - 1][2])); }
      if constexpr (P == PC + 6) { cf4(cr, ci, mr, mi, 0.f, 0.f); float t = cr, u = ci; swap16(cr, t); swap16(ci, u); cr = vadd(cr, t); ci = vadd(ci, u); }
      if constexpr (P == PC + 7) { float t = cr, u = ci; swap32(cr, t); swap32(ci, u); cr = vadd(cr, t); ci = vadd(ci, u); cf4(cr, ci, pwr, pwi, prer, prei); }
      if constexpr (P == PC + 8) { sr[0] = cr; si[0] = ci; cf4(cr, ci, a8r, a8i, er[0], ei[0]); er[0] = sr[0]; ei[0] = si[0]; er[1] = cr; ei[1] = ci; }
      if constexpr (P == PC + 9) { kc4(er[0], ei[0], kr, nkr, nki); kc4(er[1], ei[1], kr, nkr, nki); }
      if constexpr (P % 16 == 4 && P / 16 >= PFW_S && P / 16 < 6) sfor<0, PFW>([&](auto U) { constexpr int k = (P / 16 - PFW_S) * PFW + U + PFW_O, m = k < 4 ? 18 + k : k < 18 ? k : k - 18;
        if constexpr (k < 22) { const unsigned o = kofs(m < 18 ? (m + 14) % 18 : m, 16384); if (o < (unsigned)cnt * 16384) for (int r = 0; r < 4; ++r) sline((u64)(wd + kb * 8192), o + ct * 128 + (4 * (c & 3) + r) * 1024); } });
      sfor<0, 8>([&](auto K) { constexpr int k = K; if constexpr (P == nxt(P2, k)) {
        for (int r = 0; r < 2; ++r) cf4c(k ? R[2 * k - 2 + r] : er[r], k ? I_[2 * k - 2 + r] : ei[r], ar, ai, nai, R[2 * k + r], I_[2 * k + r]);
        hp[0][k] = pk2(R[2 * k], R[2 * k + 1]); hp[1][k] = pk2(I_[2 * k], I_[2 * k + 1]); } });
      static_assert(PT > nxt(P2, 7) && PS > PT && PF > PS && PC % 16 == 0, "scan schedule");
      if constexpr (P == PT) {
        __builtin_amdgcn_s_setprio(3);
        for (int part = 0; part < 2; ++part) {
          td[part] = mfma((u32x4){hp[part][0], hp[part][1], hp[part][2], hp[part][3]}, oh[0], (f32x16){});
          td[part] = mfma((u32x4){hp[part][4], hp[part][5], hp[part][6], hp[part][7]}, oh[1], td[part]); pin(td[part]); }
        __builtin_amdgcn_s_setprio(HPB); }
      if constexpr (P == PS) { for (int k = 0; k < 4; ++k) for (int d = 0; d < 4; ++d) fr4[k][d] = pk2(td[k >> 1][8 * (k & 1) + 2 * d], td[k >> 1][8 * (k & 1) + 2 * d + 1]); }
      if constexpr (P == PS) { for (int k = 0; k < 4; ++k)
          __builtin_amdgcn_raw_buffer_store_b128(fr4[k], rws, a2pb + (2 * g + sb_) * 1024 + va + ((k & 1) * NKS + (k >> 1) * 32) * 1024, 0, 16); }
      if constexpr (P == 100) { const int ks = kb + lane, p6 = (ks * 43) >> 8, pg = ks < 84 ? p6 + (p6 >= 7) : 7 + 8 * (ks - 84), pb = ks < 84 ? ks - 6 * p6 : 0;
        fo_g = lane < cnt ? (pg * 8 + 4 + 2 * rsel + (pb >= 3)) * 128 : OOB; pin(fo_g); }
      if constexpr (P == 104) { for (int rb = 0; rb < 2; ++rb) { const int e = 4 * rb + (l5 >> 3), d = (e * 11) >> 5;
          pa[rb] = (3 + d) * SLT + (e - 3 * d) * 2048 + w6 + (lane & 7) * 256 + ((h ^ (lane & 7)) * 16); pin(pa[rb]); } }
      if constexpr (P == 106) { tdl = (8 * h + q) * 128 + (((blk * 2 + (pp >> 1)) ^ ((q >> 1) << 2)) * 16) + (pp & 1) * 8; pin(tdl); }
      if constexpr (P == PF) { wait_vm<hb_cnt(PF, false) - hb_cnt(PS, true)>(); set_flag(w);
        if (cc == 31) __builtin_amdgcn_raw_buffer_store_b64((u32x2){t1l, t1h}, rws, (lane == 0 && sb_ == 0) ? WS_E1 + g * 128 : OOB, 0, 16); }
    };
    u32x4 cq[8][4];
    u32x2 fs;
    const unsigned fo_s = lane < 8 ? ((4 * w + (lane >> 1)) * 8 + 2 * rsel + (lane & 1)) * 128 : OOB;
    sfor<0, 8>([&](auto N) { constexpr int n = N, g0 = hb_g0(n), done = n ? hb_cnt(16 * n - 1, true) : 0;
      if constexpr (n > 0) { if constexpr (n == 1) wait_vm<done>(); STB(30 + 2 * n) bar(); STB(31 + 2 * n) } else STB(31)
      if constexpr (n > 1) { wait_vm<done - hb_cnt(16 * (n > 1 ? n - 2 : 0) + hb_g0(n - 2) + 3, false)>(); rd_c(n, cq[n - 2]); sb(); }
      sfor<0, hb_ops(n)>([&](auto I) { constexpr int i = I, io = i < g0 ? i : i - 4;
        if constexpr (i >= g0 && i < g0 + 4) { if constexpr (i == g0 && n > 1) { for (int s = 0; s < 4; ++s) use(cq[n - 2][s]); } c_op(N, IC(i - g0)); }
        else if constexpr (n >= 6) { constexpr int e = 5 * (n - 6) + (io >> 1); wd_op(IC(e < 4 ? 18 + e : e), IC(e), IC(io & 1)); }
        if constexpr (16 * n + i == PPOLL) ld64_sc1(fs, sws, fo_s, flagb);
        side(IC(16 * n + i));
        sb();
      });
    });
    __builtin_amdgcn_s_setprio(HPC);
    STAMP(3)
    auto poll = [&](unsigned fo, u32x2 fv) {
#pragma unroll 1
      for (unsigned spins = 0; spins < SPIN_MAX; ++spins) {
        const bool ok = fo == OOB || (fv[0] == (unsigned)tag2 && fv[1] == (unsigned)(tag2 >> 32));
        if (__all(ok)) break;
        __builtin_amdgcn_s_sleep(SLP);
        ld64_sc1(fv, sws, fo, flagb); wait_vm<0>(); pin(fv);
      }
    };
    wait_vm<hb_cnt(127, true) - hb_cnt(16 * 6 + 7, false)>();
    rd_c(0, cq[6]);
    const unsigned a2c = WS_A2 + (unsigned)(c * 4 + 2 * rsel) * (NKS * 1024);
    const i32x4 sg0 = mk_srd(ws + a2c + (64 + kb) * 1024, cnt * 1024), sg1 = mk_srd(ws + a2c + (NKS + 64 + kb) * 1024, cnt * 1024);
    const unsigned vs0 = va + a2c + w * 8192, vs1 = vs0 + NKS * 1024;
    wait_lgkm0();
    dma_wd(IC(16), IC(16)); dma_wd(IC(17), IC(17));
    wait_cnt();
    ld64_sc1(fs, sws, fo_s, flagb);
    sfor<10, 13>([&](auto E) { dma_wd(E, E); });
    wait_vm<6>(); pin(fs);
    STAMP(7)
    poll(fo_s, fs);
    STAMP(4)
    unsigned tb2[4][2];
    auto rd_half = [&](int e, int cb) { const int k = cell_k(e) == 0 ? (cell_c(e) >= 5 * SLT) : cell_k(e) + 1;
      return lds_tr2(tb2[k][cb] + (cell_c(e) - (k == 0 ? 3 * SLT : k == 1 ? 5 * SLT : k == 3 ? OFF_B : 0)), 512); };
    f32x16 acc2[2][2];
    u32x4 L[76], wq[4][2], bf[2][2];
    unsigned so_l = 0;
    auto soL = [&](auto M) { constexpr int m = M;
      if constexpr (m < 16) { if constexpr (!(m & 3)) { const int kg = ((m >> 2) + lnd(xcd)) & 3; so_l = ((kg >> 1) * 32 + (kg & 1) * 4) * 1024; } }
      else so_l = kofs(m - 16, 1024); };
    auto ldH = [&](auto M, auto R) { constexpr int m = M, r = R;
      if constexpr (m < 16) ld128_sc1<(m & 3) * 1024>(L[2 * m + r], sws, r ? vs1 : vs0, so_l); else ld128_sc1<0>(L[2 * m + r], r ? sg1 : sg0, va, so_l); };
    auto ldL = [&](auto M) { soL(M); ldH(M, IC(0)); ldH(M, IC(1)); };
    sfor<0, p2_l0(0)>(ldL);
    sfor<0, 38>([&](auto M) { constexpr int m = M, mm = m - 16;
      constexpr bool one = p2_l0(m + 1) == p2_l0(m) + 1;
      wait_vm<p2_after(m)>(); pin(L[2 * m]); pin(L[2 * m + 1]);
      sfor<0, 4>([&](auto G) { constexpr int gp = G, cb = gp >> 1, rb = gp & 1;
        if constexpr (m < 16) acc2[cb][rb] = mfma(cq[2 * (m >> 2) + cb][m & 3], L[2 * m + rb], m == 0 ? (f32x16){} : acc2[cb][rb]);
        else acc2[cb][rb] = mfma(mm < 18 ? bf[mm & 1][cb] : wq[mm < 18 ? 0 : mm - 18][cb], L[2 * m + rb], acc2[cb][rb]);
        sb();
        if constexpr (gp < 2) {
          if constexpr (m >= 15 && mm + 1 < 18) bf[(mm + 1) & 1][gp] = rd_half((mm + 5) % 18, gp);
          if constexpr (m == 0 || m == 1) wd_op(IC(13 + m), IC(13 + m), G);
          if constexpr (m == 2 || m == 3) { constexpr int k = 2 * (m - 2) + gp;
            for (int u = 0; u < 2; ++u) { tb2[k][u] = (tdl ^ (u * 64)) + (k == 0 ? 3 * SLT + w6 : k == 1 ? 5 * SLT + w6 : k == 2 ? w4 : OFF_B + w2); pin(tb2[k][u]); } }
          if constexpr (m == 4 && gp == 0) rd_c(1, cq[7]);
          if constexpr (m == 5) { if constexpr (gp == 0) { for (int s = 0; s < 4; ++s) use(cq[7][s]); } wd_op(IC(15), IC(15), G); }
          if constexpr (m >= 7 && m <= 10) { if constexpr (gp == 0) { use(wq[m - 7][0]); use(wq[m - 7][1]); } wd_op(IC(m - 7), IC(m - 7), G); }
          if constexpr (m >= 6 && m <= 9) wq[m - 6][gp] = rd_half(m - 6, gp);
          if constexpr (gp == 1 && m == PCK - 1) ld64_sc1(fs, sws, fo_g, flagb);
          if constexpr (gp == 1 && one) soL(IC(p2_l0(m)));
        } else {
          if constexpr (gp == 2 && m == PCK) { STAMP(5) wait_vm<p2_extra(PCK) + 2 * (16 - p2_l0(PCK - 1))>(); pin(fs); poll(fo_g, fs); STAMP(6) }
          if constexpr (one) ldH(IC(p2_l0(m)), IC(gp - 2));
          else if constexpr (gp == 2) sfor<p2_l0(m), p2_l0(m + 1)>(ldL);
        }
        sb();
      });
    });
    STAMP(9)
    for (int rb = 0; rb < 2; ++rb) for (int rq = 0; rq < 4; ++rq) for (int cb = 0; cb < 2; ++cb)
      *(LAS f32x4*)(size_t)((pa[rb] ^ (rq * 32)) + cb * 128) =
          (f32x4){acc2[cb][rb][4 * rq], acc2[cb][rb][4 * rq + 1], acc2[cb][rb][4 * rq + 2], acc2[cb][rb][4 * rq + 3]};
    wait_lgkm0(); bar();
    STAMP(11)
  }
  STAMP(12)
  STAMP_OUT
  asm volatile(".Lpf_end:");
}

extern "C" void kernel_launch(void* const* d_in, const int* in_sizes, int n_in, void* d_out, int out_size, void* d_ws, size_t ws_size,
                              hipStream_t stream) {
  (void)hipFuncSetAttribute((const void*)fused, hipFuncAttributeMaxDynamicSharedMemorySize, LDS_BYTES);
  fused<<<256, 512, LDS_BYTES, stream>>>((const u16*)d_in[0], (const u16*)d_in[1], (const u16*)d_in[2], (const u16*)d_in[3], (const u16*)d_in[4],
                                         (const u16*)d_in[5], (const u16*)d_in[6], (const u16*)d_in[7], (const u16*)d_in[8], (const u16*)d_in[10],
                                         (const u16*)d_in[11], (const u16*)d_in[12], (char*)d_ws, (float*)d_out);
}
```

```cpp
#include <hip/hip_runtime.h>
#include <hip/hip_bf16.h>
#include <type_traits>

typedef unsigned short u16;
typedef unsigned long long u64;
typedef short s16x4 __attribute__((ext_vector_type(4)));
typedef __bf16 bf16x2 __attribute__((ext_vector_type(2)));
typedef __bf16 bf16x8 __attribute__((ext_vector_type(8)));
typedef float f32x2 __attribute__((ext_vector_type(2)));
typedef float f32x4 __attribute__((ext_vector_type(4)));
typedef float f32x16 __attribute__((ext_vector_type(16)));
typedef unsigned u32x2 __attribute__((ext_vector_type(2)));
typedef unsigned u32x4 __attribute__((ext_vector_type(4)));
typedef int i32x4 __attribute__((ext_vector_type(4)));
#define LAS __attribute__((address_space(3)))
#define DEVI __device__ __forceinline__

constexpr int SL = 2048, SD = 512, SP = 512, SF = 1376;
constexpr int NKS = 150;
constexpr unsigned WS_E1 = 0;
constexpr unsigned WS_E2 = 2048;
constexpr unsigned WS_GRAN = 262144;
constexpr unsigned WS_FLAG = 524288;
constexpr unsigned WS_A2 = 786432;
constexpr unsigned A2_BYTES = 64u * NKS * 1024u;
constexpr unsigned WS_BYTES = WS_A2 + A2_BYTES;
constexpr unsigned OOB = 0x40000000u;
constexpr int SLT = 24832;
constexpr int OFF_B = 16384, OFF_WU = 12288 + 32;
constexpr int MISC = 6 * SLT;
constexpr int M_ST = MISC, M_SD = MISC + 1024, M_CNT = MISC + 2048, M_CS = MISC + 2304, M_T = MISC + 3840;
constexpr int LDS_BYTES = 163840;
constexpr unsigned SPIN_MAX = 1u << 16;
constexpr int cell_k(int e) { return e >= 15 ? 2 : (e % 5) < 3 ? 0 : 1; }
constexpr int cell_c(int e) { return e >= 15 ? ((e - 13) % 3) * SLT + OFF_B : (e % 5) < 3 ? (3 + (e / 5 + 2) % 3) * SLT + (e % 5) * 2048 : ((e / 5 + 2) % 3) * SLT + (e % 5 - 3) * 2048; }

#define STAMP_DECL
#define STAMP(i) {}
#define STAMP_OUT
#define STB(i) {}
template <int B, int E, class Fn> DEVI void sfor(Fn&& f) {
  if constexpr (B < E) { f(std::integral_constant<int, B>{}); sfor<B + 1, E>(f); }
}
DEVI i32x4 mk_srd(const void* p, unsigned bytes) {
  u64 a = (u64)p; return (i32x4){(int)(unsigned)a, (int)((unsigned)(a >> 32) & 0xffffu), (int)bytes, 0x00020000};
}
DEVI void dma(i32x4 srd, unsigned voff, unsigned soff, unsigned lds) {
  asm volatile("s_mov_b32 m0, %0\n\ts_nop 0\n\tbuffer_load_dwordx4 %1, %2, %3 offen lds" ::"s"(lds), "v"(voff), "s"(srd), "s"(soff) : "memory");
}
template <int OFF> DEVI void ld128_sc1(u32x4& d, i32x4 srd, unsigned voff, unsigned soff) {
  asm volatile("buffer_load_dwordx4 %0, %1, %2, %3 offen offset:%4 sc1" : "=v"(d) : "v"(voff), "s"(srd), "s"(soff), "n"(OFF) : "memory");
}
template <int OFF> DEVI void ld128_sc1_rw(u32x4& d, i32x4 srd, unsigned voff, unsigned soff) {
  asm volatile("buffer_load_dwordx4 %0, %1, %2, %3 offen offset:%4 sc1" : "+v"(d) : "v"(voff), "s"(srd), "s"(soff), "n"(OFF) : "memory");
}
DEVI void ld64_sc1(u32x2& d, i32x4 srd, unsigned voff, unsigned soff) {
  asm volatile("buffer_load_dwordx2 %0, %1, %2, %3 offen sc1" : "=v"(d) : "v"(voff), "s"(srd), "s"(soff) : "memory");
}
DEVI void ld64(u32x2& d, i32x4 srd, unsigned voff, unsigned soff) {
  asm volatile("buffer_load_dwordx2 %0, %1, %2, %3 offen" : "=v"(d) : "v"(voff), "s"(srd), "s"(soff) : "memory");
}
DEVI void ld16(unsigned& d, i32x4 srd, unsigned voff) {
  asm volatile("buffer_load_ushort %0, %1, %2, 0 offen" : "=v"(d) : "v"(voff), "s"(srd) : "memory");
}
template <int N> DEVI void wait_vm() { asm volatile("s_waitcnt vmcnt(%0)" ::"n"(N) : "memory"); }
template <class T> DEVI void pin(T& a) { asm volatile("" : "+v"(a)::"memory"); }
template <class T> DEVI void use(const T& a) { asm volatile("" ::"v"(a) : "memory"); }
DEVI int lnd(int a) { asm volatile("" : "+s"(a)); return a; }
DEVI int ftid() { int t = threadIdx.x; asm volatile("" : "+v"(t)); return t; }
DEVI void wait_lgkm0() { asm volatile("s_waitcnt lgkmcnt(0)" ::: "memory"); }
DEVI void bar() { asm volatile("s_barrier" ::: "memory"); }
DEVI void sb() { __builtin_amdgcn_sched_barrier(0); }
DEVI float lo2f(unsigned u) { return __uint_as_float(u << 16); }
DEVI float hi2f(unsigned u) { return __uint_as_float(u & 0xffff0000u); }
DEVI unsigned pk2(float a, float b) { f32x2 v = {a, b}; return __builtin_bit_cast(unsigned, __builtin_convertvector(v, bf16x2)); }
DEVI float rbf(float a) { return lo2f(pk2(a, a)); }
DEVI u32x4 lds_r128(unsigned a) { return *(LAS u32x4*)(size_t)a; }
DEVI u32x2 lds_tr(unsigned a) { return __builtin_bit_cast(u32x2, __builtin_amdgcn_ds_read_tr16_b64_v4i16((LAS s16x4*)(size_t)a)); }
DEVI u32x4 lds_tr2(unsigned a, unsigned d) { u32x2 x = lds_tr(a), y = lds_tr(a + d); return (u32x4){x[0], x[1], y[0], y[1]}; }
DEVI f32x16 mfma(u32x4 a, u32x4 b, f32x16 c) {
  return __builtin_amdgcn_mfma_f32_32x32x16_bf16(__builtin_bit_cast(bf16x8, a), __builtin_bit_cast(bf16x8, b), c, 0, 0, 0);
}
DEVI float dot2(unsigned a, unsigned b, float c) {
  return __builtin_amdgcn_fdot2_f32_bf16(__builtin_bit_cast(bf16x2, a), __builtin_bit_cast(bf16x2, b), c, false);
}
DEVI void swap16(float& a, float& b) { auto r = __builtin_amdgcn_permlane16_swap(__float_as_uint(a), __float_as_uint(b), false, false); a = __uint_as_float(r[0]); b = __uint_as_float(r[1]); }
DEVI void swap32(float& a, float& b) { auto r = __builtin_amdgcn_permlane32_swap(__float_as_uint(a), __float_as_uint(b), false, false); a = __uint_as_float(r[0]); b = __uint_as_float(r[1]); }
DEVI float sum32(float v) { float a = v, b = v; swap32(a, b); return a + b; }
DEVI float sum16(float v) { float a = v, b = v; swap16(a, b); return a + b; }
DEVI void cmul(float& dr, float& di, float ar, float ai, float br, float bi) { float t = ar * br - ai * bi; di = ar * bi + ai * br; dr = t; }
DEVI void cfma(float& hr, float& hi, float ar, float ai, float br, float bi) {
  float t = __builtin_fmaf(ar, hr, __builtin_fmaf(-ai, hi, br)); hi = __builtin_fmaf(ar, hi, __builtin_fmaf(ai, hr, bi)); hr = t;
}

template <int OFF> DEVI void dmao(i32x4 srd, unsigned voff, unsigned soff) {
  asm volatile("buffer_load_dwordx4 %0, %1, %2 offen offset:%3 lds" ::"v"(voff), "s"(srd), "s"(soff), "n"(OFF) : "memory");
}
DEVI void setm0(unsigned lds) { asm volatile("s_mov_b32 m0, %0\n\ts_nop 0" ::"s"(lds) : "memory"); }
DEVI void touch(i32x4 srd, unsigned voff, unsigned soff) {
  asm volatile("buffer_load_dword %0, %1, %2 offen lds" ::"v"(voff), "s"(srd), "s"(soff) : "memory");
}
DEVI f32x4 mfma16(u32x4 a, u32x4 b, f32x4 c) {
  return __builtin_amdgcn_mfma_f32_16x16x32_bf16(__builtin_bit_cast(bf16x8, a), __builtin_bit_cast(bf16x8, b), c, 0, 0, 0);
}
DEVI float vadd(float a, float b) { float r; asm("v_add_f32 %0, %1, %2" : "=v"(r) : "v"(a), "v"(b)); return r; }
DEVI float vfma(float a, float b, float c) { float r; asm("v_fma_f32 %0, %1, %2, %3" : "=v"(r) : "v"(a), "v"(b), "v"(c)); return r; }
DEVI f32x2 pfma(f32x2 a, f32x2 b, f32x2 c) { return __builtin_elementwise_fma(a, b, c); }
DEVI f32x2 sp(float a) { return (f32x2){a, a}; }
DEVI void cfma2(f32x2& hr, f32x2& hi, f32x2 a, f32x2 br, f32x2 bi) {
  f32x2 t1, t2;
  asm("v_pk_fma_f32 %2, %4, %1, %5 op_sel:[1,0,0] op_sel_hi:[1,1,1] neg_lo:[1,0,0] neg_hi:[1,0,0]\n\t"
      "v_pk_fma_f32 %3, %4, %0, %6 op_sel:[1,0,0] op_sel_hi:[1,1,1]\n\t"
      "v_pk_fma_f32 %0, %4, %0, %2 op_sel:[0,0,0] op_sel_hi:[0,1,1]\n\t"
      "v_pk_fma_f32 %1, %4, %1, %3 op_sel:[0,0,0] op_sel_hi:[0,1,1]"
      : "+v"(hr), "+v"(hi), "=&v"(t1), "=&v"(t2) : "v"(a), "v"(br), "v"(bi));
}
DEVI void cout2(unsigned& o0, unsigned& o1, f32x2 k, f32x2 gr, f32x2 gi) {
  f32x2 t1, t2;
  asm("v_pk_mul_f32 %0, %2, %4 op_sel:[1,0] op_sel_hi:[1,1]\n\t"
      "v_pk_mul_f32 %1, %2, %3 op_sel:[1,0] op_sel_hi:[1,1]\n\t"
      "v_pk_fma_f32 %0, %2, %3, %0 op_sel:[0,0,0] op_sel_hi:[0,1,1] neg_lo:[0,0,1] neg_hi:[0,0,1]\n\t"
      "v_pk_fma_f32 %1, %2, %4, %1 op_sel:[0,0,0] op_sel_hi:[0,1,1] neg_lo:[1,0,1] neg_hi:[1,0,1]"
      : "=&v"(t1), "=&v"(t2) : "v"(k), "v"(gr), "v"(gi));
  o0 = pk2(t1[0], t1[1]); o1 = pk2(t2[0], t2[1]);
}
#define SLP 4
#define WN 10
#define PGV 50
#define PCV 64
#define P2D 16
#define PTV 88
#define PSV 89
#define PFV 106
#define PPLV 120
#define PFW 1
#define PFW_S 2
#define PFW_O 0
#define PFX 0
#define ROTX (2 * (xcd >> 1) + 4 * (xcd & 1))
#define PLATE 0
#define AVAR 0
#define STA(i) {}
#define HPA 0
#define WEARLY 0
#define PFW0 0
#define HPB 0
#define HPC 0
#define PFG 0
#define PFG_O 0
#define PCK 11
#define PSM (PCK - 3)
#define WG 6
#define EPK 1
#define CPE 0
#define PFT 1
#define OAUX 16
#define LEADX (c + g)
#define LEAD 1
#define IC(x) std::integral_constant<int, (x)>{}
constexpr int p2_extra(int m) { return ((m == 0 || m == 1 || m == 5 || (m >= 7 && m <= 10)) ? 2 : 0) + (m == PSM ? 1 : 0); }
constexpr int p2_exsum(int a, int b) { int t = 0; for (int m = a; m <= b; ++m) t += p2_extra(m); return t; }
constexpr int p2_l0(int m) { return m <= PCK ? (m + WN / 2 < 16 ? m + WN / 2 : 16) : (m + WG < 38 ? m + WG : 38); }
constexpr int p2_after(int m) {
  int pos = 0, posL[40] = {};
  for (int k = 0; k < p2_l0(0); ++k) { pos += 2; posL[k] = pos; }
  pos += 10;
  for (int s = 0; s < m; ++s) { pos += p2_extra(s); for (int k = p2_l0(s); k < p2_l0(s + 1); ++k) { pos += 2; posL[k] = pos; } }
  return pos - posL[m];
}
constexpr int PPUB = 25, PG = PGV, PC = PCV, P2 = PC + P2D, PT = PTV, PS = PSV, PF = PFV, PPOLL = PPLV;
constexpr int hb_ops(int n) { return n < 6 ? 10 : 14; }
constexpr int hb_g0(int n) { return n < 6 ? 3 : 4; }
constexpr int hb_extra(int P) { return P == PPUB ? 1 : P == PG ? 8 : P == PS ? 4 : P == PF ? 1 : P == PPOLL ? 1 : 0; }
constexpr int hb_cnt(int P, bool with_extra) {
  int t = 0;
  for (int n = 0; n < 8; ++n) for (int i = 0; i < hb_ops(n); ++i) { const int p = 16 * n + i; if (p > P) return t; t += (n >= 6 || (i >= hb_g0(n) && i < hb_g0(n) + 4)); if (p < P || with_extra) t += hb_extra(p); }
  return t;
}
constexpr int nxt(int P, int d) { for (; d > 0; --d) { ++P; if (P % 16 >= hb_ops(P / 16)) P = (P / 16 + 1) * 16; } return P; }
DEVI u64 utag(u32x2 e) { const u64 t = (((u64)(unsigned)__builtin_amdgcn_readfirstlane(e[1]) << 32) | (unsigned)__builtin_amdgcn_readfirstlane(e[0])) + 1; return t ? t : 1; }
template <int CTRL> DEVI float dppf(float v) { return __int_as_float(__builtin_amdgcn_update_dpp(0, __float_as_int(v), CTRL, 0xf, 0xf, true)); }
DEVI void sline(u64 base, unsigned off) {
  asm volatile("s_load_dwordx2 s[98:99], %0, %1" ::"s"(base), "s"(off + 60) : "memory", "s98", "s99");
}
template <int IMM> DEVI void setm0i(unsigned base) { asm volatile("s_add_i32 m0, %0, %1\n\ts_nop 0" ::"s"(base), "n"(IMM) : "memory", "scc"); }
DEVI void miss2(unsigned& n, u32x4 a, u32x4 b, unsigned tl, unsigned th) {
  unsigned t, u;
  asm("v_xor_b32 %1, %7, %3\n\tv_xor_b32 %2, %7, %5\n\tv_bitop3_b32 %1, %4, %8, %1 bitop3:0xbe\n\tv_bitop3_b32 %2, %6, %8, %2 bitop3:0xbe\n\t"
      "v_min_u32 %1, 1, %1\n\tv_min_u32 %2, 1, %2\n\tv_add3_u32 %0, %0, %1, %2"
      : "+v"(n), "=&v"(t), "=&v"(u) : "v"(a[1]), "v"(a[3]), "v"(b[1]), "v"(b[3]), "s"(tl), "s"(th));
}
DEVI unsigned geglu2(float g0, float g1, float u0, float u1, float k1, float k3, float s2) {
  float a, b;
  asm("v_mul_f32 %0, %4, %4\n\tv_mul_f32 %1, %5, %5\n\tv_fma_f32 %0, %0, %7, %6\n\tv_fma_f32 %1, %1, %7, %6\n\t"
      "v_mul_f32 %0, %0, %4\n\tv_mul_f32 %1, %1, %5\n\tv_exp_f32 %0, %0\n\tv_exp_f32 %1, %1\n\tv_mul_f32 %2, %2, %4\n\t"
      "v_fma_f32 %0, %0, %8, %8\n\tv_fma_f32 %1, %1, %8, %8\n\tv_rcp_f32 %0, %0\n\tv_rcp_f32 %1, %1\n\tv_mul_f32 %3, %3, %5\n\t"
      "v_mul_f32 %0, %0, %2\n\tv_mul_f32 %1, %1, %3\n\tv_cvt_pk_bf16_f32 %0, %0, %1"
      : "=&v"(a), "=&v"(b), "+v"(u0), "+v"(u1) : "v"(g0), "v"(g1), "v"(k1), "v"(k3), "v"(s2));
  return __float_as_uint(a);
}
DEVI void cf4(float& hr, float& hi, float ar, float ai, float br, float bi) {
  float t, u;
  asm("v_fma_f32 %0, -%5, %3, %6\n\tv_fma_f32 %1, %5, %2, %7\n\tv_fmac_f32 %0, %4, %2\n\tv_fmac_f32 %1, %4, %3"
      : "=&v"(t), "=&v"(u) : "v"(hr), "v"(hi), "v"(ar), "v"(ai), "v"(br), "v"(bi));
  hr = t; hi = u;
}
DEVI void csq(float& dr, float& di, float r, float i) {
  float t, u;
  asm("v_mul_f32 %0, %2, %2\n\tv_mul_f32 %1, %2, %3\n\tv_fma_f32 %0, -%3, %3, %0\n\tv_add_f32 %1, %1, %1" : "=&v"(t), "=&v"(u) : "v"(r), "v"(i));
  dr = t; di = u;
}
DEVI void cf4c(float hr, float hi, float ar, float ai, float nai, float& br, float& bi) {
  asm("v_fmac_f32 %0, %5, %3\n\tv_fmac_f32 %1, %6, %2\n\tv_fmac_f32 %0, %4, %2\n\tv_fmac_f32 %1, %4, %3"
      : "+v"(br), "+v"(bi) : "v"(hr), "v"(hi), "v"(ar), "v"(ai), "v"(nai));
}
DEVI void sc4(float& r, float& i, float rs, float rc, float csr, float csi) {
  asm("v_mul_f32 %0, %2, %0\n\tv_mul_f32 %1, %2, %1\n\tv_fmac_f32 %0, %3, %4\n\tv_fmac_f32 %1, %3, %5"
      : "+v"(r), "+v"(i) : "v"(rs), "v"(rc), "v"(csr), "v"(csi));
}
DEVI void kc4(float& r, float& i, float kr, float nkr, float nki) {
  float t, u;
  asm("v_mul_f32 %0, %4, %2\n\tv_mul_f32 %1, %5, %3\n\tv_fmac_f32 %0, %6, %3\n\tv_fmac_f32 %1, %6, %2"
      : "=&v"(t), "=&v"(u) : "v"(r), "v"(i), "v"(kr), "v"(nkr), "v"(nki));
  r = t; i = u;
}

__global__ __launch_bounds__(512, 2) void fused(const u16* __restrict__ x, const u16* __restrict__ lam_re, const u16* __restrict__ lam_im,
                                                const u16* __restrict__ Bre, const u16* __restrict__ Bim, const u16* __restrict__ Cre,
                                                const u16* __restrict__ Cim, const u16* __restrict__ logdt, const u16* __restrict__ dskip,
                                                const u16* __restrict__ wg, const u16* __restrict__ wu, const u16* __restrict__ wd,
                                                char* __restrict__ ws, float* __restrict__ out) {
  extern __shared__ __attribute__((aligned(16))) char shm[];
  asm volatile(".Lpf_beg:");
  STAMP_DECL STAMP(0)
  unsigned perm;
  const i32x4 sx = mk_srd(x, SL * SD * 2);
  {
    const int b0 = blockIdx.x, t = threadIdx.x, lead = ((b0 >> 6) + (b0 >> 3)) & 3, rot = (b0 * 5) & 6;
    perm = (0x76543210u - (15u << (4 * lead)) * (1u + ((((lead + 1) >> 2) ^ 1) << 16)) + rot * 0x11111111u) & 0x77777777u;
    const unsigned v0 = (((t >> 2) & 64) + ((t >> 3) & 16) + ((t >> 4) & 5) + (t & 40)) * 1024 + (((t & 7) ^ ((t >> 4) & 3)) * 16);
    const unsigned so = (((b0 & 6) * 2 + (b0 >> 6)) << 17) + (perm & 7) * 128;
    asm volatile("" ::"s"(lam_re), "s"(lam_im), "s"(Bre), "s"(Bim), "s"(Cre), "s"(Cim), "s"(logdt), "s"(dskip), "s"(wg), "s"(wu), "s"(wd), "s"(ws), "s"(out));
    const int j0 = __builtin_amdgcn_readfirstlane(t >> 6);
    const unsigned v1 = (v0 ^ 64) + 1024;
    setm0i<SLT>(j0 * 2048); dmao<0>(sx, v0, so); dmao<1024>(sx, v1, so);
    const i32x4 sb0 = mk_srd((j0 >> 2) ? Bim : Bre, SP * SD * 2);
    const unsigned vb0 = ((t >> 3) & 31) * 1024 + (((t & 7) ^ ((t >> 4) & 7)) * 16), sob = (((b0 & 1) * 8 + ((b0 >> 3) & 7)) << 15);
    setm0i<SLT + OFF_B>(j0 * 1024); dmao<0>(sb0, vb0, sob + (perm & 7) * 128);
    const unsigned k1 = ((perm >> 4) & 7) * 128;
    setm0i<2 * SLT>(j0 * 2048); dmao<0>(sx, v0, (so & ~1023u) + k1); dmao<1024>(sx, v1, (so & ~1023u) + k1);
    setm0i<2 * SLT + OFF_B>(j0 * 1024); dmao<0>(sb0, vb0, sob + k1);
    { const unsigned ix = b0 >> 3, q4 = (b0 >> 1) & 3;
      const char* p0 = (j0 & 1) ? (const char*)wu : (const char*)wg;
      const char* p1 = (j0 & 1) ? ((ix & 16) ? (const char*)Cim : (const char*)Cre) : (const char*)wd;
      const char* p2 = (j0 & 1) ? (const char*)ws : (const char*)out + q4 * 1048576;
      const char* p3 = (const char*)ws + WS_A2 + (q4 * 2 + (j0 & 1)) * (8 * NKS * 1024);
      const char* pb = (j0 & 4) ? ((j0 & 2) ? p3 : p2) : ((j0 & 2) ? p1 : p0);
      const unsigned off = j0 == 3 ? (ix & 15) * 32768 : ix * (j0 < 3 ? 44032 : j0 == 4 ? 32768 : j0 == 5 ? 24576 : 38400);
      sline((u64)pb, off); }
  }
  auto chunk = [&](int v) { return (perm >> (4 * v)) & 7; };
  const int j = __builtin_amdgcn_readfirstlane(ftid() >> 6), w = j & 3, sb_ = w & 1, pi = w >> 1;
  const int b = lnd(blockIdx.x), xcd = b & 7, idx = b >> 3;
  const int c = (xcd >> 1) * 4 + (idx >> 3), rsel = xcd & 1, gi = idx & 7, g = rsel * 8 + gi;
  auto get_fb = [&]() { const int l = (lnd(gi) + 1) >> 3; return (6 * (g - rsel)) * (1 - l) + (84 + rsel) * l; };
  auto get_ct = [&]() { const int slow = (3 - lnd((int)((u64)wd >> 7))) & 7; return lnd(gi == 7 ? slow : gi == slow ? 7 : gi); };
  const i32x4 sws = mk_srd(ws, WS_BYTES);
  const __amdgpu_buffer_rsrc_t rws = __builtin_amdgcn_make_buffer_rsrc(ws, 0, WS_BYTES, 0x00020000);
  const unsigned flagb = WS_FLAG + c * 16384;
  const unsigned a2pb = WS_A2 + (unsigned)(c * 4 + 2 * pi) * (NKS * 1024);
  const unsigned w4 = w * 4096, w2 = w * 2048, w6 = w * 6144;
  const unsigned so_x = (c * 128 + 64 * (w >> 1) + 16 * (w & 1)) * 1024;
  u64 tag2 = 0;
  auto set_flag = [&](int fi) {
    __builtin_amdgcn_raw_buffer_store_b64((u32x2){(unsigned)tag2, (unsigned)(tag2 >> 32)}, rws, (ftid() & 63) == 0 ? flagb + (g * 8 + fi) * 128 : OOB, 0, 16);
  };
  auto wait_cnt = [&]() {
#pragma unroll 1
    for (unsigned spins = 0; spins < SPIN_MAX; ++spins)
      if (__builtin_amdgcn_readfirstlane(__hip_atomic_load((LAS unsigned*)(size_t)M_CNT, __ATOMIC_RELAXED, __HIP_MEMORY_SCOPE_WORKGROUP)) == 4u) break;
    asm volatile("" ::: "memory");
  };

  if (j < 4) {
    const int lane = ftid() & 63, l5 = lane & 31, h = lane >> 5, blk = (lane >> 4) & 1, q = (lane & 15) >> 2, pp = lane & 3;
    unsigned vx[4], vw[3], ra[4], wt[2];
    const i32x4 sw = mk_srd((w >> 1) ? wu : wg, SD * SF * 2);
    const unsigned ld_w = ((w >> 1) ? OFF_WU : 0) + (w & 1) * 6144;
    unsigned so_w = 0;
    u32x4 sel;
    {
      const unsigned sw0 = ((lane & 7) ^ (lane >> 4)) * 16;
      for (int r = 0; r < 4; ++r) vx[r] = (32 * (lane >> 5) + 8 * ((lane >> 3) & 1) + ((lane >> 4) & 1) + r) * 1024 + (sw0 ^ ((r & 1) * 64));
      auto ldX = [&](auto S) { constexpr int s = S;
        const unsigned so = so_x + chunk(s) * 128;
        setm0i<((s + 1) % 6) * SLT>(w4);
        dmao<0>(sx, vx[0], so); dmao<1024>(sx, vx[1], so); dmao<2048>(sx, vx[2], so); dmao<3072>(sx, vx[3], so);
      };
      u32x2 ep2;
      STAMP(1)
      if (w == 0 && lane == 0) *(LAS unsigned*)(size_t)M_CNT = 0;
      const unsigned xa = w * 4096 + lane * 16;
      unsigned ti;
      auto setup = [&]() {
        for (int s = 0; s < 4; ++s) { ra[s] = pi * 8192 + l5 * 128 + (((2 * (s ^ pi) + h) ^ ((l5 >> 1) & 7)) * 16); pin(ra[s]); }
        for (int p = 0; p < 2; ++p) { wt[p] = 3 * SLT + (blk ? OFF_WU : 0) + (8 * h + q) * 192 + (((pp & 1) << 1) | (pp >> 1)) * 8 + sb_ * 96 + (p ? -pi * 3072 : pi * 3072); pin(wt[p]); }
        const unsigned selv = (((lane & 15) == 0 && blk == 0) || ((lane & 15) == 4 && blk == 1)) ? 0x3f803f80u : 0u; sel = (u32x4){selv, selv, selv, selv}; pin(sel);
        ti = (lane & 4) ? M_CNT + 64 - M_ST : (64 * (w >> 1) + 32 * (lane >> 5) + 16 * (w & 1) + 4 * (lane & 3) + ((lane >> 3) & 3)) * 8;
        pin(ti);
      };
      auto setup2 = [&]() { const int q12 = (lane * 43) >> 9, r12 = lane - 12 * q12;
        for (int i = 0; i < 3; ++i) { const int r = r12 + 4 * i, cy = r >= 12, col = r - 12 * cy;
          vw[i] = (gi == 7 && col >= 2) ? OOB : (5 * i + q12 + cy) * (SF * 2) + col * 16 - i * 1024; pin(vw[i]); }
        so_w = (w & 1) * 32 * (SF * 2) + get_fb() * 32; };
      float s1[4] = {}, s2[4] = {};
      auto stats = [&](auto S, auto&& mid) { constexpr int s = S;
        u32x4 xr[4];
        for (int k = 0; k < 4; ++k) xr[k] = lds_r128(((s + 1) % 6) * SLT + k * 1024 + xa);
        sb(); mid(); sb();
        for (int e = 0; e < 4; ++e) for (int i = 0; i < 4; ++i) { const unsigned u = xr[i][e]; s1[i] = dot2(u, 0x3f803f80u, s1[i]); s2[i] = dot2(u, u, s2[i]); }
        for (int i = 0; i < 4; ++i) { pin(s1[i]); pin(s2[i]); }
        sb(); };
      wait_vm<3>(); wait_lgkm0(); STA(46) bar(); STA(47)
      stats(IC(0), [&] { ldX(IC(2)); ld64_sc1(ep2, sws, 0, WS_E2 + c * 128); });
      wait_vm<5>(); STA(48) bar(); STA(49)
      stats(IC(1), [&] { ldX(IC(3)); setup(); });
      sfor<2, 8>([&](auto S) { constexpr int s = S;
        wait_vm<(s < 7 ? 4 : 0)>();
        stats(S, [&] { if constexpr (s < 6) ldX(IC(s + 2)); if constexpr (s == 6) setup2(); });
        if constexpr (s < 7) { STA(46 + 2 * s) bar(); STA(47 + 2 * s) }
      });
      pin(ep2); tag2 = utag(ep2);
      float t1[2], t2[2];
      for (int k = 0; k < 2; ++k) { const bool o = lane & 1;
        t1[k] = (o ? s1[2 * k + 1] : s1[2 * k]) + dppf<0xB1>(o ? s1[2 * k] : s1[2 * k + 1]); t2[k] = (o ? s2[2 * k + 1] : s2[2 * k]) + dppf<0xB1>(o ? s2[2 * k] : s2[2 * k + 1]); }
      const bool o = lane & 2;
      float sm = (o ? t1[1] : t1[0]) + dppf<0x4E>(o ? t1[0] : t1[1]), sq = (o ? t2[1] : t2[0]) + dppf<0x4E>(o ? t2[0] : t2[1]);
      sm += dppf<0x104>(sm); sq += dppf<0x104>(sq);
      const float mean = sm * (1.f / SD);
      const float var = fmaxf(sq * (1.f / SD) - mean * mean, 0.f);
      const float sd = rbf(__builtin_amdgcn_sqrtf(rbf(rbf(var) + 1e-5f)));
      const float r_ = __builtin_amdgcn_rcpf(sd); const unsigned nm = pk2(-mean, -mean);
      *(LAS f32x2*)(size_t)(M_ST + ti) = (f32x2){r_, lo2f(nm) * r_}; *(LAS f32x2*)(size_t)(M_SD + ti) = (f32x2){sd, __uint_as_float(nm)};
    }
    f32x16 acc[3][2];
    f32x4 csg[3] = {};
    u32x4 af[2][2]; u32x2 wf[2][3][2];
    auto rd1 = [&](auto N, auto S, auto I) { constexpr int n = N, s = S, i = I;
      const unsigned a_ = ra[s] + (n % 3) * SLT, t_ = wt[s & 1] + (n % 3) * SLT + s * 3072;
      if constexpr (i == 0) { wf[s & 1][0][0] = lds_tr(t_); wf[s & 1][0][1] = lds_tr(t_ + 768); }
      if constexpr (i == 1) { af[s & 1][0] = lds_r128(a_); af[s & 1][1] = lds_r128(a_ + 4096); }
      if constexpr (i == 2) { wf[s & 1][1][0] = lds_tr(t_ + 32); wf[s & 1][1][1] = lds_tr(t_ + 32 + 768); }
      if constexpr (i == 3) { wf[s & 1][2][0] = lds_tr(t_ + 64); wf[s & 1][2][1] = lds_tr(t_ + 64 + 768); }
    };
    auto W = [&](int p, int k) { return (u32x4){wf[p][k][0][0], wf[p][k][0][1], wf[p][k][1][0], wf[p][k][1][1]}; };
    f32x2 st[2], sd[2]; float ek[2][3], csp[3]; u32x4 cf[3]; unsigned vs0 = 0;
    const unsigned csa_ = h ? M_T + 8192 + lane * 4 : M_CS + w * 384 + lane * 4;
    STAMP(22)
    wait_vm<0>(); wait_lgkm0(); bar();
    STAMP(2)
    sfor<0, 4>([&](auto I) { rd1(IC(0), IC(0), I); });
    sfor<0, 32>([&](auto K) { constexpr int n = K / 4, s = K % 4, nn = (K + 1) / 4, sn = (K + 1) % 4;
      sfor<0, 6>([&](auto I) { constexpr int i = I;
        acc[i >> 1][i & 1] = mfma(W(s & 1, i >> 1), af[s & 1][i & 1], K == 0 ? (f32x16){} : acc[i >> 1][i & 1]);
        sb();
        if constexpr (s == 3 && n < 7) { if constexpr (i == 0) { if constexpr (n >= 1) wait_vm<(n < 6 ? 10 : 0)>(); wait_lgkm0(); STB(32 + 2 * n) bar(); STB(33 + 2 * n) } if constexpr (i >= 1 && i < 5) rd1(IC(nn), IC(sn), IC(i - 1)); }
        else if constexpr (K < 31 && i < 4) rd1(IC(nn), IC(sn), I);
        if constexpr (i >= 4 && K < 23) {
          constexpr int xn = (K + 1) / 4 + 2, kk = (K + 1) % 4, o0 = i == 5 ? (kk == 0 ? 0 : 3 * kk) : (kk == 0 ? 0 : 3 * kk - 1), o1 = i == 5 ? (kk == 3 ? 10 : 3 * kk + 2) : (kk == 0 ? 0 : 3 * kk);
          sfor<o0, o1>([&](auto O) { constexpr int o = O;
            if constexpr (o < 4) { if constexpr (xn > 2) { if constexpr (o == 0) setm0i<(xn % 3) * SLT>(w4); dmao<o * 1024>(sx, vx[o], so_x + chunk((xn + 5) & 7) * 128); } }
            else { constexpr int u = o - 4; if constexpr (u % 3 == 0) setm0i<(3 + xn % 3) * SLT + u * 1024>(ld_w);
              dmao<(u % 3) * 1024>(sw, vw[u % 3], so_w + chunk((xn + 5) & 7) * (64 * SF * 2) + (u / 3) * 16 * (SF * 2)); } }); }
        if constexpr (K == 24 && i == 4) { const unsigned ia = (64 * pi + 32 * ((lane >> 2) & 1) + 4 * ((lane >> 3) & 3) + (lane & 3)) * 8;
          for (int rb = 0; rb < 2; ++rb) { st[rb] = *(LAS f32x2*)(size_t)(M_ST + ia + rb * 128); sd[rb] = *(LAS f32x2*)(size_t)(M_SD + ia + rb * 128); } }
        if constexpr (K == 25 && i >= 4) { constexpr int rb = i - 4; const float rs = st[rb][0];
          ek[rb][0] = -2.302208198f * rs; ek[rb][1] = -0.1029432397f * rs * rs * rs; ek[rb][2] = sd[rb][0] * sd[rb][0]; pin(ek[rb][0]); pin(ek[rb][1]); pin(ek[rb][2]); }
        if constexpr (K == 28 && i == 4) { for (int k = 0; k < 3; ++k) csp[k] = *(LAS float*)(size_t)(M_CS + ((w ^ 2) * 3 + k) * 128 + l5 * 4); }
        if constexpr (K == 29 && i == 4) { vs0 = a2pb + lane * 16 + (64 + get_fb() + 3 * sb_) * 1024; pin(vs0); }
        if constexpr (K == 31 && i == 0) { wait_lgkm0();
          if (lane == 0) __hip_atomic_fetch_add((LAS unsigned*)(size_t)M_CNT, 1u, __ATOMIC_RELAXED, __HIP_MEMORY_SCOPE_WORKGROUP); }
        if constexpr (K == 31 && i >= 2 && i < 5) { constexpr int k = i - 2;
          const float cs = h ? 0.f : csg[k][0] + csp[k];
          const float c0 = rbf(cs), r1 = cs - c0, c1 = rbf(r1), c2 = r1 - c1;
          cf[k] = (u32x4){pk2(c0, c1), pk2(c2, 0.f), 0u, 0u}; pin(cf[k]); }
        sb();
      });
      if constexpr ((!(s & 1) || K == 29) && K < 31) { for (int k = 0; k < 3; ++k) csg[k] = mfma16(sel, W(s & 1, k), csg[k]); sb(); }
      if constexpr (K == 30) { for (int k = 0; k < 3; ++k) csg[k] = mfma16(sel, W(1, k), csg[k]); sb(); }
      if constexpr (K == 26) { *(LAS float*)(size_t)(csa_) = csg[0][0]; *(LAS float*)(size_t)(csa_ + 128) = csg[1][0]; *(LAS float*)(size_t)(csa_ + 256) = csg[2][0]; sb(); }
    });
    STAMP(8)
    for (int k = 0; k < 3; ++k) for (int rb = 0; rb < 2; ++rb) { const unsigned nm = __float_as_uint(sd[rb][1]); acc[k][rb] = mfma(cf[k], (u32x4){nm, nm, 0u, 0u}, acc[k][rb]); }
    STAMP(7)
    sfor<0, 3>([&](auto K) { constexpr int k = K;
      const unsigned vst = (gi < 7 || 3 * sb_ + k == 0) ? vs0 + k * 1024 : OOB;
      sfor<0, 2>([&](auto R_) { constexpr int rb = R_;
        const float k1 = ek[rb][0], k3 = ek[rb][1], s2 = ek[rb][2];
        unsigned o[4];
        for (int i = 0; i < 4; ++i) {
          const f32x2 gv = {acc[k][rb][2 * i], acc[k][rb][2 * i + 1]}, uv = {acc[k][rb][8 + 2 * i], acc[k][rb][9 + 2 * i]};
          const f32x2 z = gv * pfma(gv * gv, sp(k3), sp(k1));
          const f32x2 d = pfma((f32x2){__builtin_amdgcn_exp2f(z[0]), __builtin_amdgcn_exp2f(z[1])}, sp(s2), sp(s2));
          const f32x2 r = (gv * uv) * (f32x2){__builtin_amdgcn_rcpf(d[0]), __builtin_amdgcn_rcpf(d[1])};
          o[i] = pk2(r[0], r[1]); }
        __builtin_amdgcn_raw_buffer_store_b128((u32x4){o[0], o[1], o[2], o[3]}, rws, vst + rb * NKS * 1024, 0, 16);
      });
    });
    sb();
    STAMP(9)
    wait_vm<0>();
    set_flag(4 + w);
    STAMP(10)
    const int t = ftid(), ch = t & 15, r4 = t >> 4, ct = get_ct();
    const int trow = 64 * rsel + 32 * ((r4 >> 2) & 1) + 8 * (r4 & 1) + 2 * (r4 >> 3) + ((r4 >> 1) & 1);
    const int irow = 64 * rsel + 32 * ((r4 >> 2) & 1) + 4 * (r4 >> 3) + (r4 & 3);
    u32x2 xv[4], dk;
    for (int it = 0; it < 4; ++it) ld64(xv[it], sx, trow * 1024 + ch * 8, c * 131072 + ct * 128 + (4 * (it & 1) + 16 * (it >> 1)) * 1024);
    ld64(dk, mk_srd(dskip, SD * 2), ch * 8, ct * 128);
    wait_vm<0>(); for (int it = 0; it < 4; ++it) pin(xv[it]); pin(dk);
    f32x4 base[4]; unsigned ph[4];
    for (int it = 0; it < 4; ++it) { const f32x2 s2 = *(LAS f32x2*)(size_t)(M_ST + (irow + 8 * (it & 1) + 16 * (it >> 1)) * 8);
      for (int e = 0; e < 4; ++e) { const unsigned xu = xv[it][e >> 1], du = dk[e >> 1];
        const float xe = (e & 1) ? hi2f(xu) : lo2f(xu), de = (e & 1) ? hi2f(du) : lo2f(du);
        base[it][e] = xe + rbf(rbf(__builtin_fmaf(xe, s2[0], s2[1])) * de); }
      const int e = 2 * it + (r4 >> 3), d = (e * 11) >> 5;
      ph[it] = (3 + d) * SLT + (e - 3 * d) * 2048 + (r4 & 7) * 256 + (ch ^ (r4 & 7)) * 16;
      pin(base[it]); pin(ph[it]); }
    float* op = out + (c * 128 + trow) * SD + ct * 64 + ch * 4;
    const __amdgpu_buffer_rsrc_t rout = __builtin_amdgcn_make_buffer_rsrc(out, 0, SL * SD * 4, 0x00020000);
    const unsigned oo = ((c * 128 + trow) * SD + ct * 64 + ch * 4) * 4;
    bar();
    STAMP(11)
    for (int it = 0; it < 4; ++it) {
      f32x4 p[4];
      for (int ww = 0; ww < 4; ++ww) p[ww] = *(LAS f32x4*)(size_t)(ph[it] + ww * 6144);
      __builtin_amdgcn_raw_buffer_store_b128(__builtin_bit_cast(u32x4, base[it] + ((p[0] + p[1]) + (p[2] + p[3]))), rout, oo + (4 * (it & 1) + 16 * (it >> 1)) * SD * 4, 0, OAUX);
    }
    if (g == 0) __builtin_amdgcn_raw_buffer_store_b64((u32x2){(unsigned)tag2, (unsigned)(tag2 >> 32)}, rws, t == 0 ? WS_E2 + c * 128 : OOB, 0, 16);
  } else {
    const int lane = ftid() & 63, l5 = lane & 31, h = lane >> 5, blk = (lane >> 4) & 1, q = (lane & 15) >> 2, pp = lane & 3;
    const unsigned va = lane * 16;
    const unsigned sw0 = ((lane & 7) ^ (lane >> 4)) * 16;
    unsigned vb[2], vw[3], vd[2];
    for (int r = 0; r < 2; ++r) vb[r] = ((lane >> 3) + 7 * r) * 1024 + (sw0 ^ (r * 64));
    const i32x4 sbm = mk_srd((w >> 1) ? Bim : Bre, SP * SD * 2);
    const unsigned so_b = (g * 32 + (w & 1) * 16) * 1024;
    auto ldB = [&](auto S) { constexpr int s = S;
      const unsigned so = so_b + chunk(s) * 128;
      setm0i<((s + 1) % 6) * SLT + OFF_B>(w2); dmao<0>(sbm, vb[0], so); dmao<1024>(sbm, vb[1], so); };
    u32x2 ep2, ep1; unsigned plr, pli, pdt;
    ld64_sc1(ep2, sws, 0, WS_E2 + c * 128);
    ld64_sc1(ep1, sws, 0, WS_E1 + g * 128);
    const int ps = (lane & 3) | ((lane & 4) << 1) | ((lane & 8) >> 1);
    { const unsigned po = (g * 32 + sb_ * 16 + ps) * 2;
      ld16(plr, mk_srd(lam_re, SP * 2), po); ld16(pli, mk_srd(lam_im, SP * 2), po); ld16(pdt, mk_srd(logdt, SP * 2), po); }
    { u64 pc; unsigned len, back;
      asm volatile("s_getpc_b64 %0\n.Lpf_here:\n\ts_mov_b32 %1, .Lpf_end-.Lpf_beg\n\ts_mov_b32 %2, .Lpf_here-.Lpf_beg" : "=s"(pc), "=s"(len), "=s"(back));
      for (unsigned o = (idx * 4 + w) * 128; o + 128 < len; o += 16384) sline((pc - back) & ~127ull, o + 128); }
    const i32x4 sw = mk_srd((w >> 1) ? wu : wg, SD * SF * 2);
    const unsigned so_w = (w & 1) * 32 * (SF * 2) + get_fb() * 32;
    const unsigned ld_w = ((w >> 1) ? OFF_WU : 0) + (w & 1) * 6144;
    auto opW = [&](auto N, auto I) { constexpr int n = N, i = I;
      if constexpr (i % 3 == 0) setm0i<(3 + n % 3) * SLT + i * 1024>(ld_w);
      dmao<(i % 3) * 1024>(sw, vw[i % 3], so_w + chunk((n + 5) & 7) * (64 * SF * 2) + (i / 3) * 16 * (SF * 2)); };
    unsigned ra[4], wa[4];
    for (int s = 0; s < 4; ++s) { ra[s] = pi * 8192 + l5 * 128 + (((2 * s + h) ^ ((l5 >> 1) & 7)) * 16);
      const int row = sb_ * 16 + ps; wa[s] = OFF_B + blk * 4096 + row * 128 + (((2 * s + h) ^ ((row >> 1) & 7)) * 16); }
    const unsigned selv = (((lane & 15) == 0 && blk == 0) || ((lane & 15) == 4 && blk == 1)) ? 0x3f803f80u : 0u;
    const u32x4 sel = {selv, selv, selv, selv};
    for (int s = 0; s < 4; ++s) { pin(ra[s]); pin(wa[s]); }
    unsigned t1l = 0, t1h = 0;
    const int cc = 2 * c + pi;
    float ar, ai, nai, kr, nkr, nki, a8r, a8i, a16r, a16i, a32r, a32i, a64r, a64i, pwr = 1.f, pwi = 0.f, a256r, a256i, mr = 1.f, mi = 0.f;
    u32x4 oh[2];
    float lr, li;
    auto params = [&](auto V) { constexpr int v = V;
      if constexpr (v == 1) { const int q12 = (lane * 43) >> 9, r12 = lane - 12 * q12;
        for (int i = 0; i < 3; ++i) { const int r = r12 + 4 * i, cy = r >= 12, col = r - 12 * cy;
          vw[i] = (gi == 7 && col >= 2) ? OOB : (5 * i + q12 + cy) * (SF * 2) + col * 16 - i * 1024; pin(vw[i]); } }
      if constexpr (v == 2) {
        pin(ep2); pin(ep1); pin(plr); pin(pli); pin(pdt);
        tag2 = utag(ep2);
        const u64 tag1 = utag(ep1); t1l = (unsigned)tag1; t1h = (unsigned)(tag1 >> 32); }
      if constexpr (v == 3) {
        lr = lo2f(plr); li = lo2f(pli);
        const float dt = rbf(__expf(lo2f(pdt)));
        const float er = __expf(dt * lr);
        const float rev = dt * li * 0.15915494309189535f;
        const float fr = rev - __builtin_rintf(rev);
        ar = er * __builtin_amdgcn_cosf(fr); ai = er * __builtin_amdgcn_sinf(fr);
        pin(ar); pin(ai); }
      if constexpr (v == 4) { a8r = ar; a8i = ai;
        for (int i = 0; i < 3; ++i) csq(a8r, a8i, a8r, a8i);
        csq(a16r, a16i, a8r, a8i); pin(a16r); pin(a16i); }
      if constexpr (v == 6) {
        const float nr = ar - 1.f, ni = ai, den = 2.f * __builtin_amdgcn_rcpf(vfma(lr, lr, li * li));
        kr = vfma(nr, lr, ni * li) * den; nki = vfma(nr, li, -(ni * lr)) * den; nkr = -kr; nai = -ai;
        pin(nai); pin(kr); pin(nkr); pin(nki); }
      if constexpr (v == 10) { csq(a32r, a32i, a16r, a16i); csq(a64r, a64i, a32r, a32i); if (blk) { pwr = a16r; pwi = a16i; mr = a64r; mi = a64i; } }
      if constexpr (v == 11) { float tr = pwr, ti = pwi; cf4(tr, ti, a32r, a32i, 0.f, 0.f); if (h) { pwr = tr; pwi = ti; } csq(a32r, a32i, a64r, a64i); }
      if constexpr (v == 12) { float ur = mr, ui = mi; cf4(ur, ui, a32r, a32i, 0.f, 0.f); if (h) { mr = ur; mi = ui; } csq(a256r, a256i, a32r, a32i); }
      if constexpr (v == 13 || v == 14) { constexpr int u = v - 13;
        const unsigned val = (((lane >> 2) & 1) == h && blk == u) ? ((lane & 1) ? 0x3f800000u : 0x3f80u) : 0u; const int d0 = ((lane >> 1) & 1) | ((lane >> 2) & 2);
        for (int d = 0; d < 4; ++d) oh[u][d] = d0 == d ? val : 0u;
        pin(oh[u]); }
      sb();
    };
    f32x16 sa[2] = {};
    f32x4 csa = {};
    { u32x4 af[2][4][2], wf[2][4];
      auto rdA = [&](int p, int s, unsigned slot) { wf[p][s] = lds_r128(slot + wa[s]); af[p][s][0] = lds_r128(slot + ra[s]); af[p][s][1] = lds_r128(slot + ra[s] + 4096); };
      auto mmA = [&](int p, int s) { sa[0] = mfma(af[p][s][0], wf[p][s], sa[0]); sa[1] = mfma(af[p][s][1], wf[p][s], sa[1]); pin(sa[0]); pin(sa[1]); };
      auto csA = [&](int p) { for (int s = 0; s < 4; ++s) csa = mfma16(sel, wf[p][s], csa); pin(csa); sb(); };
      auto W3 = [&](auto N, auto H_) { sfor<0, 3>([&](auto I) { opW(N, IC(3 * H_ + I)); }); };
      unsigned rh[4], wh[4];
      for (int s = 0; s < 4; ++s) { rh[s] = ra[s] + 3 * SLT; wh[s] = wa[s] + 3 * SLT - 8192; pin(rh[s]); pin(wh[s]); wh[s] += 8192; wa[s] -= 8192; pin(wa[s]); wa[s] += 8192; }
      ldB(IC(2));
      __builtin_amdgcn_s_setprio(HPA);
      wait_vm<10>(); STA(46) bar(); STA(47) for (int s = 0; s < 4; ++s) rdA(0, s, SLT); sb(); ldB(IC(3));
      sfor<1, 8>([&](auto V) { constexpr int v = V, p = v & 1, sl = (v + 1) % 6;
        wait_vm<(v == 1 ? 9 : WEARLY ? (v < 4 ? 4 : v == 4 ? 7 : v == 5 ? 10 : v == 6 ? 11 : 6) : (v < 5 ? 4 : v == 5 ? 7 : v == 6 ? 11 : 6))>(); wait_lgkm0();
        if constexpr (v == 7) __builtin_amdgcn_s_setprio(3);
        STA(46 + 2 * v) bar(); STA(47 + 2 * v)
        sfor<0, 8>([&](auto I) { constexpr int i = I;
          sa[i & 1] = mfma(af[p ^ 1][i >> 1][i & 1], wf[p ^ 1][i >> 1], sa[i & 1]); pin(sa[i & 1]);
          sb();
          if constexpr (i < 6) sfor<2 * i, 2 * i + 2>([&](auto R) { constexpr int r = R, s = r / 3, k = r % 3, o = (sl % 3) * SLT;
            if constexpr (k == 0) wf[p][s] = lds_r128((sl < 3 ? wa[s] : wh[s]) + o); else af[p][s][k - 1] = lds_r128((sl < 3 ? ra[s] : rh[s]) + o + (k - 1) * 4096); });
          if constexpr (i == 6 && v < 5) ldB(IC(v + 3));
          if constexpr (i == 7 && v == 4) W3(IC(0), IC(0));
          if constexpr (i == 6 && v == 5) W3(IC(0), IC(1));
          if constexpr (i == 7 && v == 5) W3(IC(1), IC(0));
          if constexpr (i == 6 && v == 6) W3(IC(1), IC(1));
          sb(); });
        csA(p ^ 1);
        params(V);
      });
      STAMP(2)
      for (int s = 0; s < 4; ++s) mmA(1, s);
      csA(1);
      __builtin_amdgcn_s_setprio(HPB);
    }
    const int kb = 22 * w - (w == 3), cnt = w < 2 ? 22 : 21, ct = get_ct();
    const i32x4 swd = mk_srd(wd + kb * 8192, cnt * 16384);
    const unsigned so_c = ct * 65536 + w * 256;
    const unsigned r2 = (xcd * 11) >> 2;
    auto kofs = [&](int p, unsigned sc_) { const unsigned r = lnd(r2 * sc_), a = r + p * sc_, b2 = r + (p - 22) * sc_; return a < b2 ? a : b2; };
    for (int r = 0; r < 2; ++r) vd[r] = ((lane >> 3) + 7 * r) * 1024 + (((lane & 7) ^ (((lane >> 4) & 1) << 2)) * 16) + ct * 128;
    unsigned so_t = 0; i32x4 sc = swd;
    auto wd_op = [&](auto M, auto E, auto R) { constexpr int m = M, e = E, r = R;
      if constexpr (r == 0) { so_t = kofs(m < 18 ? (m + 14) % 18 : m, 16384); setm0i<cell_c(e)>(cell_k(e) == 0 ? w6 : cell_k(e) == 1 ? w4 : w2); }
      dmao<r * 1024>(swd, vd[r], so_t);
    };
    auto dma_wd = [&](auto M, auto E) { wd_op(M, E, IC(0)); wd_op(M, E, IC(1)); };
    auto c_op = [&](auto N, auto I) { constexpr int n = N, i = I;
      if constexpr (i == 0) { const int kg = ((n >> 1) + lnd(xcd)) & 3; so_t = so_c + (n & 1) * 32768 + (kg & 1) * 128; sc = mk_srd((kg >> 1) ? Cim : Cre, SD * SP * 2); }
      if constexpr (!(i & 1)) setm0i<(n & 1) ? ((i >> 1) ? M_T : cell_c(15)) : cell_c(16 + (i >> 1))>(w2);
      dmao<(i & 1) * 1024>(sc, vb[i & 1], so_t + (i >> 1) * 16384);
    };
    unsigned rcx[4];
    for (int s = 0; s < 4; ++s) rcx[s] = OFF_B + w * 2048 + blk * SLT + (lane & 15) * 128 + (((2 * s + h) ^ ((l5 >> 1) & 7)) * 16);
    const unsigned rcd = 2 * SLT + blk * (M_T - cell_c(15) - SLT);
    auto rd_c = [&](int n, u32x4* d) { for (int s = 0; s < 4; ++s) d[s] = lds_r128(rcx[s] + ((n & 1) ? rcd : 0u)); };
    const unsigned granb = WS_GRAN + (g * 2 + sb_) * 8192;
    const i32x4 sgr = mk_srd(ws + granb, 8192);
    const unsigned vg = (32 - cc) * 256 + va;
    float R[16], I_[16], csr, csi, er[2], ei[2], sr[4], si[4], prer, prei, cr, ci;
    f32x4 tb[8];
    u32x4 ge[8], fr4[4];
    f32x16 td[2];
    unsigned hp[2][8];
    unsigned nbad = 0, fo_g = 0, pa[2], tdl = 0;
    auto side = [&](auto P_) { constexpr int P = P_;
      if constexpr (P == 0) { for (int k = 0; k < 8; ++k) tb[k] = *(LAS f32x4*)(size_t)(M_ST + pi * 512 + (lane >> 4) * 128 + k * 16);
        csr = csa[0]; float t = csr; swap32(csr, t); csi = csr; swap16(csr, csi); }
      if constexpr (P >= 1 && P < 9) { constexpr int k = P - 1;
        for (int r = 0; r < 2; ++r) { R[2 * k + r] = sa[0][2 * k + r]; I_[2 * k + r] = sa[1][2 * k + r]; swap16(R[2 * k + r], I_[2 * k + r]); }
        for (int r = 0; r < 2; ++r) sc4(R[2 * k + r], I_[2 * k + r], tb[k][2 * r], tb[k][2 * r + 1], csr, csi); }
      if constexpr (P == 9) { for (int r = 0; r < 2; ++r) { er[r] = R[r]; ei[r] = I_[r]; cf4(er[r], ei[r], ar, ai, R[2 + r], I_[2 + r]); } }
      if constexpr (P >= 16 && P < 22) { constexpr int k = P - 14; for (int r = 0; r < 2; ++r) cf4(er[r], ei[r], ar, ai, R[2 * k + r], I_[2 * k + r]); }
      if constexpr (P == 22) { sr[0] = er[0]; si[0] = ei[0]; cf4(sr[0], si[0], a8r, a8i, er[1], ei[1]); sr[1] = sr[0]; swap16(sr[0], sr[1]); si[1] = si[0]; swap16(si[0], si[1]); }
      if constexpr (P == 23) { sr[2] = sr[0]; swap32(sr[0], sr[2]); si[2] = si[0]; swap32(si[0], si[2]); sr[3] = sr[1]; swap32(sr[1], sr[3]); si[3] = si[1]; swap32(si[1], si[3]); }
      if constexpr (P == 24) { cr = sr[0]; ci = si[0]; cf4(cr, ci, a16r, a16i, sr[1], si[1]); sr[1] = cr; si[1] = ci; cf4(cr, ci, a16r, a16i, sr[2], si[2]); sr[2] = cr; si[2] = ci; }
      if constexpr (P == PPUB) { cf4(cr, ci, a16r, a16i, sr[3], si[3]);
        __builtin_amdgcn_raw_buffer_store_b128((u32x4){__float_as_uint(cr), t1l, __float_as_uint(ci), t1h}, rws, lane < 16 ? granb + (31 - cc) * 256 + va : OOB, 0, 16); }
      if constexpr (P == 32) { prer = h ? (blk ? sr[2] : sr[1]) : (blk ? sr[0] : 0.f); prei = h ? (blk ? si[2] : si[1]) : (blk ? si[0] : 0.f); }
      sfor<0, 16>([&](auto K) { constexpr int k = K; if constexpr (P == nxt(33, k < 4 ? k / 2 : k - 2)) kc4(R[k], I_[k], kr, nkr, nki); });
      if constexpr (P >= 53 && P < 58) params(IC(P - 43));
      if constexpr (P == PG) sfor<0, 8>([&](auto K) { constexpr int k = K; ld128_sc1<(k & 3) * 1024>(ge[k], sgr, vg, (k >> 2) * 4096); });
      if constexpr (P == PC) { STB(23) wait_vm<hb_cnt(PC, false) - hb_cnt(PG, true)>(); for (int i = 0; i < 8; ++i) pin(ge[i]); STB(24) }
      if constexpr (P == PC + 3) STB(25) if constexpr (P == PC + 6) STB(26) if constexpr (P == PC + 9) STB(27)
      if constexpr (P == PC || P == PC + 1) { constexpr int i = 4 * (P - PC); miss2(nbad, ge[i], ge[i + 1], t1l, t1h); miss2(nbad, ge[i + 2], ge[i + 3], t1l, t1h); }
      if constexpr (P == PC + 2) { const unsigned nout = 8 - ((cc + 3 - (lane >> 4)) >> 2);
#pragma unroll 1
        for (unsigned spins = 0; spins < SPIN_MAX; ++spins) {
          if (__all(nbad == nout)) break;
          STAMP(28)
          __builtin_amdgcn_s_sleep(SLP);
          sfor<0, 8>([&](auto I) { constexpr int i = I; ld128_sc1_rw<(i & 3) * 1024>(ge[i], sgr, vg, (i >> 2) * 4096); });
          wait_vm<0>(); for (int i = 0; i < 8; ++i) pin(ge[i]);
          nbad = 0;
          for (int i = 0; i < 8; i += 2) miss2(nbad, ge[i], ge[i + 1], t1l, t1h);
        }
        cr = __uint_as_float(ge[7][0]); ci = __uint_as_float(ge[7][2]); cf4(cr, ci, a256r, a256i, __uint_as_float(ge[6][0]), __uint_as_float(ge[6][2])); }
      if constexpr (P >= PC + 3 && P < PC + 6) { constexpr int i = 5 - 2 * (P - PC - 3);
        cf4(cr, ci, a256r, a256i, __uint_as_float(ge[i][0]), __uint_as_float(ge[i][2])); cf4(cr, ci, a256r, a256i, __uint_as_float(ge[i - 1][0]), __uint_as_float(ge[i - 1][2])); }
      if constexpr (P == PC + 6) { cf4(cr, ci, mr, mi, 0.f, 0.f); float t = cr, u = ci; swap16(cr, t); swap16(ci, u); cr = vadd(cr, t); ci = vadd(ci, u); }
      if constexpr (P == PC + 7) { float t = cr, u = ci; swap32(cr, t); swap32(ci, u); cr = vadd(cr, t); ci = vadd(ci, u); cf4(cr, ci, pwr, pwi, prer, prei); }
      if constexpr (P == PC + 8) { sr[0] = cr; si[0] = ci; cf4(cr, ci, a8r, a8i, er[0], ei[0]); er[0] = sr[0]; ei[0] = si[0]; er[1] = cr; ei[1] = ci; }
      if constexpr (P == PC + 9) { kc4(er[0], ei[0], kr, nkr, nki); kc4(er[1], ei[1], kr, nkr, nki); }
      if constexpr (P % 16 == 4 && P / 16 >= PFW_S && P / 16 < 6) sfor<0, PFW>([&](auto U) { constexpr int k = (P / 16 - PFW_S) * PFW + U + PFW_O, m = k < 4 ? 18 + k : k < 18 ? k : k - 18;
        if constexpr (k < 22) { const unsigned o = kofs(m < 18 ? (m + 14) % 18 : m, 16384); if (o < (unsigned)cnt * 16384) for (int r = 0; r < 4; ++r) sline((u64)(wd + kb * 8192), o + ct * 128 + (4 * (c & 3) + r) * 1024); } });
      sfor<0, 8>([&](auto K) { constexpr int k = K; if constexpr (P == nxt(P2, k)) {
        for (int r = 0; r < 2; ++r) cf4c(k ? R[2 * k - 2 + r] : er[r], k ? I_[2 * k - 2 + r] : ei[r], ar, ai, nai, R[2 * k + r], I_[2 * k + r]);
        hp[0][k] = pk2(R[2 * k], R[2 * k + 1]); hp[1][k] = pk2(I_[2 * k], I_[2 * k + 1]); } });
      static_assert(PT > nxt(P2, 7) && PS > PT && PF > PS && PC % 16 == 0, "scan schedule");
      if constexpr (P == PT) {
        __builtin_amdgcn_s_setprio(3);
        for (int part = 0; part < 2; ++part) {
          td[part] = mfma((u32x4){hp[part][0], hp[part][1], hp[part][2], hp[part][3]}, oh[0], (f32x16){});
          td[part] = mfma((u32x4){hp[part][4], hp[part][5], hp[part][6], hp[part][7]}, oh[1], td[part]); pin(td[part]); }
        __builtin_amdgcn_s_setprio(HPB); }
      if constexpr (P == PS) { for (int k = 0; k < 4; ++k) for (int d = 0; d < 4; ++d) fr4[k][d] = pk2(td[k >> 1][8 * (k & 1) + 2 * d], td[k >> 1][8 * (k & 1) + 2 * d + 1]); }
      if constexpr (P == PS) { for (int k = 0; k < 4; ++k)
          __builtin_amdgcn_raw_buffer_store_b128(fr4[k], rws, a2pb + (2 * g + sb_) * 1024 + va + ((k & 1) * NKS + (k >> 1) * 32) * 1024, 0, 16); }
      if constexpr (P == 100) { const int ks = kb + lane, p6 = (ks * 43) >> 8, pg = ks < 84 ? p6 + (p6 >= 7) : 7 + 8 * (ks - 84), pb = ks < 84 ? ks - 6 * p6 : 0;
        fo_g = lane < cnt ? (pg * 8 + 4 + 2 * rsel + (pb >= 3)) * 128 : OOB; pin(fo_g); }
      if constexpr (P == 104) { for (int rb = 0; rb < 2; ++rb) { const int e = 4 * rb + (l5 >> 3), d = (e * 11) >> 5;
          pa[rb] = (3 + d) * SLT + (e - 3 * d) * 2048 + w6 + (lane & 7) * 256 + ((h ^ (lane & 7)) * 16); pin(pa[rb]); } }
      if constexpr (P == 106) { tdl = (8 * h + q) * 128 + (((blk * 2 + (pp >> 1)) ^ ((q >> 1) << 2)) * 16) + (pp & 1) * 8; pin(tdl); }
      if constexpr (P == PF) { wait_vm<hb_cnt(PF, false) - hb_cnt(PS, true)>(); set_flag(w);
        if (cc == 31) __builtin_amdgcn_raw_buffer_store_b64((u32x2){t1l, t1h}, rws, (lane == 0 && sb_ == 0) ? WS_E1 + g * 128 : OOB, 0, 16); }
    };
    u32x4 cq[8][4];
    u32x2 fs;
    const unsigned fo_s = lane < 8 ? ((4 * w + (lane >> 1)) * 8 + 2 * rsel + (lane & 1)) * 128 : OOB;
    sfor<0, 8>([&](auto N) { constexpr int n = N, g0 = hb_g0(n), done = n ? hb_cnt(16 * n - 1, true) : 0;
      if constexpr (n > 0) { if constexpr (n == 1) wait_vm<done>(); STB(30 + 2 * n) bar(); STB(31 + 2 * n) } else STB(31)
      if constexpr (n > 1) { wait_vm<done - hb_cnt(16 * (n > 1 ? n - 2 : 0) + hb_g0(n - 2) + 3, false)>(); rd_c(n, cq[n - 2]); sb(); }
      sfor<0, hb_ops(n)>([&](auto I) { constexpr int i = I, io = i < g0 ? i : i - 4;
        if constexpr (i >= g0 && i < g0 + 4) { if constexpr (i == g0 && n > 1) { for (int s = 0; s < 4; ++s) use(cq[n - 2][s]); } c_op(N, IC(i - g0)); }
        else if constexpr (n >= 6) { constexpr int e = 5 * (n - 6) + (io >> 1); wd_op(IC(e < 4 ? 18 + e : e), IC(e), IC(io & 1)); }
        if constexpr (16 * n + i == PPOLL) ld64_sc1(fs, sws, fo_s, flagb);
        side(IC(16 * n + i));
        sb();
      });
    });
    __builtin_amdgcn_s_setprio(HPC);
    STAMP(3)
    auto poll = [&](unsigned fo, u32x2 fv) {
#pragma unroll 1
      for (unsigned spins = 0; spins < SPIN_MAX; ++spins) {
        const bool ok = fo == OOB || (fv[0] == (unsigned)tag2 && fv[1] == (unsigned)(tag2 >> 32));
        if (__all(ok)) break;
        __builtin_amdgcn_s_sleep(SLP);
        ld64_sc1(fv, sws, fo, flagb); wait_vm<0>(); pin(fv);
      }
    };
    const unsigned a2c = WS_A2 + (unsigned)(c * 4 + 2 * rsel) * (NKS * 1024);
    const i32x4 sg0 = mk_srd(ws + a2c + (64 + kb) * 1024, cnt * 1024), sg1 = mk_srd(ws + a2c + (NKS + 64 + kb) * 1024, cnt * 1024);
    const unsigned vs0 = va + a2c + w * 8192, vs1 = vs0 + NKS * 1024;
    wait_vm<hb_cnt(127, true) - hb_cnt(PPOLL, true)>(); pin(fs);
    rd_c(0, cq[6]);
    STAMP(7)
    poll(fo_s, fs);
    STAMP(4)
    unsigned tb2[4][2];
    auto rd_half = [&](int e, int cb) { const int k = cell_k(e) == 0 ? (cell_c(e) >= 5 * SLT) : cell_k(e) + 1;
      return lds_tr2(tb2[k][cb] + (cell_c(e) - (k == 0 ? 3 * SLT : k == 1 ? 5 * SLT : k == 3 ? OFF_B : 0)), 512); };
    f32x16 acc2[2][2];
    u32x4 L[76], wq[4][2], bf[2][2];
    unsigned so_l = 0;
    auto soL = [&](auto M) { constexpr int m = M;
      if constexpr (m < 16) { if constexpr (!(m & 3)) { const int kg = ((m >> 2) + lnd(xcd)) & 3; so_l = ((kg >> 1) * 32 + (kg & 1) * 4) * 1024; } }
      else so_l = kofs(m - 16, 1024); };
    auto ldH = [&](auto M, auto R) { constexpr int m = M, r = R;
      if constexpr (m < 16) ld128_sc1<(m & 3) * 1024>(L[2 * m + r], sws, r ? vs1 : vs0, so_l); else ld128_sc1<0>(L[2 * m + r], r ? sg1 : sg0, va, so_l); };
    auto ldL = [&](auto M) { soL(M); ldH(M, IC(0)); ldH(M, IC(1)); };
    sfor<0, p2_l0(0)>(ldL);
    wait_lgkm0();
    dma_wd(IC(16), IC(16)); dma_wd(IC(17), IC(17));
    wait_cnt();
    sfor<10, 13>([&](auto E) { dma_wd(E, E); });
    sfor<0, 38>([&](auto M) { constexpr int m = M, mm = m - 16;
      constexpr bool one = p2_l0(m + 1) == p2_l0(m) + 1;
      wait_vm<p2_after(m)>(); pin(L[2 * m]); pin(L[2 * m + 1]);
      sfor<0, 4>([&](auto G) { constexpr int gp = G, cb = gp >> 1, rb = gp & 1;
        if constexpr (m < 16) acc2[cb][rb] = mfma(cq[2 * (m >> 2) + cb][m & 3], L[2 * m + rb], m == 0 ? (f32x16){} : acc2[cb][rb]);
        else acc2[cb][rb] = mfma(mm < 18 ? bf[mm & 1][cb] : wq[mm < 18 ? 0 : mm - 18][cb], L[2 * m + rb], acc2[cb][rb]);
        sb();
        if constexpr (gp < 2) {
          if constexpr (m >= 15 && mm + 1 < 18) bf[(mm + 1) & 1][gp] = rd_half((mm + 5) % 18, gp);
          if constexpr (m == 0 || m == 1) wd_op(IC(13 + m), IC(13 + m), G);
          if constexpr (m == 2 || m == 3) { constexpr int k = 2 * (m - 2) + gp;
            for (int u = 0; u < 2; ++u) { tb2[k][u] = (tdl ^ (u * 64)) + (k == 0 ? 3 * SLT + w6 : k == 1 ? 5 * SLT + w6 : k == 2 ? w4 : OFF_B + w2); pin(tb2[k][u]); } }
          if constexpr (m == 4 && gp == 0) rd_c(1, cq[7]);
          if constexpr (m == 5) { if constexpr (gp == 0) { for (int s = 0; s < 4; ++s) use(cq[7][s]); } wd_op(IC(15), IC(15), G); }
          if constexpr (m >= 7 && m <= 10) { if constexpr (gp == 0) { use(wq[m - 7][0]); use(wq[m - 7][1]); } wd_op(IC(m - 7), IC(m - 7), G); }
          if constexpr (m >= 6 && m <= 9) wq[m - 6][gp] = rd_half(m - 6, gp);
          if constexpr (gp == 1 && m == PSM) ld64_sc1(fs, sws, fo_g, flagb);
          if constexpr (gp == 1 && one) soL(IC(p2_l0(m)));
        } else {
          if constexpr (gp == 2 && m == PCK) { STAMP(5) wait_vm<p2_exsum(PSM + 1, PCK) + 2 * (16 - p2_l0(PSM))>(); pin(fs); poll(fo_g, fs); STAMP(6) }
          if constexpr (one) ldH(IC(p2_l0(m)), IC(gp - 2));
          else if constexpr (gp == 2) sfor<p2_l0(m), p2_l0(m + 1)>(ldL);
        }
        sb();
      });
    });
    STAMP(9)
    for (int rb = 0; rb < 2; ++rb) for (int rq = 0; rq < 4; ++rq) for (int cb = 0; cb < 2; ++cb)
      *(LAS f32x4*)(size_t)((pa[rb] ^ (rq * 32)) + cb * 128) =
          (f32x4){acc2[cb][rb][4 * rq], acc2[cb][rb][4 * rq + 1], acc2[cb][rb][4 * rq + 2], acc2[cb][rb][4 * rq + 3]};
    wait_lgkm0(); bar();
    STAMP(11)
  }
  STAMP(12)
  STAMP_OUT
  asm volatile(".Lpf_end:");
}

extern "C" void kernel_launch(void* const* d_in, const int* in_sizes, int n_in, void* d_out, int out_size, void* d_ws, size_t ws_size,
                              hipStream_t stream) {
  (void)hipFuncSetAttribute((const void*)fused, hipFuncAttributeMaxDynamicSharedMemorySize, LDS_BYTES);
  fused<<<256, 512, LDS_BYTES, stream>>>((const u16*)d_in[0], (const u16*)d_in[1], (const u16*)d_in[2], (const u16*)d_in[3], (const u16*)d_in[4],
                                         (const u16*)d_in[5], (const u16*)d_in[6], (const u16*)d_in[7], (const u16*)d_in[8], (const u16*)d_in[10],
                                         (const u16*)d_in[11], (const u16*)d_in[12], (char*)d_ws, (float*)d_out);
}
```

```cpp
#include <hip/hip_runtime.h>
#include <hip/hip_bf16.h>
#include <type_traits>

typedef unsigned short u16;
typedef unsigned long long u64;
typedef short s16x4 __attribute__((ext_vector_type(4)));
typedef __bf16 bf16x2 __attribute__((ext_vector_type(2)));
typedef __bf16 bf16x8 __attribute__((ext_vector_type(8)));
typedef float f32x2 __attribute__((ext_vector_type(2)));
typedef float f32x4 __attribute__((ext_vector_type(4)));
typedef float f32x16 __attribute__((ext_vector_type(16)));
typedef unsigned u32x2 __attribute__((ext_vector_type(2)));
typedef unsigned u32x4 __attribute__((ext_vector_type(4)));
typedef int i32x4 __attribute__((ext_vector_type(4)));
#define LAS __attribute__((address_space(3)))
#define DEVI __device__ __forceinline__

constexpr int SL = 2048, SD = 512, SP = 512, SF = 1376;
constexpr int NKS = 150;
constexpr unsigned WS_E1 = 0;
constexpr unsigned WS_E2 = 2048;
constexpr unsigned WS_GRAN = 262144;
constexpr unsigned WS_FLAG = 524288;
constexpr unsigned WS_A2 = 786432;
constexpr unsigned A2_BYTES = 64u * NKS * 1024u;
constexpr unsigned WS_BYTES = WS_A2 + A2_BYTES;
constexpr unsigned OOB = 0x40000000u;
constexpr int SLT = 24832;
constexpr int OFF_B = 16384, OFF_WU = 12288 + 32;
constexpr int MISC = 6 * SLT;
constexpr int M_ST = MISC, M_SD = MISC + 1024, M_CNT = MISC + 2048, M_CS = MISC + 2304, M_T = MISC + 3840;
constexpr int LDS_BYTES = 163840;
constexpr unsigned SPIN_MAX = 1u << 16;
constexpr int cell_k(int e) { return e >= 15 ? 2 : (e % 5) < 3 ? 0 : 1; }
constexpr int cell_c(int e) { return e >= 15 ? ((e - 13) % 3) * SLT + OFF_B : (e % 5) < 3 ? (3 + (e / 5 + 2) % 3) * SLT + (e % 5) * 2048 : ((e / 5 + 2) % 3) * SLT + (e % 5 - 3) * 2048; }

#define STAMP_DECL
#define STAMP(i) {}
#define STAMP_OUT
#define STB(i) {}
template <int B, int E, class Fn> DEVI void sfor(Fn&& f) {
  if constexpr (B < E) { f(std::integral_constant<int, B>{}); sfor<B + 1, E>(f); }
}
DEVI i32x4 mk_srd(const void* p, unsigned bytes) {
  u64 a = (u64)p; return (i32x4){(int)(unsigned)a, (int)((unsigned)(a >> 32) & 0xffffu), (int)bytes, 0x00020000};
}
DEVI void dma(i32x4 srd, unsigned voff, unsigned soff, unsigned lds) {
  asm volatile("s_mov_b32 m0, %0\n\ts_nop 0\n\tbuffer_load_dwordx4 %1, %2, %3 offen lds" ::"s"(lds), "v"(voff), "s"(srd), "s"(soff) : "memory");
}
template <int OFF> DEVI void ld128_sc1(u32x4& d, i32x4 srd, unsigned voff, unsigned soff) {
  asm volatile("buffer_load_dwordx4 %0, %1, %2, %3 offen offset:%4 sc1" : "=v"(d) : "v"(voff), "s"(srd), "s"(soff), "n"(OFF) : "memory");
}
template <int OFF> DEVI void ld128_sc1_rw(u32x4& d, i32x4 srd, unsigned voff, unsigned soff) {
  asm volatile("buffer_load_dwordx4 %0, %1, %2, %3 offen offset:%4 sc1" : "+v"(d) : "v"(voff), "s"(srd), "s"(soff), "n"(OFF) : "memory");
}
DEVI void ld64_sc1(u32x2& d, i32x4 srd, unsigned voff, unsigned soff) {
  asm volatile("buffer_load_dwordx2 %0, %1, %2, %3 offen sc1" : "=v"(d) : "v"(voff), "s"(srd), "s"(soff) : "memory");
}
DEVI void ld64(u32x2& d, i32x4 srd, unsigned voff, unsigned soff) {
  asm volatile("buffer_load_dwordx2 %0, %1, %2, %3 offen" : "=v"(d) : "v"(voff), "s"(srd), "s"(soff) : "memory");
}
DEVI void ld16(unsigned& d, i32x4 srd, unsigned voff) {
  asm volatile("buffer_load_ushort %0, %1, %2, 0 offen" : "=v"(d) : "v"(voff), "s"(srd) : "memory");
}
template <int N> DEVI void wait_vm() { asm volatile("s_waitcnt vmcnt(%0)" ::"n"(N) : "memory"); }
template <class T> DEVI void pin(T& a) { asm volatile("" : "+v"(a)::"memory"); }
template <class T> DEVI void use(const T& a) { asm volatile("" ::"v"(a) : "memory"); }
DEVI int lnd(int a) { asm volatile("" : "+s"(a)); return a; }
DEVI int ftid() { int t = threadIdx.x; asm volatile("" : "+v"(t)); return t; }
DEVI void wait_lgkm0() { asm volatile("s_waitcnt lgkmcnt(0)" ::: "memory"); }
DEVI void bar() { asm volatile("s_barrier" ::: "memory"); }
DEVI void sb() { __builtin_amdgcn_sched_barrier(0); }
DEVI float lo2f(unsigned u) { return __uint_as_float(u << 16); }
DEVI float hi2f(unsigned u) { return __uint_as_float(u & 0xffff0000u); }
DEVI unsigned pk2(float a, float b) { f32x2 v = {a, b}; return __builtin_bit_cast(unsigned, __builtin_convertvector(v, bf16x2)); }
DEVI float rbf(float a) { return lo2f(pk2(a, a)); }
DEVI u32x4 lds_r128(unsigned a) { return *(LAS u32x4*)(size_t)a; }
DEVI u32x2 lds_tr(unsigned a) { return __builtin_bit_cast(u32x2, __builtin_amdgcn_ds_read_tr16_b64_v4i16((LAS s16x4*)(size_t)a)); }
DEVI u32x4 lds_tr2(unsigned a, unsigned d) { u32x2 x = lds_tr(a), y = lds_tr(a + d); return (u32x4){x[0], x[1], y[0], y[1]}; }
DEVI f32x16 mfma(u32x4 a, u32x4 b, f32x16 c) {
  return __builtin_amdgcn_mfma_f32_32x32x16_bf16(__builtin_bit_cast(bf16x8, a), __builtin_bit_cast(bf16x8, b), c, 0, 0, 0);
}
DEVI float dot2(unsigned a, unsigned b, float c) {
  return __builtin_amdgcn_fdot2_f32_bf16(__builtin_bit_cast(bf16x2, a), __builtin_bit_cast(bf16x2, b), c, false);
}
DEVI void swap16(float& a, float& b) { auto r = __builtin_amdgcn_permlane16_swap(__float_as_uint(a), __float_as_uint(b), false, false); a = __uint_as_float(r[0]); b = __uint_as_float(r[1]); }
DEVI void swap32(float& a, float& b) { auto r = __builtin_amdgcn_permlane32_swap(__float_as_uint(a), __float_as_uint(b), false, false); a = __uint_as_float(r[0]); b = __uint_as_float(r[1]); }
DEVI float sum32(float v) { float a = v, b = v; swap32(a, b); return a + b; }
DEVI float sum16(float v) { float a = v, b = v; swap16(a, b); return a + b; }
DEVI void cmul(float& dr, float& di, float ar, float ai, float br, float bi) { float t = ar * br - ai * bi; di = ar * bi + ai * br; dr = t; }
DEVI void cfma(float& hr, float& hi, float ar, float ai, float br, float bi) {
  float t = __builtin_fmaf(ar, hr, __builtin_fmaf(-ai, hi, br)); hi = __builtin_fmaf(ar, hi, __builtin_fmaf(ai, hr, bi)); hr = t;
}

template <int OFF> DEVI void dmao(i32x4 srd, unsigned voff, unsigned soff) {
  asm volatile("buffer_load_dwordx4 %0, %1, %2 offen offset:%3 lds" ::"v"(voff), "s"(srd), "s"(soff), "n"(OFF) : "memory");
}
DEVI void setm0(unsigned lds) { asm volatile("s_mov_b32 m0, %0\n\ts_nop 0" ::"s"(lds) : "memory"); }
DEVI void touch(i32x4 srd, unsigned voff, unsigned soff) {
  asm volatile("buffer_load_dword %0, %1, %2 offen lds" ::"v"(voff), "s"(srd), "s"(soff) : "memory");
}
DEVI f32x4 mfma16(u32x4 a, u32x4 b, f32x4 c) {
  return __builtin_amdgcn_mfma_f32_16x16x32_bf16(__builtin_bit_cast(bf16x8, a), __builtin_bit_cast(bf16x8, b), c, 0, 0, 0);
}
DEVI float vadd(float a, float b) { float r; asm("v_add_f32 %0, %1, %2" : "=v"(r) : "v"(a), "v"(b)); return r; }
DEVI float vfma(float a, float b, float c) { float r; asm("v_fma_f32 %0, %1, %2, %3" : "=v"(r) : "v"(a), "v"(b), "v"(c)); return r; }
DEVI f32x2 pfma(f32x2 a, f32x2 b, f32x2 c) { return __builtin_elementwise_fma(a, b, c); }
DEVI f32x2 sp(float a) { return (f32x2){a, a}; }
DEVI void cfma2(f32x2& hr, f32x2& hi, f32x2 a, f32x2 br, f32x2 bi) {
  f32x2 t1, t2;
  asm("v_pk_fma_f32 %2, %4, %1, %5 op_sel:[1,0,0] op_sel_hi:[1,1,1] neg_lo:[1,0,0] neg_hi:[1,0,0]\n\t"
      "v_pk_fma_f32 %3, %4, %0, %6 op_sel:[1,0,0] op_sel_hi:[1,1,1]\n\t"
      "v_pk_fma_f32 %0, %4, %0, %2 op_sel:[0,0,0] op_sel_hi:[0,1,1]\n\t"
      "v_pk_fma_f32 %1, %4, %1, %3 op_sel:[0,0,0] op_sel_hi:[0,1,1]"
      : "+v"(hr), "+v"(hi), "=&v"(t1), "=&v"(t2) : "v"(a), "v"(br), "v"(bi));
}
DEVI void cout2(unsigned& o0, unsigned& o1, f32x2 k, f32x2 gr, f32x2 gi) {
  f32x2 t1, t2;
  asm("v_pk_mul_f32 %0, %2, %4 op_sel:[1,0] op_sel_hi:[1,1]\n\t"
      "v_pk_mul_f32 %1, %2, %3 op_sel:[1,0] op_sel_hi:[1,1]\n\t"
      "v_pk_fma_f32 %0, %2, %3, %0 op_sel:[0,0,0] op_sel_hi:[0,1,1] neg_lo:[0,0,1] neg_hi:[0,0,1]\n\t"
      "v_pk_fma_f32 %1, %2, %4, %1 op_sel:[0,0,0] op_sel_hi:[0,1,1] neg_lo:[1,0,1] neg_hi:[1,0,1]"
      : "=&v"(t1), "=&v"(t2) : "v"(k), "v"(gr), "v"(gi));
  o0 = pk2(t1[0], t1[1]); o1 = pk2(t2[0], t2[1]);
}
#define SLP 4
#define WN 10
#define PGV 50
#define PCV 64
#define P2D 16
#define PTV 88
#define PSV 89
#define PFV 106
#define PPLV 120
#define PFW 1
#define PFW_S 2
#define PFW_O 0
#define PFX 0
#define ROTX (2 * (xcd >> 1) + 4 * (xcd & 1))
#define PLATE 0
#define AVAR 0
#define STA(i) {}
#define HPA 0
#define WEARLY 0
#define PFW0 0
#define HPB 0
#define HPC 0
#define PFG 0
#define PFG_O 0
#define PCK 11
#define PSM (PCK - 3)
#define WG 6
#define EPK 1
#define CPE 0
#define PFT 0
#define OAUX 16
#define LEADX (c + g)
#define LEAD 1
#define IC(x) std::integral_constant<int, (x)>{}
constexpr int p2_extra(int m) { return ((m == 0 || m == 1 || m == 5 || (m >= 7 && m <= 10)) ? 2 : 0) + (m == PSM ? 1 : 0); }
constexpr int p2_exsum(int a, int b) { int t = 0; for (int m = a; m <= b; ++m) t += p2_extra(m); return t; }
constexpr int p2_l0(int m) { return m <= PCK ? (m + WN / 2 < 16 ? m + WN / 2 : 16) : (m + WG < 38 ? m + WG : 38); }
constexpr int p2_after(int m) {
  int pos = 0, posL[40] = {};
  for (int k = 0; k < p2_l0(0); ++k) { pos += 2; posL[k] = pos; }
  pos += 10;
  for (int s = 0; s < m; ++s) { pos += p2_extra(s); for (int k = p2_l0(s); k < p2_l0(s + 1); ++k) { pos += 2; posL[k] = pos; } }
  return pos - posL[m];
}
constexpr int PPUB = 25, PG = PGV, PC = PCV, P2 = PC + P2D, PT = PTV, PS = PSV, PF = PFV, PPOLL = PPLV;
constexpr int hb_ops(int n) { return n < 6 ? 10 : 14; }
constexpr int hb_g0(int n) { return n < 6 ? 3 : 4; }
constexpr int hb_extra(int P) { return P == PPUB ? 1 : P == PG ? 8 : P == PS ? 4 : P == PF ? 1 : P == PPOLL ? 1 : 0; }
constexpr int hb_cnt(int P, bool with_extra) {
  int t = 0;
  for (int n = 0; n < 8; ++n) for (int i = 0; i < hb_ops(n); ++i) { const int p = 16 * n + i; if (p > P) return t; t += (n >= 6 || (i >= hb_g0(n) && i < hb_g0(n) + 4)); if (p < P || with_extra) t += hb_extra(p); }
  return t;
}
constexpr int nxt(int P, int d) { for (; d > 0; --d) { ++P; if (P % 16 >= hb_ops(P / 16)) P = (P / 16 + 1) * 16; } return P; }
DEVI u64 utag(u32x2 e) { const u64 t = (((u64)(unsigned)__builtin_amdgcn_readfirstlane(e[1]) << 32) | (unsigned)__builtin_amdgcn_readfirstlane(e[0])) + 1; return t ? t : 1; }
template <int CTRL> DEVI float dppf(float v) { return __int_as_float(__builtin_amdgcn_update_dpp(0, __float_as_int(v), CTRL, 0xf, 0xf, true)); }
DEVI void sline(u64 base, unsigned off) {
  asm volatile("s_load_dwordx2 s[98:99], %0, %1" ::"s"(base), "s"(off + 60) : "memory", "s98", "s99");
}
template <int IMM> DEVI void setm0i(unsigned base) { asm volatile("s_add_i32 m0, %0, %1\n\ts_nop 0" ::"s"(base), "n"(IMM) : "memory", "scc"); }
DEVI void miss2(unsigned& n, u32x4 a, u32x4 b, unsigned tl, unsigned th) {
  unsigned t, u;
  asm("v_xor_b32 %1, %7, %3\n\tv_xor_b32 %2, %7, %5\n\tv_bitop3_b32 %1, %4, %8, %1 bitop3:0xbe\n\tv_bitop3_b32 %2, %6, %8, %2 bitop3:0xbe\n\t"
      "v_min_u32 %1, 1, %1\n\tv_min_u32 %2, 1, %2\n\tv_add3_u32 %0, %0, %1, %2"
      : "+v"(n), "=&v"(t), "=&v"(u) : "v"(a[1]), "v"(a[3]), "v"(b[1]), "v"(b[3]), "s"(tl), "s"(th));
}
DEVI unsigned geglu2(float g0, float g1, float u0, float u1, float k1, float k3, float s2) {
  float a, b;
  asm("v_mul_f32 %0, %4, %4\n\tv_mul_f32 %1, %5, %5\n\tv_fma_f32 %0, %0, %7, %6\n\tv_fma_f32 %1, %1, %7, %6\n\t"
      "v_mul_f32 %0, %0, %4\n\tv_mul_f32 %1, %1, %5\n\tv_exp_f32 %0, %0\n\tv_exp_f32 %1, %1\n\tv_mul_f32 %2, %2, %4\n\t"
      "v_fma_f32 %0, %0, %8, %8\n\tv_fma_f32 %1, %1, %8, %8\n\tv_rcp_f32 %0, %0\n\tv_rcp_f32 %1, %1\n\tv_mul_f32 %3, %3, %5\n\t"
      "v_mul_f32 %0, %0, %2\n\tv_mul_f32 %1, %1, %3\n\tv_cvt_pk_bf16_f32 %0, %0, %1"
      : "=&v"(a), "=&v"(b), "+v"(u0), "+v"(u1) : "v"(g0), "v"(g1), "v"(k1), "v"(k3), "v"(s2));
  return __float_as_uint(a);
}
DEVI void cf4(float& hr, float& hi, float ar, float ai, float br, float bi) {
  float t, u;
  asm("v_fma_f32 %0, -%5, %3, %6\n\tv_fma_f32 %1, %5, %2, %7\n\tv_fmac_f32 %0, %4, %2\n\tv_fmac_f32 %1, %4, %3"
      : "=&v"(t), "=&v"(u) : "v"(hr), "v"(hi), "v"(ar), "v"(ai), "v"(br), "v"(bi));
  hr = t; hi = u;
}
DEVI void csq(float& dr, float& di, float r, float i) {
  float t, u;
  asm("v_mul_f32 %0, %2, %2\n\tv_mul_f32 %1, %2, %3\n\tv_fma_f32 %0, -%3, %3, %0\n\tv_add_f32 %1, %1, %1" : "=&v"(t), "=&v"(u) : "v"(r), "v"(i));
  dr = t; di = u;
}
DEVI void cf4c(float hr, float hi, float ar, float ai, float nai, float& br, float& bi) {
  asm("v_fmac_f32 %0, %5, %3\n\tv_fmac_f32 %1, %6, %2\n\tv_fmac_f32 %0, %4, %2\n\tv_fmac_f32 %1, %4, %3"
      : "+v"(br), "+v"(bi) : "v"(hr), "v"(hi), "v"(ar), "v"(ai), "v"(nai));
}
DEVI void sc4(float& r, float& i, float rs, float rc, float csr, float csi) {
  asm("v_mul_f32 %0, %2, %0\n\tv_mul_f32 %1, %2, %1\n\tv_fmac_f32 %0, %3, %4\n\tv_fmac_f32 %1, %3, %5"
      : "+v"(r), "+v"(i) : "v"(rs), "v"(rc), "v"(csr), "v"(csi));
}
DEVI void kc4(float& r, float& i, float kr, float nkr, float nki) {
  float t, u;
  asm("v_mul_f32 %0, %4, %2\n\tv_mul_f32 %1, %5, %3\n\tv_fmac_f32 %0, %6, %3\n\tv_fmac_f32 %1, %6, %2"
      : "=&v"(t), "=&v"(u) : "v"(r), "v"(i), "v"(kr), "v"(nkr), "v"(nki));
  r = t; i = u;
}

__global__ __launch_bounds__(512, 2) void fused(const u16* __restrict__ x, const u16* __restrict__ lam_re, const u16* __restrict__ lam_im,
                                                const u16* __restrict__ Bre, const u16* __restrict__ Bim, const u16* __restrict__ Cre,
                                                const u16* __restrict__ Cim, const u16* __restrict__ logdt, const u16* __restrict__ dskip,
                                                const u16* __restrict__ wg, const u16* __restrict__ wu, const u16* __restrict__ wd,
                                                char* __restrict__ ws, float* __restrict__ out) {
  extern __shared__ __attribute__((aligned(16))) char shm[];
  asm volatile(".Lpf_beg:");
  STAMP_DECL STAMP(0)
  unsigned perm;
  const i32x4 sx = mk_srd(x, SL * SD * 2);
  {
    const int b0 = blockIdx.x, t = threadIdx.x, lead = ((b0 >> 6) + (b0 >> 3)) & 3, rot = (b0 * 5) & 6;
    perm = (0x76543210u - (15u << (4 * lead)) * (1u + ((((lead + 1) >> 2) ^ 1) << 16)) + rot * 0x11111111u) & 0x77777777u;
    const unsigned v0 = (((t >> 2) & 64) + ((t >> 3) & 16) + ((t >> 4) & 5) + (t & 40)) * 1024 + (((t & 7) ^ ((t >> 4) & 3)) * 16);
    const unsigned so = (((b0 & 6) * 2 + (b0 >> 6)) << 17) + (perm & 7) * 128;
    asm volatile("" ::"s"(lam_re), "s"(lam_im), "s"(Bre), "s"(Bim), "s"(Cre), "s"(Cim), "s"(logdt), "s"(dskip), "s"(wg), "s"(wu), "s"(wd), "s"(ws), "s"(out));
    const int j0 = __builtin_amdgcn_readfirstlane(t >> 6);
    const unsigned v1 = (v0 ^ 64) + 1024;
    setm0i<SLT>(j0 * 2048); dmao<0>(sx, v0, so); dmao<1024>(sx, v1, so);
    const i32x4 sb0 = mk_srd((j0 >> 2) ? Bim : Bre, SP * SD * 2);
    const unsigned vb0 = ((t >> 3) & 31) * 1024 + (((t & 7) ^ ((t >> 4) & 7)) * 16), sob = (((b0 & 1) * 8 + ((b0 >> 3) & 7)) << 15);
    setm0i<SLT + OFF_B>(j0 * 1024); dmao<0>(sb0, vb0, sob + (perm & 7) * 128);
    const unsigned k1 = ((perm >> 4) & 7) * 128;
    setm0i<2 * SLT>(j0 * 2048); dmao<0>(sx, v0, (so & ~1023u) + k1); dmao<1024>(sx, v1, (so & ~1023u) + k1);
    setm0i<2 * SLT + OFF_B>(j0 * 1024); dmao<0>(sb0, vb0, sob + k1);
  }
  auto chunk = [&](int v) { return (perm >> (4 * v)) & 7; };
  const int j = __builtin_amdgcn_readfirstlane(ftid() >> 6), w = j & 3, sb_ = w & 1, pi = w >> 1;
  const int b = lnd(blockIdx.x), xcd = b & 7, idx = b >> 3;
  const int c = (xcd >> 1) * 4 + (idx >> 3), rsel = xcd & 1, gi = idx & 7, g = rsel * 8 + gi;
  auto get_fb = [&]() { const int l = (lnd(gi) + 1) >> 3; return (6 * (g - rsel)) * (1 - l) + (84 + rsel) * l; };
  auto get_ct = [&]() { const int slow = (3 - lnd((int)((u64)wd >> 7))) & 7; return lnd(gi == 7 ? slow : gi == slow ? 7 : gi); };
  const i32x4 sws = mk_srd(ws, WS_BYTES);
  const __amdgpu_buffer_rsrc_t rws = __builtin_amdgcn_make_buffer_rsrc(ws, 0, WS_BYTES, 0x00020000);
  const unsigned flagb = WS_FLAG + c * 16384;
  const unsigned a2pb = WS_A2 + (unsigned)(c * 4 + 2 * pi) * (NKS * 1024);
  const unsigned w4 = w * 4096, w2 = w * 2048, w6 = w * 6144;
  const unsigned so_x = (c * 128 + 64 * (w >> 1) + 16 * (w & 1)) * 1024;
  u64 tag2 = 0;
  auto set_flag = [&](int fi) {
    __builtin_amdgcn_raw_buffer_store_b64((u32x2){(unsigned)tag2, (unsigned)(tag2 >> 32)}, rws, (ftid() & 63) == 0 ? flagb + (g * 8 + fi) * 128 : OOB, 0, 16);
  };
  auto wait_cnt = [&]() {
#pragma unroll 1
    for (unsigned spins = 0; spins < SPIN_MAX; ++spins)
      if (__builtin_amdgcn_readfirstlane(__hip_atomic_load((LAS unsigned*)(size_t)M_CNT, __ATOMIC_RELAXED, __HIP_MEMORY_SCOPE_WORKGROUP)) == 4u) break;
    asm volatile("" ::: "memory");
  };

  if (j < 4) {
    const int lane = ftid() & 63, l5 = lane & 31, h = lane >> 5, blk = (lane >> 4) & 1, q = (lane & 15) >> 2, pp = lane & 3;
    unsigned vx[4], vw[3], ra[4], wt[2];
    u32x4 xk[3][4][2];
    const i32x4 sw = mk_srd((w >> 1) ? wu : wg, SD * SF * 2);
    const unsigned ld_w = ((w >> 1) ? OFF_WU : 0) + (w & 1) * 6144;
    unsigned so_w = 0;
    u32x4 sel;
    {
      const unsigned sw0 = ((lane & 7) ^ (lane >> 4)) * 16;
      for (int r = 0; r < 4; ++r) vx[r] = (32 * (lane >> 5) + 8 * ((lane >> 3) & 1) + ((lane >> 4) & 1) + r) * 1024 + (sw0 ^ ((r & 1) * 64));
      auto ldX = [&](auto S) { constexpr int s = S;
        const unsigned so = so_x + chunk(s) * 128;
        setm0i<((s + 1) % 6) * SLT>(w4);
        dmao<0>(sx, vx[0], so); dmao<1024>(sx, vx[1], so); dmao<2048>(sx, vx[2], so); dmao<3072>(sx, vx[3], so);
      };
      u32x2 ep2;
      STAMP(1)
      if (w == 0 && lane == 0) *(LAS unsigned*)(size_t)M_CNT = 0;
      const unsigned xa = w * 4096 + lane * 16;
      unsigned ti;
      auto setup = [&]() {
        for (int s = 0; s < 4; ++s) { ra[s] = pi * 8192 + l5 * 128 + (((2 * (s ^ pi) + h) ^ ((l5 >> 1) & 7)) * 16); pin(ra[s]); }
        for (int p = 0; p < 2; ++p) { wt[p] = 3 * SLT + (blk ? OFF_WU : 0) + (8 * h + q) * 192 + (((pp & 1) << 1) | (pp >> 1)) * 8 + sb_ * 96 + (p ? -pi * 3072 : pi * 3072); pin(wt[p]); }
        const unsigned selv = (((lane & 15) == 0 && blk == 0) || ((lane & 15) == 4 && blk == 1)) ? 0x3f803f80u : 0u; sel = (u32x4){selv, selv, selv, selv}; pin(sel);
        ti = (lane & 4) ? M_CNT + 64 - M_ST : (64 * (w >> 1) + 32 * (lane >> 5) + 16 * (w & 1) + 4 * (lane & 3) + ((lane >> 3) & 3)) * 8;
        pin(ti);
      };
      auto setup2 = [&]() { const int q12 = (lane * 43) >> 9, r12 = lane - 12 * q12;
        for (int i = 0; i < 3; ++i) { const int r = r12 + 4 * i, cy = r >= 12, col = r - 12 * cy;
          vw[i] = (gi == 7 && col >= 2) ? OOB : (5 * i + q12 + cy) * (SF * 2) + col * 16 - i * 1024; pin(vw[i]); }
        so_w = (w & 1) * 32 * (SF * 2) + get_fb() * 32; };
      float s1[4] = {}, s2[4] = {};
      auto stats = [&](auto S, auto&& mid) { constexpr int s = S;
        u32x4 xr[4];
        for (int k = 0; k < 4; ++k) xr[k] = lds_r128(((s + 1) % 6) * SLT + k * 1024 + xa);
        sb(); mid(); sb();
        for (int e = 0; e < 4; ++e) for (int i = 0; i < 4; ++i) { const unsigned u = xr[i][e]; s1[i] = dot2(u, 0x3f803f80u, s1[i]); s2[i] = dot2(u, u, s2[i]); }
        for (int i = 0; i < 4; ++i) { pin(s1[i]); pin(s2[i]); }
        sb(); };
      wait_vm<3>(); wait_lgkm0(); STA(46) bar(); STA(47)
      stats(IC(0), [&] { ldX(IC(2)); ld64_sc1(ep2, sws, 0, WS_E2 + c * 128); });
      wait_vm<5>(); STA(48) bar(); STA(49)
      stats(IC(1), [&] { ldX(IC(3)); setup(); });
      sfor<2, 8>([&](auto S) { constexpr int s = S;
        wait_vm<(s < 7 ? 4 : 0)>();
        stats(S, [&] {
          if constexpr (s >= 3 && s < 6) { const unsigned hi = lnd(s * SLT); for (int k = 0; k < 4; ++k) { const unsigned a = ra[k] + hi; xk[s - 3][k][0] = lds_r128(a); xk[s - 3][k][1] = lds_r128(a + 4096); } }
          if constexpr (s < 6) ldX(IC(s + 2)); if constexpr (s == 6) setup2(); });
        if constexpr (s >= 3 && s < 6) wait_lgkm0();
        if constexpr (s < 7) { STA(46 + 2 * s) bar(); STA(47 + 2 * s) }
      });
      pin(ep2); tag2 = utag(ep2);
      float t1[2], t2[2];
      for (int k = 0; k < 2; ++k) { const bool o = lane & 1;
        t1[k] = (o ? s1[2 * k + 1] : s1[2 * k]) + dppf<0xB1>(o ? s1[2 * k] : s1[2 * k + 1]); t2[k] = (o ? s2[2 * k + 1] : s2[2 * k]) + dppf<0xB1>(o ? s2[2 * k] : s2[2 * k + 1]); }
      const bool o = lane & 2;
      float sm = (o ? t1[1] : t1[0]) + dppf<0x4E>(o ? t1[0] : t1[1]), sq = (o ? t2[1] : t2[0]) + dppf<0x4E>(o ? t2[0] : t2[1]);
      sm += dppf<0x104>(sm); sq += dppf<0x104>(sq);
      const float mean = sm * (1.f / SD);
      const float var = fmaxf(sq * (1.f / SD) - mean * mean, 0.f);
      const float sd = rbf(__builtin_amdgcn_sqrtf(rbf(rbf(var) + 1e-5f)));
      const float r_ = __builtin_amdgcn_rcpf(sd); const unsigned nm = pk2(-mean, -mean);
      *(LAS f32x2*)(size_t)(M_ST + ti) = (f32x2){r_, lo2f(nm) * r_}; *(LAS f32x2*)(size_t)(M_SD + ti) = (f32x2){sd, __uint_as_float(nm)};
    }
    f32x16 acc[3][2];
    f32x4 csg[3] = {};
    u32x4 af[2][2]; u32x2 wf[2][3][2];
    auto rd1 = [&](auto N, auto S, auto I) { constexpr int n = N, s = S, i = I;
      const unsigned a_ = ra[s] + (n % 3) * SLT, t_ = wt[s & 1] + (n % 3) * SLT + s * 3072;
      if constexpr (i == 0) { wf[s & 1][0][0] = lds_tr(t_); wf[s & 1][0][1] = lds_tr(t_ + 768); }
      if constexpr (i == 1 && n >= 3) { af[s & 1][0] = lds_r128(a_); af[s & 1][1] = lds_r128(a_ + 4096); }
      if constexpr (i == 2) { wf[s & 1][1][0] = lds_tr(t_ + 32); wf[s & 1][1][1] = lds_tr(t_ + 32 + 768); }
      if constexpr (i == 3) { wf[s & 1][2][0] = lds_tr(t_ + 64); wf[s & 1][2][1] = lds_tr(t_ + 64 + 768); }
    };
    auto W = [&](int p, int k) { return (u32x4){wf[p][k][0][0], wf[p][k][0][1], wf[p][k][1][0], wf[p][k][1][1]}; };
    f32x2 st[2], sd[2]; float ek[2][3], csp[3]; u32x4 cf[3]; unsigned vs0 = 0;
    const unsigned csa_ = h ? M_T + 8192 + lane * 4 : M_CS + w * 384 + lane * 4;
    STAMP(22)
    wait_vm<0>(); wait_lgkm0(); bar();
    STAMP(2)
    sfor<0, 4>([&](auto I) { rd1(IC(0), IC(0), I); });
    sfor<0, 32>([&](auto K) { constexpr int n = K / 4, s = K % 4, nn = (K + 1) / 4, sn = (K + 1) % 4;
      sfor<0, 6>([&](auto I) { constexpr int i = I;
        acc[i >> 1][i & 1] = mfma(W(s & 1, i >> 1), n < 3 ? xk[n < 3 ? n : 0][s][i & 1] : af[s & 1][i & 1], K == 0 ? (f32x16){} : acc[i >> 1][i & 1]);
        sb();
        if constexpr (s == 3 && n < 7) { if constexpr (i == 0) { if constexpr (n >= 1) wait_vm<(n < 4 ? 6 : n < 6 ? 10 : 0)>(); wait_lgkm0(); STB(32 + 2 * n) bar(); STB(33 + 2 * n) } if constexpr (i >= 1 && i < 5) rd1(IC(nn), IC(sn), IC(i - 1)); }
        else if constexpr (K < 31 && i < 4) rd1(IC(nn), IC(sn), I);
        if constexpr (i >= 4 && K < 23) {
          constexpr int xn = (K + 1) / 4 + 2, kk = (K + 1) % 4, o0 = i == 5 ? (kk == 0 ? 0 : 3 * kk) : (kk == 0 ? 0 : 3 * kk - 1), o1 = i == 5 ? (kk == 3 ? 10 : 3 * kk + 2) : (kk == 0 ? 0 : 3 * kk);
          sfor<o0, o1>([&](auto O) { constexpr int o = O;
            if constexpr (o < 4) { if constexpr (xn > 5) { if constexpr (o == 0) setm0i<(xn % 3) * SLT>(w4); dmao<o * 1024>(sx, vx[o], so_x + chunk((xn + 2) & 7) * 128); } }
            else { constexpr int u = o - 4; if constexpr (u % 3 == 0) setm0i<(3 + xn % 3) * SLT + u * 1024>(ld_w);
              dmao<(u % 3) * 1024>(sw, vw[u % 3], so_w + chunk((xn + 2) & 7) * (64 * SF * 2) + (u / 3) * 16 * (SF * 2)); } }); }
        if constexpr (K == 24 && i == 4) { const unsigned ia = (64 * pi + 32 * ((lane >> 2) & 1) + 4 * ((lane >> 3) & 3) + (lane & 3)) * 8;
          for (int rb = 0; rb < 2; ++rb) { st[rb] = *(LAS f32x2*)(size_t)(M_ST + ia + rb * 128); sd[rb] = *(LAS f32x2*)(size_t)(M_SD + ia + rb * 128); } }
        if constexpr (K == 25 && i >= 4) { constexpr int rb = i - 4; const float rs = st[rb][0];
          ek[rb][0] = -2.302208198f * rs; ek[rb][1] = -0.1029432397f * rs * rs * rs; ek[rb][2] = sd[rb][0] * sd[rb][0]; pin(ek[rb][0]); pin(ek[rb][1]); pin(ek[rb][2]); }
        if constexpr (K == 28 && i == 4) { for (int k = 0; k < 3; ++k) csp[k] = *(LAS float*)(size_t)(M_CS + ((w ^ 2) * 3 + k) * 128 + l5 * 4); }
        if constexpr (K == 29 && i == 4) { vs0 = a2pb + lane * 16 + (64 + get_fb() + 3 * sb_) * 1024; pin(vs0); }
        if constexpr (K == 31 && i == 0) { wait_lgkm0();
          if (lane == 0) __hip_atomic_fetch_add((LAS unsigned*)(size_t)M_CNT, 1u, __ATOMIC_RELAXED, __HIP_MEMORY_SCOPE_WORKGROUP); }
        if constexpr (K == 31 && i >= 2 && i < 5) { constexpr int k = i - 2;
          const float cs = h ? 0.f : csg[k][0] + csp[k];
          const float c0 = rbf(cs), r1 = cs - c0, c1 = rbf(r1), c2 = r1 - c1;
          cf[k] = (u32x4){pk2(c0, c1), pk2(c2, 0.f), 0u, 0u}; pin(cf[k]); }
        sb();
      });
      if constexpr ((!(s & 1) || K == 29) && K < 31) { for (int k = 0; k < 3; ++k) csg[k] = mfma16(sel, W(s & 1, k), csg[k]); sb(); }
      if constexpr (K == 30) { for (int k = 0; k < 3; ++k) csg[k] = mfma16(sel, W(1, k), csg[k]); sb(); }
      if constexpr (K == 26) { *(LAS float*)(size_t)(csa_) = csg[0][0]; *(LAS float*)(size_t)(csa_ + 128) = csg[1][0]; *(LAS float*)(size_t)(csa_ + 256) = csg[2][0]; sb(); }
    });
    STAMP(8)
    for (int k = 0; k < 3; ++k) for (int rb = 0; rb < 2; ++rb) { const unsigned nm = __float_as_uint(sd[rb][1]); acc[k][rb] = mfma(cf[k], (u32x4){nm, nm, 0u, 0u}, acc[k][rb]); }
    STAMP(7)
    sfor<0, 3>([&](auto K) { constexpr int k = K;
      const unsigned vst = (gi < 7 || 3 * sb_ + k == 0) ? vs0 + k * 1024 : OOB;
      sfor<0, 2>([&](auto R_) { constexpr int rb = R_;
        const float k1 = ek[rb][0], k3 = ek[rb][1], s2 = ek[rb][2];
        unsigned o[4];
        for (int i = 0; i < 4; ++i) {
          const f32x2 gv = {acc[k][rb][2 * i], acc[k][rb][2 * i + 1]}, uv = {acc[k][rb][8 + 2 * i], acc[k][rb][9 + 2 * i]};
          const f32x2 z = gv * pfma(gv * gv, sp(k3), sp(k1));
          const f32x2 d = pfma((f32x2){__builtin_amdgcn_exp2f(z[0]), __builtin_amdgcn_exp2f(z[1])}, sp(s2), sp(s2));
          const f32x2 r = (gv * uv) * (f32x2){__builtin_amdgcn_rcpf(d[0]), __builtin_amdgcn_rcpf(d[1])};
          o[i] = pk2(r[0], r[1]); }
        __builtin_amdgcn_raw_buffer_store_b128((u32x4){o[0], o[1], o[2], o[3]}, rws, vst + rb * NKS * 1024, 0, 16);
      });
    });
    sb();
    STAMP(9)
    wait_vm<0>();
    set_flag(4 + w);
    STAMP(10)
    const int t = ftid(), ch = t & 15, r4 = t >> 4, ct = get_ct();
    const int trow = 64 * rsel + 32 * ((r4 >> 2) & 1) + 8 * (r4 & 1) + 2 * (r4 >> 3) + ((r4 >> 1) & 1);
    const int irow = 64 * rsel + 32 * ((r4 >> 2) & 1) + 4 * (r4 >> 3) + (r4 & 3);
    u32x2 xv[4], dk;
    for (int it = 0; it < 4; ++it) ld64(xv[it], sx, trow * 1024 + ch * 8, c * 131072 + ct * 128 + (4 * (it & 1) + 16 * (it >> 1)) * 1024);
    ld64(dk, mk_srd(dskip, SD * 2), ch * 8, ct * 128);
    wait_vm<0>(); for (int it = 0; it < 4; ++it) pin(xv[it]); pin(dk);
    f32x4 base[4]; unsigned ph[4];
    for (int it = 0; it < 4; ++it) { const unsigned io = (irow + 8 * (it & 1) + 16 * (it >> 1)) * 8;
      const float rs = *(LAS float*)(size_t)(M_ST + io), nm = lo2f(*(LAS unsigned*)(size_t)(M_SD + io + 4));
      for (int e = 0; e < 4; ++e) { const unsigned xu = xv[it][e >> 1], du = dk[e >> 1];
        const float xe = (e & 1) ? hi2f(xu) : lo2f(xu), de = (e & 1) ? hi2f(du) : lo2f(du);
        base[it][e] = xe + rbf(rbf(rbf(xe + nm) * rs) * de); }
      const int e = 2 * it + (r4 >> 3), d = (e * 11) >> 5;
      ph[it] = (3 + d) * SLT + (e - 3 * d) * 2048 + (r4 & 7) * 256 + (ch ^ (r4 & 7)) * 16;
      pin(base[it]); pin(ph[it]); }
    float* op = out + (c * 128 + trow) * SD + ct * 64 + ch * 4;
    const __amdgpu_buffer_rsrc_t rout = __builtin_amdgcn_make_buffer_rsrc(out, 0, SL * SD * 4, 0x00020000);
    const unsigned oo = ((c * 128 + trow) * SD + ct * 64 + ch * 4) * 4;
    bar();
    STAMP(11)
    for (int it = 0; it < 4; ++it) {
      f32x4 p[4];
      for (int ww = 0; ww < 4; ++ww) p[ww] = *(LAS f32x4*)(size_t)(ph[it] + ww * 6144);
      __builtin_amdgcn_raw_buffer_store_b128(__builtin_bit_cast(u32x4, base[it] + ((p[0] + p[1]) + (p[2] + p[3]))), rout, oo + (4 * (it & 1) + 16 * (it >> 1)) * SD * 4, 0, OAUX);
    }
    if (g == 0) __builtin_amdgcn_raw_buffer_store_b64((u32x2){(unsigned)tag2, (unsigned)(tag2 >> 32)}, rws, t == 0 ? WS_E2 + c * 128 : OOB, 0, 16);
  } else {
    const int lane = ftid() & 63, l5 = lane & 31, h = lane >> 5, blk = (lane >> 4) & 1, q = (lane & 15) >> 2, pp = lane & 3;
    const unsigned va = lane * 16;
    const unsigned sw0 = ((lane & 7) ^ (lane >> 4)) * 16;
    unsigned vb[2], vw[3], vd[2];
    for (int r = 0; r < 2; ++r) vb[r] = ((lane >> 3) + 7 * r) * 1024 + (sw0 ^ (r * 64));
    const i32x4 sbm = mk_srd((w >> 1) ? Bim : Bre, SP * SD * 2);
    const unsigned so_b = (g * 32 + (w & 1) * 16) * 1024;
    auto ldB = [&](auto S) { constexpr int s = S;
      const unsigned so = so_b + chunk(s) * 128;
      setm0i<((s + 1) % 6) * SLT + OFF_B>(w2); dmao<0>(sbm, vb[0], so); dmao<1024>(sbm, vb[1], so); };
    u32x2 ep2, ep1; unsigned plr, pli, pdt;
    ld64_sc1(ep2, sws, 0, WS_E2 + c * 128);
    ld64_sc1(ep1, sws, 0, WS_E1 + g * 128);
    const int ps = (lane & 3) | ((lane & 4) << 1) | ((lane & 8) >> 1);
    { const unsigned po = (g * 32 + sb_ * 16 + ps) * 2;
      ld16(plr, mk_srd(lam_re, SP * 2), po); ld16(pli, mk_srd(lam_im, SP * 2), po); ld16(pdt, mk_srd(logdt, SP * 2), po); }
    { u64 pc; unsigned len, back;
      asm volatile("s_getpc_b64 %0\n.Lpf_here:\n\ts_mov_b32 %1, .Lpf_end-.Lpf_beg\n\ts_mov_b32 %2, .Lpf_here-.Lpf_beg" : "=s"(pc), "=s"(len), "=s"(back));
      for (unsigned o = (idx * 4 + w) * 128; o + 128 < len; o += 16384) sline((pc - back) & ~127ull, o + 128); }
    const i32x4 sw = mk_srd((w >> 1) ? wu : wg, SD * SF * 2);
    const unsigned so_w = (w & 1) * 32 * (SF * 2) + get_fb() * 32;
    const unsigned ld_w = ((w >> 1) ? OFF_WU : 0) + (w & 1) * 6144;
    auto opW = [&](auto N, auto I) { constexpr int n = N, i = I;
      if constexpr (i % 3 == 0) setm0i<(3 + n % 3) * SLT + i * 1024>(ld_w);
      dmao<(i % 3) * 1024>(sw, vw[i % 3], so_w + chunk((n + 2) & 7) * (64 * SF * 2) + (i / 3) * 16 * (SF * 2)); };
    unsigned ra[4], wa[4];
    for (int s = 0; s < 4; ++s) { ra[s] = pi * 8192 + l5 * 128 + (((2 * s + h) ^ ((l5 >> 1) & 7)) * 16);
      const int row = sb_ * 16 + ps; wa[s] = OFF_B + blk * 4096 + row * 128 + (((2 * s + h) ^ ((row >> 1) & 7)) * 16); }
    const unsigned selv = (((lane & 15) == 0 && blk == 0) || ((lane & 15) == 4 && blk == 1)) ? 0x3f803f80u : 0u;
    const u32x4 sel = {selv, selv, selv, selv};
    for (int s = 0; s < 4; ++s) { pin(ra[s]); pin(wa[s]); }
    unsigned t1l = 0, t1h = 0;
    const int cc = 2 * c + pi;
    float ar, ai, nai, kr, nkr, nki, a8r, a8i, a16r, a16i, a32r, a32i, a64r, a64i, pwr = 1.f, pwi = 0.f, a256r, a256i, mr = 1.f, mi = 0.f;
    u32x4 oh[2];
    float lr, li;
    auto params = [&](auto V) { constexpr int v = V;
      if constexpr (v == 1) { const int q12 = (lane * 43) >> 9, r12 = lane - 12 * q12;
        for (int i = 0; i < 3; ++i) { const int r = r12 + 4 * i, cy = r >= 12, col = r - 12 * cy;
          vw[i] = (gi == 7 && col >= 2) ? OOB : (5 * i + q12 + cy) * (SF * 2) + col * 16 - i * 1024; pin(vw[i]); } }
      if constexpr (v == 2) {
        pin(ep2); pin(ep1); pin(plr); pin(pli); pin(pdt);
        tag2 = utag(ep2);
        const u64 tag1 = utag(ep1); t1l = (unsigned)tag1; t1h = (unsigned)(tag1 >> 32); }
      if constexpr (v == 3) {
        lr = lo2f(plr); li = lo2f(pli);
        const float dt = rbf(__expf(lo2f(pdt)));
        const float er = __expf(dt * lr);
        const float rev = dt * li * 0.15915494309189535f;
        const float fr = rev - __builtin_rintf(rev);
        ar = er * __builtin_amdgcn_cosf(fr); ai = er * __builtin_amdgcn_sinf(fr);
        pin(ar); pin(ai); }
      if constexpr (v == 4) { a8r = ar; a8i = ai;
        for (int i = 0; i < 3; ++i) csq(a8r, a8i, a8r, a8i);
        csq(a16r, a16i, a8r, a8i); pin(a16r); pin(a16i); }
      if constexpr (v == 6) {
        const float nr = ar - 1.f, ni = ai, den = 2.f * __builtin_amdgcn_rcpf(vfma(lr, lr, li * li));
        kr = vfma(nr, lr, ni * li) * den; nki = vfma(nr, li, -(ni * lr)) * den; nkr = -kr; nai = -ai;
        pin(nai); pin(kr); pin(nkr); pin(nki); }
      if constexpr (v == 10) { csq(a32r, a32i, a16r, a16i); csq(a64r, a64i, a32r, a32i); if (blk) { pwr = a16r; pwi = a16i; mr = a64r; mi = a64i; } }
      if constexpr (v == 11) { float tr = pwr, ti = pwi; cf4(tr, ti, a32r, a32i, 0.f, 0.f); if (h) { pwr = tr; pwi = ti; } csq(a32r, a32i, a64r, a64i); }
      if constexpr (v == 12) { float ur = mr, ui = mi; cf4(ur, ui, a32r, a32i, 0.f, 0.f); if (h) { mr = ur; mi = ui; } csq(a256r, a256i, a32r, a32i); }
      if constexpr (v == 13 || v == 14) { constexpr int u = v - 13;
        const unsigned val = (((lane >> 2) & 1) == h && blk == u) ? ((lane & 1) ? 0x3f800000u : 0x3f80u) : 0u; const int d0 = ((lane >> 1) & 1) | ((lane >> 2) & 2);
        for (int d = 0; d < 4; ++d) oh[u][d] = d0 == d ? val : 0u;
        pin(oh[u]); }
      sb();
    };
    f32x16 sa[2] = {};
    f32x4 csa = {};
    { u32x4 af[2][4][2], wf[2][4];
      auto rdA = [&](int p, int s, unsigned slot) { wf[p][s] = lds_r128(slot + wa[s]); af[p][s][0] = lds_r128(slot + ra[s]); af[p][s][1] = lds_r128(slot + ra[s] + 4096); };
      auto mmA = [&](int p, int s) { sa[0] = mfma(af[p][s][0], wf[p][s], sa[0]); sa[1] = mfma(af[p][s][1], wf[p][s], sa[1]); pin(sa[0]); pin(sa[1]); };
      auto csA = [&](int p) { for (int s = 0; s < 4; ++s) csa = mfma16(sel, wf[p][s], csa); pin(csa); sb(); };
      auto W3 = [&](auto N, auto H_) { sfor<0, 3>([&](auto I) { opW(N, IC(3 * H_ + I)); }); };
      unsigned rh[4], wh[4];
      for (int s = 0; s < 4; ++s) { rh[s] = ra[s] + 3 * SLT; wh[s] = wa[s] + 3 * SLT - 8192; pin(rh[s]); pin(wh[s]); wh[s] += 8192; wa[s] -= 8192; pin(wa[s]); wa[s] += 8192; }
      ldB(IC(2));
      __builtin_amdgcn_s_setprio(HPA);
      wait_vm<10>(); STA(46) bar(); STA(47) for (int s = 0; s < 4; ++s) rdA(0, s, SLT); sb(); ldB(IC(3));
      sfor<1, 8>([&](auto V) { constexpr int v = V, p = v & 1, sl = (v + 1) % 6;
        wait_vm<(v == 1 ? 9 : WEARLY ? (v < 4 ? 4 : v == 4 ? 7 : v == 5 ? 10 : v == 6 ? 11 : 6) : (v < 5 ? 4 : v == 5 ? 7 : v == 6 ? 11 : 6))>(); wait_lgkm0();
        if constexpr (v == 7) __builtin_amdgcn_s_setprio(3);
        STA(46 + 2 * v) bar(); STA(47 + 2 * v)
        sfor<0, 8>([&](auto I) { constexpr int i = I;
          sa[i & 1] = mfma(af[p ^ 1][i >> 1][i & 1], wf[p ^ 1][i >> 1], sa[i & 1]); pin(sa[i & 1]);
          sb();
          if constexpr (i < 6) sfor<2 * i, 2 * i + 2>([&](auto R) { constexpr int r = R, s = r / 3, k = r % 3, o = (sl % 3) * SLT;
            if constexpr (k == 0) wf[p][s] = lds_r128((sl < 3 ? wa[s] : wh[s]) + o); else af[p][s][k - 1] = lds_r128((sl < 3 ? ra[s] : rh[s]) + o + (k - 1) * 4096); });
          if constexpr (i == 6 && v < 5) ldB(IC(v + 3));
          if constexpr (i == 7 && v == 4) W3(IC(0), IC(0));
          if constexpr (i == 6 && v == 5) W3(IC(0), IC(1));
          if constexpr (i == 7 && v == 5) W3(IC(1), IC(0));
          if constexpr (i == 6 && v == 6) W3(IC(1), IC(1));
          sb(); });
        csA(p ^ 1);
        params(V);
      });
      STAMP(2)
      for (int s = 0; s < 4; ++s) mmA(1, s);
      csA(1);
      __builtin_amdgcn_s_setprio(HPB);
    }
    const int kb = 22 * w - (w == 3), cnt = w < 2 ? 22 : 21, ct = get_ct();
    const i32x4 swd = mk_srd(wd + kb * 8192, cnt * 16384);
    const unsigned so_c = ct * 65536 + w * 256;
    const unsigned r2 = (xcd * 11) >> 2;
    auto kofs = [&](int p, unsigned sc_) { const unsigned r = lnd(r2 * sc_), a = r + p * sc_, b2 = r + (p - 22) * sc_; return a < b2 ? a : b2; };
    for (int r = 0; r < 2; ++r) vd[r] = ((lane >> 3) + 7 * r) * 1024 + (((lane & 7) ^ (((lane >> 4) & 1) << 2)) * 16) + ct * 128;
    unsigned so_t = 0; i32x4 sc = swd;
    auto wd_op = [&](auto M, auto E, auto R) { constexpr int m = M, e = E, r = R;
      if constexpr (r == 0) { so_t = kofs(m < 18 ? (m + 14) % 18 : m, 16384); setm0i<cell_c(e)>(cell_k(e) == 0 ? w6 : cell_k(e) == 1 ? w4 : w2); }
      dmao<r * 1024>(swd, vd[r], so_t);
    };
    auto dma_wd = [&](auto M, auto E) { wd_op(M, E, IC(0)); wd_op(M, E, IC(1)); };
    auto c_op = [&](auto N, auto I) { constexpr int n = N, i = I;
      if constexpr (i == 0) { const int kg = ((n >> 1) + lnd(xcd)) & 3; so_t = so_c + (n & 1) * 32768 + (kg & 1) * 128; sc = mk_srd((kg >> 1) ? Cim : Cre, SD * SP * 2); }
      if constexpr (!(i & 1)) setm0i<(n & 1) ? ((i >> 1) ? M_T : cell_c(15)) : cell_c(16 + (i >> 1))>(w2);
      dmao<(i & 1) * 1024>(sc, vb[i & 1], so_t + (i >> 1) * 16384);
    };
    unsigned rcx[4];
    for (int s = 0; s < 4; ++s) rcx[s] = OFF_B + w * 2048 + blk * SLT + (lane & 15) * 128 + (((2 * s + h) ^ ((l5 >> 1) & 7)) * 16);
    const unsigned rcd = 2 * SLT + blk * (M_T - cell_c(15) - SLT);
    auto rd_c = [&](int n, u32x4* d) { for (int s = 0; s < 4; ++s) d[s] = lds_r128(rcx[s] + ((n & 1) ? rcd : 0u)); };
    const unsigned granb = WS_GRAN + (g * 2 + sb_) * 8192;
    const i32x4 sgr = mk_srd(ws + granb, 8192);
    const unsigned vg = (32 - cc) * 256 + va;
    float R[16], I_[16], csr, csi, er[2], ei[2], sr[4], si[4], prer, prei, cr, ci;
    f32x4 tb[8];
    u32x4 ge[8], fr4[4];
    f32x16 td[2];
    unsigned hp[2][8];
    unsigned nbad = 0, fo_g = 0, pa[2], tdl = 0;
    auto side = [&](auto P_) { constexpr int P = P_;
      if constexpr (P == 0) { for (int k = 0; k < 8; ++k) tb[k] = *(LAS f32x4*)(size_t)(M_ST + pi * 512 + (lane >> 4) * 128 + k * 16);
        csr = csa[0]; float t = csr; swap32(csr, t); csi = csr; swap16(csr, csi); }
      if constexpr (P >= 1 && P < 9) { constexpr int k = P - 1;
        for (int r = 0; r < 2; ++r) { R[2 * k + r] = sa[0][2 * k + r]; I_[2 * k + r] = sa[1][2 * k + r]; swap16(R[2 * k + r], I_[2 * k + r]); }
        for (int r = 0; r < 2; ++r) sc4(R[2 * k + r], I_[2 * k + r], tb[k][2 * r], tb[k][2 * r + 1], csr, csi); }
      if constexpr (P == 9) { for (int r = 0; r < 2; ++r) { er[r] = R[r]; ei[r] = I_[r]; cf4(er[r], ei[r], ar, ai, R[2 + r], I_[2 + r]); } }
      if constexpr (P >= 16 && P < 22) { constexpr int k = P - 14; for (int r = 0; r < 2; ++r) cf4(er[r], ei[r], ar, ai, R[2 * k + r], I_[2 * k + r]); }
      if constexpr (P == 22) { sr[0] = er[0]; si[0] = ei[0]; cf4(sr[0], si[0], a8r, a8i, er[1], ei[1]); sr[1] = sr[0]; swap16(sr[0], sr[1]); si[1] = si[0]; swap16(si[0], si[1]); }
      if constexpr (P == 23) { sr[2] = sr[0]; swap32(sr[0], sr[2]); si[2] = si[0]; swap32(si[0], si[2]); sr[3] = sr[1]; swap32(sr[1], sr[3]); si[3] = si[1]; swap32(si[1], si[3]); }
      if constexpr (P == 24) { cr = sr[0]; ci = si[0]; cf4(cr, ci, a16r, a16i, sr[1], si[1]); sr[1] = cr; si[1] = ci; cf4(cr, ci, a16r, a16i, sr[2], si[2]); sr[2] = cr; si[2] = ci; }
      if constexpr (P == PPUB) { cf4(cr, ci, a16r, a16i, sr[3], si[3]);
        __builtin_amdgcn_raw_buffer_store_b128((u32x4){__float_as_uint(cr), t1l, __float_as_uint(ci), t1h}, rws, lane < 16 ? granb + (31 - cc) * 256 + va : OOB, 0, 16); }
      if constexpr (P == 32) { prer = h ? (blk ? sr[2] : sr[1]) : (blk ? sr[0] : 0.f); prei = h ? (blk ? si[2] : si[1]) : (blk ? si[0] : 0.f); }
      sfor<0, 16>([&](auto K) { constexpr int k = K; if constexpr (P == nxt(33, k < 4 ? k / 2 : k - 2)) kc4(R[k], I_[k], kr, nkr, nki); });
      if constexpr (P >= 53 && P < 58) params(IC(P - 43));
      if constexpr (P == PG) sfor<0, 8>([&](auto K) { constexpr int k = K; ld128_sc1<(k & 3) * 1024>(ge[k], sgr, vg, (k >> 2) * 4096); });
      if constexpr (P == PC) { STB(23) wait_vm<hb_cnt(PC, false) - hb_cnt(PG, true)>(); for (int i = 0; i < 8; ++i) pin(ge[i]); STB(24) }
      if constexpr (P == PC + 3) STB(25) if constexpr (P == PC + 6) STB(26) if constexpr (P == PC + 9) STB(27)
      if constexpr (P == PC || P == PC + 1) { constexpr int i = 4 * (P - PC); miss2(nbad, ge[i], ge[i + 1], t1l, t1h); miss2(nbad, ge[i + 2], ge[i + 3], t1l, t1h); }
      if constexpr (P == PC + 2) { const unsigned nout = 8 - ((cc + 3 - (lane >> 4)) >> 2);
#pragma unroll 1
        for (unsigned spins = 0; spins < SPIN_MAX; ++spins) {
          if (__all(nbad == nout)) break;
          STAMP(28)
          __builtin_amdgcn_s_sleep(SLP);
          sfor<0, 8>([&](auto I) { constexpr int i = I; ld128_sc1_rw<(i & 3) * 1024>(ge[i], sgr, vg, (i >> 2) * 4096); });
          wait_vm<0>(); for (int i = 0; i < 8; ++i) pin(ge[i]);
          nbad = 0;
          for (int i = 0; i < 8; i += 2) miss2(nbad, ge[i], ge[i + 1], t1l, t1h);
        }
        cr = __uint_as_float(ge[7][0]); ci = __uint_as_float(ge[7][2]); cf4(cr, ci, a256r, a256i, __uint_as_float(ge[6][0]), __uint_as_float(ge[6][2])); }
      if constexpr (P >= PC + 3 && P < PC + 6) { constexpr int i = 5 - 2 * (P - PC - 3);
        cf4(cr, ci, a256r, a256i, __uint_as_float(ge[i][0]), __uint_as_float(ge[i][2])); cf4(cr, ci, a256r, a256i, __uint_as_float(ge[i - 1][0]), __uint_as_float(ge[i - 1][2])); }
      if constexpr (P == PC + 6) { cf4(cr, ci, mr, mi, 0.f, 0.f); float t = cr, u = ci; swap16(cr, t); swap16(ci, u); cr = vadd(cr, t); ci = vadd(ci, u); }
      if constexpr (P == PC + 7) { float t = cr, u = ci; swap32(cr, t); swap32(ci, u); cr = vadd(cr, t); ci = vadd(ci, u); cf4(cr, ci, pwr, pwi, prer, prei); }
      if constexpr (P == PC + 8) { sr[0] = cr; si[0] = ci; cf4(cr, ci, a8r, a8i, er[0], ei[0]); er[0] = sr[0]; ei[0] = si[0]; er[1] = cr; ei[1] = ci; }
      if constexpr (P == PC + 9) { kc4(er[0], ei[0], kr, nkr, nki); kc4(er[1], ei[1], kr, nkr, nki); }
      if constexpr (P % 16 == 4 && P / 16 >= PFW_S && P / 16 < 6) sfor<0, PFW>([&](auto U) { constexpr int k = (P / 16 - PFW_S) * PFW + U + PFW_O, m = k < 4 ? 18 + k : k < 18 ? k : k - 18;
        if constexpr (k < 22) { const unsigned o = kofs(m < 18 ? (m + 14) % 18 : m, 16384); if (o < (unsigned)cnt * 16384) for (int r = 0; r < 4; ++r) sline((u64)(wd + kb * 8192), o + ct * 128 + (4 * (c & 3) + r) * 1024); } });
      sfor<0, 8>([&](auto K) { constexpr int k = K; if constexpr (P == nxt(P2, k)) {
        for (int r = 0; r < 2; ++r) cf4c(k ? R[2 * k - 2 + r] : er[r], k ? I_[2 * k - 2 + r] : ei[r], ar, ai, nai, R[2 * k + r], I_[2 * k + r]);
        hp[0][k] = pk2(R[2 * k], R[2 * k + 1]); hp[1][k] = pk2(I_[2 * k], I_[2 * k + 1]); } });
      static_assert(PT > nxt(P2, 7) && PS > PT && PF > PS && PC % 16 == 0, "scan schedule");
      if constexpr (P == PT) {
        __builtin_amdgcn_s_setprio(3);
        for (int part = 0; part < 2; ++part) {
          td[part] = mfma((u32x4){hp[part][0], hp[part][1], hp[part][2], hp[part][3]}, oh[0], (f32x16){});
          td[part] = mfma((u32x4){hp[part][4], hp[part][5], hp[part][6], hp[part][7]}, oh[1], td[part]); pin(td[part]); }
        __builtin_amdgcn_s_setprio(HPB); }
      if constexpr (P == PS) { for (int k = 0; k < 4; ++k) for (int d = 0; d < 4; ++d) fr4[k][d] = pk2(td[k >> 1][8 * (k & 1) + 2 * d], td[k >> 1][8 * (k & 1) + 2 * d + 1]); }
      if constexpr (P == PS) { for (int k = 0; k < 4; ++k)
          __builtin_amdgcn_raw_buffer_store_b128(fr4[k], rws, a2pb + (2 * g + sb_) * 1024 + va + ((k & 1) * NKS + (k >> 1) * 32) * 1024, 0, 16); }
      if constexpr (P == 100) { const int ks = kb + lane, p6 = (ks * 43) >> 8, pg = ks < 84 ? p6 + (p6 >= 7) : 7 + 8 * (ks - 84), pb = ks < 84 ? ks - 6 * p6 : 0;
        fo_g = lane < cnt ? (pg * 8 + 4 + 2 * rsel + (pb >= 3)) * 128 : OOB; pin(fo_g); }
      if constexpr (P == 104) { for (int rb = 0; rb < 2; ++rb) { const int e = 4 * rb + (l5 >> 3), d = (e * 11) >> 5;
          pa[rb] = (3 + d) * SLT + (e - 3 * d) * 2048 + w6 + (lane & 7) * 256 + ((h ^ (lane & 7)) * 16); pin(pa[rb]); } }
      if constexpr (P == 106) { tdl = (8 * h + q) * 128 + (((blk * 2 + (pp >> 1)) ^ ((q >> 1) << 2)) * 16) + (pp & 1) * 8; pin(tdl); }
      if constexpr (P == PF) { wait_vm<hb_cnt(PF, false) - hb_cnt(PS, true)>(); set_flag(w);
        if (cc == 31) __builtin_amdgcn_raw_buffer_store_b64((u32x2){t1l, t1h}, rws, (lane == 0 && sb_ == 0) ? WS_E1 + g * 128 : OOB, 0, 16); }
    };
    u32x4 cq[8][4];
    u32x2 fs;
    const unsigned fo_s = lane < 8 ? ((4 * w + (lane >> 1)) * 8 + 2 * rsel + (lane & 1)) * 128 : OOB;
    sfor<0, 8>([&](auto N) { constexpr int n = N, g0 = hb_g0(n), done = n ? hb_cnt(16 * n - 1, true) : 0;
      if constexpr (n > 0) { if constexpr (n == 1) wait_vm<done>(); STB(30 + 2 * n) bar(); STB(31 + 2 * n) } else STB(31)
      if constexpr (n > 1) { wait_vm<done - hb_cnt(16 * (n > 1 ? n - 2 : 0) + hb_g0(n - 2) + 3, false)>(); rd_c(n, cq[n - 2]); sb(); }
      sfor<0, hb_ops(n)>([&](auto I) { constexpr int i = I, io = i < g0 ? i : i - 4;
        if constexpr (i >= g0 && i < g0 + 4) { if constexpr (i == g0 && n > 1) { for (int s = 0; s < 4; ++s) use(cq[n - 2][s]); } c_op(N, IC(i - g0)); }
        else if constexpr (n >= 6) { constexpr int e = 5 * (n - 6) + (io >> 1); wd_op(IC(e < 4 ? 18 + e : e), IC(e), IC(io & 1)); }
        if constexpr (16 * n + i == PPOLL) ld64_sc1(fs, sws, fo_s, flagb);
        side(IC(16 * n + i));
        sb();
      });
    });
    __builtin_amdgcn_s_setprio(HPC);
    STAMP(3)
    auto poll = [&](unsigned fo, u32x2 fv) {
#pragma unroll 1
      for (unsigned spins = 0; spins < SPIN_MAX; ++spins) {
        const bool ok = fo == OOB || (fv[0] == (unsigned)tag2 && fv[1] == (unsigned)(tag2 >> 32));
        if (__all(ok)) break;
        __builtin_amdgcn_s_sleep(SLP);
        ld64_sc1(fv, sws, fo, flagb); wait_vm<0>(); pin(fv);
      }
    };
    const unsigned a2c = WS_A2 + (unsigned)(c * 4 + 2 * rsel) * (NKS * 1024);
    const i32x4 sg0 = mk_srd(ws + a2c + (64 + kb) * 1024, cnt * 1024), sg1 = mk_srd(ws + a2c + (NKS + 64 + kb) * 1024, cnt * 1024);
    const unsigned vs0 = va + a2c + w * 8192, vs1 = vs0 + NKS * 1024;
    wait_vm<hb_cnt(127, true) - hb_cnt(PPOLL, true)>(); pin(fs);
    rd_c(0, cq[6]);
    STAMP(7)
    poll(fo_s, fs);
    STAMP(4)
    unsigned tb2[4][2];
    auto rd_half = [&](int e, int cb) { const int k = cell_k(e) == 0 ? (cell_c(e) >= 5 * SLT) : cell_k(e) + 1;
      return lds_tr2(tb2[k][cb] + (cell_c(e) - (k == 0 ? 3 * SLT : k == 1 ? 5 * SLT : k == 3 ? OFF_B : 0)), 512); };
    f32x16 acc2[2][2];
    u32x4 L[76], wq[4][2], bf[2][2];
    unsigned so_l = 0;
    auto soL = [&](auto M) { constexpr int m = M;
      if constexpr (m < 16) { if constexpr (!(m & 3)) { const int kg = ((m >> 2) + lnd(xcd)) & 3; so_l = ((kg >> 1) * 32 + (kg & 1) * 4) * 1024; } }
      else so_l = kofs(m - 16, 1024); };
    auto ldH = [&](auto M, auto R) { constexpr int m = M, r = R;
      if constexpr (m < 16) ld128_sc1<(m & 3) * 1024>(L[2 * m + r], sws, r ? vs1 : vs0, so_l); else ld128_sc1<0>(L[2 * m + r], r ? sg1 : sg0, va, so_l); };
    auto ldL = [&](auto M) { soL(M); ldH(M, IC(0)); ldH(M, IC(1)); };
    sfor<0, p2_l0(0)>(ldL);
    wait_lgkm0();
    dma_wd(IC(16), IC(16)); dma_wd(IC(17), IC(17));
    wait_cnt();
    sfor<10, 13>([&](auto E) { dma_wd(E, E); });
    sfor<0, 34>([&](auto M) { constexpr int m = M, mm = m - 16;
      constexpr bool one = p2_l0(m + 1) == p2_l0(m) + 1;
      wait_vm<p2_after(m)>(); pin(L[2 * m]); pin(L[2 * m + 1]);
      sfor<0, 4>([&](auto G) { constexpr int gp = G, cb = gp >> 1, rb = gp & 1;
        if constexpr (m < 16) acc2[cb][rb] = mfma(cq[2 * (m >> 2) + cb][m & 3], L[2 * m + rb], m == 0 ? (f32x16){} : acc2[cb][rb]);
        else acc2[cb][rb] = mfma(mm < 18 ? bf[mm & 1][cb] : wq[mm < 18 ? 0 : mm - 18][cb], L[2 * m + rb], acc2[cb][rb]);
        sb();
        if constexpr (gp < 2) {
          if constexpr (m >= 15 && mm + 1 < 18) bf[(mm + 1) & 1][gp] = rd_half((mm + 5) % 18, gp);
          if constexpr (m == 0 || m == 1) wd_op(IC(13 + m), IC(13 + m), G);
          if constexpr (m == 2 || m == 3) { constexpr int k = 2 * (m - 2) + gp;
            for (int u = 0; u < 2; ++u) { tb2[k][u] = (tdl ^ (u * 64)) + (k == 0 ? 3 * SLT + w6 : k == 1 ? 5 * SLT + w6 : k == 2 ? w4 : OFF_B + w2); pin(tb2[k][u]); } }
          if constexpr (m == 4 && gp == 0) rd_c(1, cq[7]);
          if constexpr (m == 5) { if constexpr (gp == 0) { for (int s = 0; s < 4; ++s) use(cq[7][s]); } wd_op(IC(15), IC(15), G); }
          if constexpr (m >= 7 && m <= 10) { if constexpr (gp == 0) { use(wq[m - 7][0]); use(wq[m - 7][1]); } wd_op(IC(m - 7), IC(m - 7), G); }
          if constexpr (m >= 6 && m <= 9) wq[m - 6][gp] = rd_half(m - 6, gp);
          if constexpr (gp == 1 && m == PSM) ld64_sc1(fs, sws, fo_g, flagb);
          if constexpr (gp == 1 && one) soL(IC(p2_l0(m)));
        } else {
          if constexpr (gp == 2 && m == PCK) { STAMP(5) wait_vm<p2_exsum(PSM + 1, PCK) + 2 * (16 - p2_l0(PSM))>(); pin(fs); poll(fo_g, fs); STAMP(6) }
          if constexpr (one) ldH(IC(p2_l0(m)), IC(gp - 2));
          else if constexpr (gp == 2) sfor<p2_l0(m), p2_l0(m + 1)>(ldL);
        }
        sb();
      });
    });
    sfor<0, 5>([&](auto B_) { constexpr int bk = B_, cb = bk >> 1, rb = bk & 1, pc = (bk - 1) >> 1, pr = (bk - 1) & 1;
      sfor<0, 4>([&](auto U) { constexpr int u = U, m = 34 + u;
        if constexpr (bk == 0) { wait_vm<6 - 2 * u>(); pin(L[2 * m]); pin(L[2 * m + 1]); }
        if constexpr (bk < 4) { acc2[cb][rb] = mfma(wq[u][cb], L[2 * m + rb], acc2[cb][rb]); pin(acc2[cb][rb]); sb(); }
        if constexpr (bk > 0 && (u > 0 || bk == 4)) sfor<(u == 1 && bk < 4) ? 0 : u, u + 1>([&](auto R) { constexpr int rq = R;
          *(LAS f32x4*)(size_t)((pa[pr] ^ (rq * 32)) + pc * 128) = (f32x4){acc2[pc][pr][4 * rq], acc2[pc][pr][4 * rq + 1], acc2[pc][pr][4 * rq + 2], acc2[pc][pr][4 * rq + 3]}; });
        sb();
      });
      if constexpr (bk == 3) STAMP(9)
    });
    wait_lgkm0(); bar();
    STAMP(11)
  }
  STAMP(12)
  STAMP_OUT
  asm volatile(".Lpf_end:");
}

extern "C" void kernel_launch(void* const* d_in, const int* in_sizes, int n_in, void* d_out, int out_size, void* d_ws, size_t ws_size,
                              hipStream_t stream) {
  (void)hipFuncSetAttribute((const void*)fused, hipFuncAttributeMaxDynamicSharedMemorySize, LDS_BYTES);
  fused<<<256, 512, LDS_BYTES, stream>>>((const u16*)d_in[0], (const u16*)d_in[1], (const u16*)d_in[2], (const u16*)d_in[3], (const u16*)d_in[4],
                                         (const u16*)d_in[5], (const u16*)d_in[6], (const u16*)d_in[7], (const u16*)d_in[8], (const u16*)d_in[10],
                                         (const u16*)d_in[11], (const u16*)d_in[12], (char*)d_ws, (float*)d_out);
}
```

```cpp
#include <hip/hip_runtime.h>
#include <hip/hip_bf16.h>
#include <type_traits>

typedef unsigned short u16;
typedef unsigned long long u64;
typedef short s16x4 __attribute__((ext_vector_type(4)));
typedef __bf16 bf16x2 __attribute__((ext_vector_type(2)));
typedef __bf16 bf16x8 __attribute__((ext_vector_type(8)));
typedef float f32x2 __attribute__((ext_vector_type(2)));
typedef float f32x4 __attribute__((ext_vector_type(4)));
typedef float f32x16 __attribute__((ext_vector_type(16)));
typedef unsigned u32x2 __attribute__((ext_vector_type(2)));
typedef unsigned u32x4 __attribute__((ext_vector_type(4)));
typedef int i32x4 __attribute__((ext_vector_type(4)));
#define LAS __attribute__((address_space(3)))
#define DEVI __device__ __forceinline__

constexpr int SL = 2048, SD = 512, SP = 512, SF = 1376;
constexpr int NKS = 150;
constexpr unsigned WS_E1 = 0;
constexpr unsigned WS_E2 = 2048;
constexpr unsigned WS_GRAN = 262144;
constexpr unsigned WS_FLAG = 524288;
constexpr unsigned WS_A2 = 786432;
constexpr unsigned A2_BYTES = 64u * NKS * 1024u;
constexpr unsigned WS_BYTES = WS_A2 + A2_BYTES;
constexpr unsigned OOB = 0x40000000u;
constexpr int SLT = 24832;
constexpr int OFF_B = 16384, OFF_WU = 12288 + 32;
constexpr int MISC = 6 * SLT;
constexpr int M_ST = MISC, M_SD = MISC + 1024, M_CNT = MISC + 2048, M_CS = MISC + 2304, M_T = MISC + 3840;
constexpr int LDS_BYTES = 163840;
constexpr unsigned SPIN_MAX = 1u << 16;
constexpr int cell_k(int e) { return e >= 15 ? 2 : (e % 5) < 3 ? 0 : 1; }
constexpr int cell_c(int e) { return e >= 15 ? ((e - 13) % 3) * SLT + OFF_B : (e % 5) < 3 ? (3 + (e / 5 + 2) % 3) * SLT + (e % 5) * 2048 : ((e / 5 + 2) % 3) * SLT + (e % 5 - 3) * 2048; }

template <int B, int E, class Fn> DEVI void sfor(Fn&& f) {
  if constexpr (B < E) { f(std::integral_constant<int, B>{}); sfor<B + 1, E>(f); }
}
DEVI i32x4 mk_srd(const void* p, unsigned bytes) {
  u64 a = (u64)p; return (i32x4){(int)(unsigned)a, (int)((unsigned)(a >> 32) & 0xffffu), (int)bytes, 0x00020000};
}
template <int OFF> DEVI void ld128_sc1(u32x4& d, i32x4 srd, unsigned voff, unsigned soff) {
  asm volatile("buffer_load_dwordx4 %0, %1, %2, %3 offen offset:%4 sc1" : "=v"(d) : "v"(voff), "s"(srd), "s"(soff), "n"(OFF) : "memory");
}
template <int OFF> DEVI void ld128_sc1_rw(u32x4& d, i32x4 srd, unsigned voff, unsigned soff) {
  asm volatile("buffer_load_dwordx4 %0, %1, %2, %3 offen offset:%4 sc1" : "+v"(d) : "v"(voff), "s"(srd), "s"(soff), "n"(OFF) : "memory");
}
DEVI void ld64_sc1(u32x2& d, i32x4 srd, unsigned voff, unsigned soff) {
  asm volatile("buffer_load_dwordx2 %0, %1, %2, %3 offen sc1" : "=v"(d) : "v"(voff), "s"(srd), "s"(soff) : "memory");
}
DEVI void ld64(u32x2& d, i32x4 srd, unsigned voff, unsigned soff) {
  asm volatile("buffer_load_dwordx2 %0, %1, %2, %3 offen" : "=v"(d) : "v"(voff), "s"(srd), "s"(soff) : "memory");
}
DEVI void ld16(unsigned& d, i32x4 srd, unsigned voff) {
  asm volatile("buffer_load_ushort %0, %1, %2, 0 offen" : "=v"(d) : "v"(voff), "s"(srd) : "memory");
}
template <int N> DEVI void wait_vm() { asm volatile("s_waitcnt vmcnt(%0)" ::"n"(N) : "memory"); }
template <class T> DEVI void pin(T& a) { asm volatile("" : "+v"(a)::"memory"); }
template <class T> DEVI void use(const T& a) { asm volatile("" ::"v"(a) : "memory"); }
DEVI int lnd(int a) { asm volatile("" : "+s"(a)); return a; }
DEVI int ftid() { int t = threadIdx.x; asm volatile("" : "+v"(t)); return t; }
DEVI void wait_lgkm0() { asm volatile("s_waitcnt lgkmcnt(0)" ::: "memory"); }
DEVI void bar() { asm volatile("s_barrier" ::: "memory"); }
DEVI void sb() { __builtin_amdgcn_sched_barrier(0); }
DEVI float lo2f(unsigned u) { return __uint_as_float(u << 16); }
DEVI float hi2f(unsigned u) { return __uint_as_float(u & 0xffff0000u); }
DEVI unsigned pk2(float a, float b) { f32x2 v = {a, b}; return __builtin_bit_cast(unsigned, __builtin_convertvector(v, bf16x2)); }
DEVI float rbf(float a) { return lo2f(pk2(a, a)); }
DEVI u32x4 lds_r128(unsigned a) { return *(LAS u32x4*)(size_t)a; }
DEVI u32x2 lds_tr(unsigned a) { return __builtin_bit_cast(u32x2, __builtin_amdgcn_ds_read_tr16_b64_v4i16((LAS s16x4*)(size_t)a)); }
DEVI u32x4 lds_tr2(unsigned a, unsigned d) { u32x2 x = lds_tr(a), y = lds_tr(a + d); return (u32x4){x[0], x[1], y[0], y[1]}; }
DEVI f32x16 mfma(u32x4 a, u32x4 b, f32x16 c) {
  return __builtin_amdgcn_mfma_f32_32x32x16_bf16(__builtin_bit_cast(bf16x8, a), __builtin_bit_cast(bf16x8, b), c, 0, 0, 0);
}
DEVI float dot2(unsigned a, unsigned b, float c) {
  return __builtin_amdgcn_fdot2_f32_bf16(__builtin_bit_cast(bf16x2, a), __builtin_bit_cast(bf16x2, b), c, false);
}
DEVI void swap16(float& a, float& b) { auto r = __builtin_amdgcn_permlane16_swap(__float_as_uint(a), __float_as_uint(b), false, false); a = __uint_as_float(r[0]); b = __uint_as_float(r[1]); }
DEVI void swap32(float& a, float& b) { auto r = __builtin_amdgcn_permlane32_swap(__float_as_uint(a), __float_as_uint(b), false, false); a = __uint_as_float(r[0]); b = __uint_as_float(r[1]); }
template <int OFF> DEVI void dmao(i32x4 srd, unsigned voff, unsigned soff) {
  asm volatile("buffer_load_dwordx4 %0, %1, %2 offen offset:%3 lds" ::"v"(voff), "s"(srd), "s"(soff), "n"(OFF) : "memory");
}
DEVI f32x4 mfma16(u32x4 a, u32x4 b, f32x4 c) {
  return __builtin_amdgcn_mfma_f32_16x16x32_bf16(__builtin_bit_cast(bf16x8, a), __builtin_bit_cast(bf16x8, b), c, 0, 0, 0);
}
DEVI float vadd(float a, float b) { float r; asm("v_add_f32 %0, %1, %2" : "=v"(r) : "v"(a), "v"(b)); return r; }
DEVI float vfma(float a, float b, float c) { float r; asm("v_fma_f32 %0, %1, %2, %3" : "=v"(r) : "v"(a), "v"(b), "v"(c)); return r; }
DEVI f32x2 pfma(f32x2 a, f32x2 b, f32x2 c) { return __builtin_elementwise_fma(a, b, c); }
DEVI f32x2 sp(float a) { return (f32x2){a, a}; }
#define IC(x) std::integral_constant<int, (x)>{}
constexpr int SLP = 4;
constexpr int WN = 10, WG = 6;
constexpr int PCK = 11, PSM = 8;
constexpr int p2_extra(int m) { return ((m == 0 || m == 1 || m == 5 || (m >= 7 && m <= 10)) ? 2 : 0) + (m == PSM ? 1 : 0); }
constexpr int p2_exsum(int a, int b) { int t = 0; for (int m = a; m <= b; ++m) t += p2_extra(m); return t; }
constexpr int p2_l0(int m) { return m <= PCK ? (m + WN / 2 < 16 ? m + WN / 2 : 16) : (m + WG < 38 ? m + WG : 38); }
constexpr int p2_after(int m) {
  int pos = 0, posL[40] = {};
  for (int k = 0; k < p2_l0(0); ++k) { pos += 2; posL[k] = pos; }
  pos += 10;
  for (int s = 0; s < m; ++s) { pos += p2_extra(s); for (int k = p2_l0(s); k < p2_l0(s + 1); ++k) { pos += 2; posL[k] = pos; } }
  return pos - posL[m];
}
constexpr int PPUB = 25, PG = 50, PC = 64, P2 = 80, PT = 88, PS = 89, PF = 100, PPOLL = 120;
constexpr int hb_ops(int n) { return n < 6 ? 10 : 14; }
constexpr int hb_g0(int n) { return n < 6 ? 3 : 4; }
constexpr int hb_extra(int P) { return P == PPUB ? 1 : P == PG ? 8 : P == PS ? 4 : P == PF ? 1 : P == PPOLL ? 1 : 0; }
constexpr int hb_cnt(int P, bool with_extra) {
  int t = 0;
  for (int n = 0; n < 8; ++n) for (int i = 0; i < hb_ops(n); ++i) { const int p = 16 * n + i; if (p > P) return t; t += (n >= 6 || (i >= hb_g0(n) && i < hb_g0(n) + 4)); if (p < P || with_extra) t += hb_extra(p); }
  return t;
}
constexpr int nxt(int P, int d) { for (; d > 0; --d) { ++P; if (P % 16 >= hb_ops(P / 16)) P = (P / 16 + 1) * 16; } return P; }
DEVI u64 utag(u32x2 e) { const u64 t = (((u64)(unsigned)__builtin_amdgcn_readfirstlane(e[1]) << 32) | (unsigned)__builtin_amdgcn_readfirstlane(e[0])) + 1; return t ? t : 1; }
template <int CTRL> DEVI float dppf(float v) { return __int_as_float(__builtin_amdgcn_update_dpp(0, __float_as_int(v), CTRL, 0xf, 0xf, true)); }
DEVI void sline(u64 base, unsigned off) {
  asm volatile("s_load_dwordx2 s[98:99], %0, %1" ::"s"(base), "s"(off + 60) : "memory", "s98", "s99");
}
template <int IMM> DEVI void setm0i(unsigned base) { asm volatile("s_add_i32 m0, %0, %1\n\ts_nop 0" ::"s"(base), "n"(IMM) : "memory", "scc"); }
DEVI void miss2(unsigned& n, u32x4 a, u32x4 b, unsigned tl, unsigned th) {
  unsigned t, u;
  asm("v_xor_b32 %1, %7, %3\n\tv_xor_b32 %2, %7, %5\n\tv_bitop3_b32 %1, %4, %8, %1 bitop3:0xbe\n\tv_bitop3_b32 %2, %6, %8, %2 bitop3:0xbe\n\t"
      "v_min_u32 %1, 1, %1\n\tv_min_u32 %2, 1, %2\n\tv_add3_u32 %0, %0, %1, %2"
      : "+v"(n), "=&v"(t), "=&v"(u) : "v"(a[1]), "v"(a[3]), "v"(b[1]), "v"(b[3]), "s"(tl), "s"(th));
}
DEVI void cf4(float& hr, float& hi, float ar, float ai, float br, float bi) {
  float t, u;
  asm("v_fma_f32 %0, -%5, %3, %6\n\tv_fma_f32 %1, %5, %2, %7\n\tv_fmac_f32 %0, %4, %2\n\tv_fmac_f32 %1, %4, %3"
      : "=&v"(t), "=&v"(u) : "v"(hr), "v"(hi), "v"(ar), "v"(ai), "v"(br), "v"(bi));
  hr = t; hi = u;
}
DEVI void csq(float& dr, float& di, float r, float i) {
  float t, u;
  asm("v_mul_f32 %0, %2, %2\n\tv_mul_f32 %1, %2, %3\n\tv_fma_f32 %0, -%3, %3, %0\n\tv_add_f32 %1, %1, %1" : "=&v"(t), "=&v"(u) : "v"(r), "v"(i));
  dr = t; di = u;
}
DEVI void cf4c(float hr, float hi, float ar, float ai, float nai, float& br, float& bi) {
  asm("v_fmac_f32 %0, %5, %3\n\tv_fmac_f32 %1, %6, %2\n\tv_fmac_f32 %0, %4, %2\n\tv_fmac_f32 %1, %4, %3"
      : "+v"(br), "+v"(bi) : "v"(hr), "v"(hi), "v"(ar), "v"(ai), "v"(nai));
}
DEVI void sc4(float& r, float& i, float rs, float rc, float csr, float csi) {
  asm("v_mul_f32 %0, %2, %0\n\tv_mul_f32 %1, %2, %1\n\tv_fmac_f32 %0, %3, %4\n\tv_fmac_f32 %1, %3, %5"
      : "+v"(r), "+v"(i) : "v"(rs), "v"(rc), "v"(csr), "v"(csi));
}
DEVI void kc4(float& r, float& i, float kr, float nkr, float nki) {
  float t, u;
  asm("v_mul_f32 %0, %4, %2\n\tv_mul_f32 %1, %5, %3\n\tv_fmac_f32 %0, %6, %3\n\tv_fmac_f32 %1, %6, %2"
      : "=&v"(t), "=&v"(u) : "v"(r), "v"(i), "v"(kr), "v"(nkr), "v"(nki));
  r = t; i = u;
}

__global__ __launch_bounds__(512, 2) void fused(const u16* __restrict__ x, const u16* __restrict__ lam_re, const u16* __restrict__ lam_im,
                                                const u16* __restrict__ Bre, const u16* __restrict__ Bim, const u16* __restrict__ Cre,
                                                const u16* __restrict__ Cim, const u16* __restrict__ logdt, const u16* __restrict__ dskip,
                                                const u16* __restrict__ wg, const u16* __restrict__ wu, const u16* __restrict__ wd,
                                                char* __restrict__ ws, float* __restrict__ out) {
  extern __shared__ __attribute__((aligned(16))) char shm[];
  asm volatile(".Lpf_beg:");
  unsigned perm;
  const i32x4 sx = mk_srd(x, SL * SD * 2);
  {
    const int b0 = blockIdx.x, t = threadIdx.x, lead = ((b0 >> 6) + (b0 >> 3)) & 3, rot = (b0 * 5) & 6;
    perm = (0x76543210u - (15u << (4 * lead)) * (1u + ((((lead + 1) >> 2) ^ 1) << 16)) + rot * 0x11111111u) & 0x77777777u;
    const unsigned v0 = (((t >> 2) & 64) + ((t >> 3) & 16) + ((t >> 4) & 5) + (t & 40)) * 1024 + (((t & 7) ^ ((t >> 4) & 3)) * 16);
    const unsigned so = (((b0 & 6) * 2 + (b0 >> 6)) << 17) + (perm & 7) * 128;
    asm volatile("" ::"s"(lam_re), "s"(lam_im), "s"(Bre), "s"(Bim), "s"(Cre), "s"(Cim), "s"(logdt), "s"(dskip), "s"(wg), "s"(wu), "s"(wd), "s"(ws), "s"(out));
    const int j0 = __builtin_amdgcn_readfirstlane(t >> 6);
    const unsigned v1 = (v0 ^ 64) + 1024;
    setm0i<SLT>(j0 * 2048); dmao<0>(sx, v0, so); dmao<1024>(sx, v1, so);
    const i32x4 sb0 = mk_srd((j0 >> 2) ? Bim : Bre, SP * SD * 2);
    const unsigned vb0 = ((t >> 3) & 31) * 1024 + (((t & 7) ^ ((t >> 4) & 7)) * 16), sob = (((b0 & 1) * 8 + ((b0 >> 3) & 7)) << 15);
    setm0i<SLT + OFF_B>(j0 * 1024); dmao<0>(sb0, vb0, sob + (perm & 7) * 128);
    const unsigned k1 = ((perm >> 4) & 7) * 128;
    setm0i<2 * SLT>(j0 * 2048); dmao<0>(sx, v0, (so & ~1023u) + k1); dmao<1024>(sx, v1, (so & ~1023u) + k1);
    setm0i<2 * SLT + OFF_B>(j0 * 1024); dmao<0>(sb0, vb0, sob + k1);
  }
  auto chunk = [&](int v) { return (perm >> (4 * v)) & 7; };
  const int j = __builtin_amdgcn_readfirstlane(ftid() >> 6), w = j & 3, sb_ = w & 1, pi = w >> 1;
  const int b = lnd(blockIdx.x), xcd = b & 7, idx = b >> 3;
  const int c = (xcd >> 1) * 4 + (idx >> 3), rsel = xcd & 1, gi = idx & 7, g = rsel * 8 + gi;
  auto get_fb = [&]() { const int l = (lnd(gi) + 1) >> 3; return (6 * (g - rsel)) * (1 - l) + (84 + rsel) * l; };
  auto get_ct = [&]() { const int slow = (3 - lnd((int)((u64)wd >> 7))) & 7; return lnd(gi == 7 ? slow : gi == slow ? 7 : gi); };
  const i32x4 sws = mk_srd(ws, WS_BYTES);
  const __amdgpu_buffer_rsrc_t rws = __builtin_amdgcn_make_buffer_rsrc(ws, 0, WS_BYTES, 0x00020000);
  const unsigned flagb = WS_FLAG + c * 16384;
  const unsigned a2pb = WS_A2 + (unsigned)(c * 4 + 2 * pi) * (NKS * 1024);
  const unsigned w4 = w * 4096, w2 = w * 2048, w6 = w * 6144;
  const unsigned so_x = (c * 128 + 64 * (w >> 1) + 16 * (w & 1)) * 1024;
  u64 tag2 = 0;
  auto set_flag = [&](int fi) {
    __builtin_amdgcn_raw_buffer_store_b64((u32x2){(unsigned)tag2, (unsigned)(tag2 >> 32)}, rws, (ftid() & 63) == 0 ? flagb + (g * 8 + fi) * 128 : OOB, 0, 16);
  };
  auto wait_cnt = [&]() {
#pragma unroll 1
    for (unsigned spins = 0; spins < SPIN_MAX; ++spins)
      if (__builtin_amdgcn_readfirstlane(__hip_atomic_load((LAS unsigned*)(size_t)M_CNT, __ATOMIC_RELAXED, __HIP_MEMORY_SCOPE_WORKGROUP)) == 4u) break;
    asm volatile("" ::: "memory");
  };
  if (j < 4) {
    const int lane = ftid() & 63, l5 = lane & 31, h = lane >> 5, blk = (lane >> 4) & 1, q = (lane & 15) >> 2, pp = lane & 3;
    unsigned vx[4], vw[3], ra[4], wt[2];
    u32x4 xk[3][4][2];
    const i32x4 sw = mk_srd((w >> 1) ? wu : wg, SD * SF * 2);
    const unsigned ld_w = ((w >> 1) ? OFF_WU : 0) + (w & 1) * 6144;
    unsigned so_w = 0;
    u32x4 sel;
    {
      const unsigned sw0 = ((lane & 7) ^ (lane >> 4)) * 16;
      for (int r = 0; r < 4; ++r) vx[r] = (32 * (lane >> 5) + 8 * ((lane >> 3) & 1) + ((lane >> 4) & 1) + r) * 1024 + (sw0 ^ ((r & 1) * 64));
      auto ldX = [&](auto S) { constexpr int s = S;
        const unsigned so = so_x + chunk(s) * 128;
        setm0i<((s + 1) % 6) * SLT>(w4);
        dmao<0>(sx, vx[0], so); dmao<1024>(sx, vx[1], so); dmao<2048>(sx, vx[2], so); dmao<3072>(sx, vx[3], so);
      };
      u32x2 ep2;
      if (w == 0 && lane == 0) *(LAS unsigned*)(size_t)M_CNT = 0;
      const unsigned xa = w * 4096 + lane * 16;
      unsigned ti;
      auto setup = [&]() {
        for (int s = 0; s < 4; ++s) { ra[s] = pi * 8192 + l5 * 128 + (((2 * (s ^ pi) + h) ^ ((l5 >> 1) & 7)) * 16); pin(ra[s]); }
        for (int p = 0; p < 2; ++p) { wt[p] = 3 * SLT + (blk ? OFF_WU : 0) + (8 * h + q) * 192 + (((pp & 1) << 1) | (pp >> 1)) * 8 + sb_ * 96 + (p ? -pi * 3072 : pi * 3072); pin(wt[p]); }
        const unsigned selv = (((lane & 15) == 0 && blk == 0) || ((lane & 15) == 4 && blk == 1)) ? 0x3f803f80u : 0u; sel = (u32x4){selv, selv, selv, selv}; pin(sel);
        ti = (lane & 4) ? M_CNT + 64 - M_ST : (64 * (w >> 1) + 32 * (lane >> 5) + 16 * (w & 1) + 4 * (lane & 3) + ((lane >> 3) & 3)) * 8;
        pin(ti);
      };
      auto setup2 = [&]() { const int q12 = (lane * 43) >> 9, r12 = lane - 12 * q12;
        for (int i = 0; i < 3; ++i) { const int r = r12 + 4 * i, cy = r >= 12, col = r - 12 * cy;
          vw[i] = (gi == 7 && col >= 2) ? OOB : (5 * i + q12 + cy) * (SF * 2) + col * 16 - i * 1024; pin(vw[i]); }
        so_w = (w & 1) * 32 * (SF * 2) + get_fb() * 32; };
      float s1[4] = {}, s2[4] = {};
      auto stats = [&](auto S, auto&& mid) { constexpr int s = S;
        u32x4 xr[4];
        for (int k = 0; k < 4; ++k) xr[k] = lds_r128(((s + 1) % 6) * SLT + k * 1024 + xa);
        sb(); mid(); sb();
        for (int e = 0; e < 4; ++e) for (int i = 0; i < 4; ++i) { const unsigned u = xr[i][e]; s1[i] = dot2(u, 0x3f803f80u, s1[i]); s2[i] = dot2(u, u, s2[i]); }
        for (int i = 0; i < 4; ++i) { pin(s1[i]); pin(s2[i]); }
        sb(); };
      wait_vm<3>(); wait_lgkm0(); bar();
      stats(IC(0), [&] { ldX(IC(2)); ld64_sc1(ep2, sws, 0, WS_E2 + c * 128); });
      wait_vm<5>(); bar();
      stats(IC(1), [&] { ldX(IC(3)); setup(); });
      sfor<2, 8>([&](auto S) { constexpr int s = S;
        wait_vm<(s < 7 ? 4 : 0)>();
        stats(S, [&] {
          if constexpr (s >= 3 && s < 6) { const unsigned hi = lnd(s * SLT); for (int k = 0; k < 4; ++k) { const unsigned a = ra[k] + hi; xk[s - 3][k][0] = lds_r128(a); xk[s - 3][k][1] = lds_r128(a + 4096); } }
          if constexpr (s < 6) ldX(IC(s + 2)); if constexpr (s == 6) setup2(); });
        if constexpr (s >= 3 && s < 6) wait_lgkm0();
        if constexpr (s < 7) { bar(); }
      });
      pin(ep2); tag2 = utag(ep2);
      float t1[2], t2[2];
      for (int k = 0; k < 2; ++k) { const bool o = lane & 1;
        t1[k] = (o ? s1[2 * k + 1] : s1[2 * k]) + dppf<0xB1>(o ? s1[2 * k] : s1[2 * k + 1]); t2[k] = (o ? s2[2 * k + 1] : s2[2 * k]) + dppf<0xB1>(o ? s2[2 * k] : s2[2 * k + 1]); }
      const bool o = lane & 2;
      float sm = (o ? t1[1] : t1[0]) + dppf<0x4E>(o ? t1[0] : t1[1]), sq = (o ? t2[1] : t2[0]) + dppf<0x4E>(o ? t2[0] : t2[1]);
      sm += dppf<0x104>(sm); sq += dppf<0x104>(sq);
      const float mean = sm * (1.f / SD);
      const float var = fmaxf(sq * (1.f / SD) - mean * mean, 0.f);
      const float sd = rbf(__builtin_amdgcn_sqrtf(rbf(rbf(var) + 1e-5f)));
      const float r_ = __builtin_amdgcn_rcpf(sd); const unsigned nm = pk2(-mean, -mean);
      *(LAS f32x2*)(size_t)(M_ST + ti) = (f32x2){r_, lo2f(nm) * r_}; *(LAS f32x2*)(size_t)(M_SD + ti) = (f32x2){sd, __uint_as_float(nm)};
    }
    f32x16 acc[3][2];
    f32x4 csg[3] = {};
    u32x4 af[2][2]; u32x2 wf[2][3][2];
    auto rd1 = [&](auto N, auto S, auto I) { constexpr int n = N, s = S, i = I;
      const unsigned a_ = ra[s] + (n % 3) * SLT, t_ = wt[s & 1] + (n % 3) * SLT + s * 3072;
      if constexpr (i == 0) { wf[s & 1][0][0] = lds_tr(t_); wf[s & 1][0][1] = lds_tr(t_ + 768); }
      if constexpr (i == 1 && n >= 3) { af[s & 1][0] = lds_r128(a_); af[s & 1][1] = lds_r128(a_ + 4096); }
      if constexpr (i == 2) { wf[s & 1][1][0] = lds_tr(t_ + 32); wf[s & 1][1][1] = lds_tr(t_ + 32 + 768); }
      if constexpr (i == 3) { wf[s & 1][2][0] = lds_tr(t_ + 64); wf[s & 1][2][1] = lds_tr(t_ + 64 + 768); }
    };
    auto W = [&](int p, int k) { return (u32x4){wf[p][k][0][0], wf[p][k][0][1], wf[p][k][1][0], wf[p][k][1][1]}; };
    f32x2 st[2], sd[2]; float csp[3]; u32x4 cf[3]; unsigned vs0 = 0;
    const unsigned csa_ = h ? M_T + 8192 + lane * 4 : M_CS + w * 384 + lane * 4;
    wait_vm<0>(); wait_lgkm0(); bar();
    sfor<0, 4>([&](auto I) { rd1(IC(0), IC(0), I); });
    sfor<0, 32>([&](auto K) { constexpr int n = K / 4, s = K % 4, nn = (K + 1) / 4, sn = (K + 1) % 4;
      sfor<0, 6>([&](auto I) { constexpr int i = I;
        acc[i >> 1][i & 1] = mfma(W(s & 1, i >> 1), n < 3 ? xk[n < 3 ? n : 0][s][i & 1] : af[s & 1][i & 1], K == 0 ? (f32x16){} : acc[i >> 1][i & 1]);
        sb();
        if constexpr (s == 3 && n < 7) { if constexpr (i == 0) { if constexpr (n >= 1) wait_vm<(n < 4 ? 6 : n < 6 ? 10 : 0)>(); wait_lgkm0(); bar(); } if constexpr (i >= 1 && i < 5) rd1(IC(nn), IC(sn), IC(i - 1)); }
        else if constexpr (K < 31 && i < 4) rd1(IC(nn), IC(sn), I);
        if constexpr (i >= 4 && K < 23) {
          constexpr int xn = (K + 1) / 4 + 2, kk = (K + 1) % 4, o0 = i == 5 ? (kk == 0 ? 0 : 3 * kk) : (kk == 0 ? 0 : 3 * kk - 1), o1 = i == 5 ? (kk == 3 ? 10 : 3 * kk + 2) : (kk == 0 ? 0 : 3 * kk);
          sfor<o0, o1>([&](auto O) { constexpr int o = O;
            if constexpr (o < 4) { if constexpr (xn > 5) { if constexpr (o == 0) setm0i<(xn % 3) * SLT>(w4); dmao<o * 1024>(sx, vx[o], so_x + chunk((xn + 2) & 7) * 128); } }
            else { constexpr int u = o - 4; if constexpr (u % 3 == 0) setm0i<(3 + xn % 3) * SLT + u * 1024>(ld_w);
              dmao<(u % 3) * 1024>(sw, vw[u % 3], so_w + chunk((xn + 2) & 7) * (64 * SF * 2) + (u / 3) * 16 * (SF * 2)); } }); }
        if constexpr (K == 24 && i == 4) { const unsigned ia = (64 * pi + 32 * ((lane >> 2) & 1) + 4 * ((lane >> 3) & 3) + (lane & 3)) * 8;
          for (int rb = 0; rb < 2; ++rb) { st[rb] = *(LAS f32x2*)(size_t)(M_ST + ia + rb * 128); sd[rb] = *(LAS f32x2*)(size_t)(M_SD + ia + rb * 128); } }
        if constexpr (K == 28 && i == 4) { for (int k = 0; k < 3; ++k) csp[k] = *(LAS float*)(size_t)(M_CS + ((w ^ 2) * 3 + k) * 128 + l5 * 4); }
        if constexpr (K == 29 && i == 4) { vs0 = a2pb + lane * 16 + (64 + get_fb() + 3 * sb_) * 1024; pin(vs0); }
        if constexpr (K == 31 && i == 0) { wait_lgkm0();
          if (lane == 0) __hip_atomic_fetch_add((LAS unsigned*)(size_t)M_CNT, 1u, __ATOMIC_RELAXED, __HIP_MEMORY_SCOPE_WORKGROUP); }
        if constexpr (K == 31 && i >= 2 && i < 5) { constexpr int k = i - 2;
          const float cs = h ? 0.f : csg[k][0] + csp[k];
          const float c0 = rbf(cs), r1 = cs - c0, c1 = rbf(r1), c2 = r1 - c1;
          cf[k] = (u32x4){pk2(c0, c1), pk2(c2, 0.f), 0u, 0u}; pin(cf[k]); }
        sb();
      });
      if constexpr ((!(s & 1) || K == 29) && K < 31) { for (int k = 0; k < 3; ++k) csg[k] = mfma16(sel, W(s & 1, k), csg[k]); sb(); }
      if constexpr (K == 30) { for (int k = 0; k < 3; ++k) csg[k] = mfma16(sel, W(1, k), csg[k]); sb(); }
      if constexpr (K == 26) { *(LAS float*)(size_t)(csa_) = csg[0][0]; *(LAS float*)(size_t)(csa_ + 128) = csg[1][0]; *(LAS float*)(size_t)(csa_ + 256) = csg[2][0]; sb(); }
    });
    for (int k = 0; k < 3; ++k) for (int rb = 0; rb < 2; ++rb) { const unsigned nm = __float_as_uint(sd[rb][1]); acc[k][rb] = mfma(cf[k], (u32x4){nm, nm, 0u, 0u}, acc[k][rb]); }
    sfor<0, 3>([&](auto K) { constexpr int k = K;
      const unsigned vst = (gi < 7 || 3 * sb_ + k == 0) ? vs0 + k * 1024 : OOB;
      sfor<0, 2>([&](auto R_) { constexpr int rb = R_;
        unsigned o[4];
        for (int i = 0; i < 4; ++i) {
          const f32x2 gv = (f32x2){acc[k][rb][2 * i], acc[k][rb][2 * i + 1]} * sp(st[rb][0]), uv = {acc[k][rb][8 + 2 * i], acc[k][rb][9 + 2 * i]};
          const f32x2 z = gv * pfma(gv * gv, sp(-0.1029432397f), sp(-2.302208198f));
          const f32x2 d = pfma((f32x2){__builtin_amdgcn_exp2f(z[0]), __builtin_amdgcn_exp2f(z[1])}, sp(sd[rb][0]), sp(sd[rb][0]));
          const f32x2 r = (gv * uv) * (f32x2){__builtin_amdgcn_rcpf(d[0]), __builtin_amdgcn_rcpf(d[1])};
          o[i] = pk2(r[0], r[1]); }
        __builtin_amdgcn_raw_buffer_store_b128((u32x4){o[0], o[1], o[2], o[3]}, rws, vst + rb * NKS * 1024, 0, 16);
      });
    });
    sb();
    wait_vm<0>();
    set_flag(4 + w);
    const int t = ftid(), ch = t & 15, r4 = t >> 4, ct = get_ct();
    const int trow = 64 * rsel + 32 * ((r4 >> 2) & 1) + 8 * (r4 & 1) + 2 * (r4 >> 3) + ((r4 >> 1) & 1);
    const int irow = 64 * rsel + 32 * ((r4 >> 2) & 1) + 4 * (r4 >> 3) + (r4 & 3);
    u32x2 xv[4], dk;
    for (int it = 0; it < 4; ++it) ld64(xv[it], sx, trow * 1024 + ch * 8, c * 131072 + ct * 128 + (4 * (it & 1) + 16 * (it >> 1)) * 1024);
    ld64(dk, mk_srd(dskip, SD * 2), ch * 8, ct * 128);
    wait_vm<0>(); for (int it = 0; it < 4; ++it) pin(xv[it]); pin(dk);
    f32x4 base[4]; unsigned ph[4];
    for (int it = 0; it < 4; ++it) { const unsigned io = (irow + 8 * (it & 1) + 16 * (it >> 1)) * 8;
      const float rs = *(LAS float*)(size_t)(M_ST + io), nm = lo2f(*(LAS unsigned*)(size_t)(M_SD + io + 4));
      for (int e = 0; e < 4; ++e) { const unsigned xu = xv[it][e >> 1], du = dk[e >> 1];
        const float xe = (e & 1) ? hi2f(xu) : lo2f(xu), de = (e & 1) ? hi2f(du) : lo2f(du);
        base[it][e] = xe + rbf(rbf(rbf(xe + nm) * rs) * de); }
      const int e = 2 * it + (r4 >> 3), d = (e * 11) >> 5;
      ph[it] = (3 + d) * SLT + (e - 3 * d) * 2048 + (r4 & 7) * 256 + (ch ^ (r4 & 7)) * 16;
      pin(base[it]); pin(ph[it]); }
    const __amdgpu_buffer_rsrc_t rout = __builtin_amdgcn_make_buffer_rsrc(out, 0, SL * SD * 4, 0x00020000);
    const unsigned oo = ((c * 128 + trow) * SD + ct * 64 + ch * 4) * 4;
    bar();
    for (int it = 0; it < 4; ++it) {
      f32x4 p[4];
      for (int ww = 0; ww < 4; ++ww) p[ww] = *(LAS f32x4*)(size_t)(ph[it] + ww * 6144);
      __builtin_amdgcn_raw_buffer_store_b128(__builtin_bit_cast(u32x4, base[it] + ((p[0] + p[1]) + (p[2] + p[3]))), rout, oo + (4 * (it & 1) + 16 * (it >> 1)) * SD * 4, 0, 16);
    }
    if (g == 0) __builtin_amdgcn_raw_buffer_store_b64((u32x2){(unsigned)tag2, (unsigned)(tag2 >> 32)}, rws, t == 0 ? WS_E2 + c * 128 : OOB, 0, 16);
  } else {
    const int lane = ftid() & 63, l5 = lane & 31, h = lane >> 5, blk = (lane >> 4) & 1, q = (lane & 15) >> 2, pp = lane & 3;
    const unsigned va = lane * 16;
    const unsigned sw0 = ((lane & 7) ^ (lane >> 4)) * 16;
    unsigned vb[2], vw[3], vd[2];
    for (int r = 0; r < 2; ++r) vb[r] = ((lane >> 3) + 7 * r) * 1024 + (sw0 ^ (r * 64));
    const i32x4 sbm = mk_srd((w >> 1) ? Bim : Bre, SP * SD * 2);
    const unsigned so_b = (g * 32 + (w & 1) * 16) * 1024;
    auto ldB = [&](auto S) { constexpr int s = S;
      const unsigned so = so_b + chunk(s) * 128;
      setm0i<((s + 1) % 6) * SLT + OFF_B>(w2); dmao<0>(sbm, vb[0], so); dmao<1024>(sbm, vb[1], so); };
    u32x2 ep2, ep1; unsigned plr, pli, pdt;
    ld64_sc1(ep2, sws, 0, WS_E2 + c * 128);
    ld64_sc1(ep1, sws, 0, WS_E1 + g * 128);
    const int ps = (lane & 3) | ((lane & 4) << 1) | ((lane & 8) >> 1);
    { const unsigned po = (g * 32 + sb_ * 16 + ps) * 2;
      ld16(plr, mk_srd(lam_re, SP * 2), po); ld16(pli, mk_srd(lam_im, SP * 2), po); ld16(pdt, mk_srd(logdt, SP * 2), po); }
    { u64 pc; unsigned len, back;
      asm volatile("s_getpc_b64 %0\n.Lpf_here:\n\ts_mov_b32 %1, .Lpf_end-.Lpf_beg\n\ts_mov_b32 %2, .Lpf_here-.Lpf_beg" : "=s"(pc), "=s"(len), "=s"(back));
      for (unsigned o = (idx * 4 + w) * 128; o + 256 < len; o += 16384) sline((pc - back) & ~127ull, o + 128); }
    const i32x4 sw = mk_srd((w >> 1) ? wu : wg, SD * SF * 2);
    const unsigned so_w = (w & 1) * 32 * (SF * 2) + get_fb() * 32;
    const unsigned ld_w = ((w >> 1) ? OFF_WU : 0) + (w & 1) * 6144;
    auto opW = [&](auto N, auto I) { constexpr int n = N, i = I;
      if constexpr (i % 3 == 0) setm0i<(3 + n % 3) * SLT + i * 1024>(ld_w);
      dmao<(i % 3) * 1024>(sw, vw[i % 3], so_w + chunk((n + 2) & 7) * (64 * SF * 2) + (i / 3) * 16 * (SF * 2)); };
    unsigned ra[4], wa[4];
    for (int s = 0; s < 4; ++s) { ra[s] = pi * 8192 + l5 * 128 + (((2 * s + h) ^ ((l5 >> 1) & 7)) * 16);
      const int row = sb_ * 16 + ps; wa[s] = OFF_B + blk * 4096 + row * 128 + (((2 * s + h) ^ ((row >> 1) & 7)) * 16); }
    const unsigned selv = (((lane & 15) == 0 && blk == 0) || ((lane & 15) == 4 && blk == 1)) ? 0x3f803f80u : 0u;
    const u32x4 sel = {selv, selv, selv, selv};
    for (int s = 0; s < 4; ++s) { pin(ra[s]); pin(wa[s]); }
    unsigned t1l = 0, t1h = 0;
    const int cc = 2 * c + pi;
    float ar, ai, nai, kr, nkr, nki, a8r, a8i, a16r, a16i, a32r, a32i, a64r, a64i, pwr = 1.f, pwi = 0.f, a256r, a256i, mr = 1.f, mi = 0.f;
    u32x4 oh[2];
    float lr, li;
    auto params = [&](auto V) { constexpr int v = V;
      if constexpr (v == 1) { const int q12 = (lane * 43) >> 9, r12 = lane - 12 * q12;
        for (int i = 0; i < 3; ++i) { const int r = r12 + 4 * i, cy = r >= 12, col = r - 12 * cy;
          vw[i] = (gi == 7 && col >= 2) ? OOB : (5 * i + q12 + cy) * (SF * 2) + col * 16 - i * 1024; pin(vw[i]); } }
      if constexpr (v == 2) {
        pin(ep2); pin(ep1); pin(plr); pin(pli); pin(pdt);
        tag2 = utag(ep2);
        const u64 tag1 = utag(ep1); t1l = (unsigned)tag1; t1h = (unsigned)(tag1 >> 32); }
      if constexpr (v == 3) {
        lr = lo2f(plr); li = lo2f(pli);
        const float dt = rbf(__expf(lo2f(pdt)));
        const float er = __expf(dt * lr);
        const float rev = dt * li * 0.15915494309189535f;
        const float fr = rev - __builtin_rintf(rev);
        ar = er * __builtin_amdgcn_cosf(fr); ai = er * __builtin_amdgcn_sinf(fr);
        pin(ar); pin(ai); }
      if constexpr (v == 4) { a8r = ar; a8i = ai;
        for (int i = 0; i < 3; ++i) csq(a8r, a8i, a8r, a8i);
        csq(a16r, a16i, a8r, a8i); pin(a16r); pin(a16i); }
      if constexpr (v == 6) {
        const float nr = ar - 1.f, ni = ai, den = 2.f * __builtin_amdgcn_rcpf(vfma(lr, lr, li * li));
        kr = vfma(nr, lr, ni * li) * den; nki = vfma(nr, li, -(ni * lr)) * den; nkr = -kr; nai = -ai;
        pin(nai); pin(kr); pin(nkr); pin(nki); }
      if constexpr (v == 10) { csq(a32r, a32i, a16r, a16i); csq(a64r, a64i, a32r, a32i); if (blk) { pwr = a16r; pwi = a16i; mr = a64r; mi = a64i; } }
      if constexpr (v == 11) { float tr = pwr, ti = pwi; cf4(tr, ti, a32r, a32i, 0.f, 0.f); if (h) { pwr = tr; pwi = ti; } csq(a32r, a32i, a64r, a64i); }
      if constexpr (v == 12) { float ur = mr, ui = mi; cf4(ur, ui, a32r, a32i, 0.f, 0.f); if (h) { mr = ur; mi = ui; } csq(a256r, a256i, a32r, a32i); }
      if constexpr (v == 13 || v == 14) { constexpr int u = v - 13;
        const unsigned val = (((lane >> 2) & 1) == h && blk == u) ? ((lane & 1) ? 0x3f800000u : 0x3f80u) : 0u; const int d0 = ((lane >> 1) & 1) | ((lane >> 2) & 2);
        for (int d = 0; d < 4; ++d) oh[u][d] = d0 == d ? val : 0u;
        pin(oh[u]); }
      sb();
    };
    f32x16 sa[2] = {};
    f32x4 csa = {};
    { u32x4 af[2][4][2], wf[2][4];
      auto rdA = [&](int p, int s, unsigned slot) { wf[p][s] = lds_r128(slot + wa[s]); af[p][s][0] = lds_r128(slot + ra[s]); af[p][s][1] = lds_r128(slot + ra[s] + 4096); };
      auto mmA = [&](int p, int s) { sa[0] = mfma(af[p][s][0], wf[p][s], sa[0]); sa[1] = mfma(af[p][s][1], wf[p][s], sa[1]); pin(sa[0]); pin(sa[1]); };
      auto csA = [&](int p) { for (int s = 0; s < 4; ++s) csa = mfma16(sel, wf[p][s], csa); pin(csa); asm volatile("s_nop 7"); sb(); };
      auto W3 = [&](auto N, auto H_) { sfor<0, 3>([&](auto I) { opW(N, IC(3 * H_ + I)); }); };
      unsigned rh[4], wh[4];
      for (int s = 0; s < 4; ++s) { rh[s] = ra[s] + 3 * SLT; wh[s] = wa[s] + 3 * SLT - 8192; pin(rh[s]); pin(wh[s]); wh[s] += 8192; wa[s] -= 8192; pin(wa[s]); wa[s] += 8192; }
      ldB(IC(2));
      wait_vm<10>(); bar(); for (int s = 0; s < 4; ++s) rdA(0, s, SLT); sb(); ldB(IC(3));
      sfor<1, 8>([&](auto V) { constexpr int v = V, p = v & 1, sl = (v + 1) % 6;
        wait_vm<(v == 1 ? 9 : v < 5 ? 4 : v == 5 ? 7 : v == 6 ? 11 : 6)>(); wait_lgkm0();
        if constexpr (v == 7) __builtin_amdgcn_s_setprio(3);
        bar();
        sfor<0, 8>([&](auto I) { constexpr int i = I;
          sa[i & 1] = mfma(af[p ^ 1][i >> 1][i & 1], wf[p ^ 1][i >> 1], sa[i & 1]); pin(sa[i & 1]);
          sb();
          if constexpr (i < 6) sfor<2 * i, 2 * i + 2>([&](auto R) { constexpr int r = R, s = r / 3, k = r % 3, o = (sl % 3) * SLT;
            if constexpr (k == 0) wf[p][s] = lds_r128((sl < 3 ? wa[s] : wh[s]) + o); else af[p][s][k - 1] = lds_r128((sl < 3 ? ra[s] : rh[s]) + o + (k - 1) * 4096); });
          if constexpr (i == 6 && v < 5) ldB(IC(v + 3));
          if constexpr (i == 7 && v == 4) W3(IC(0), IC(0));
          if constexpr (i == 6 && v == 5) W3(IC(0), IC(1));
          if constexpr (i == 7 && v == 5) W3(IC(1), IC(0));
          if constexpr (i == 6 && v == 6) W3(IC(1), IC(1));
          sb(); });
        csA(p ^ 1);
        params(V);
      });
      for (int s = 0; s < 4; ++s) mmA(1, s);
      csA(1);
      __builtin_amdgcn_s_setprio(0);
    }
    const int kb = 22 * w - (w == 3), cnt = w < 2 ? 22 : 21, ct = get_ct();
    const i32x4 swd = mk_srd(wd + kb * 8192, cnt * 16384);
    const unsigned so_c = ct * 65536 + w * 256;
    const unsigned r2 = (xcd * 11) >> 2;
    auto kofs = [&](int p, unsigned sc_) { const unsigned r = lnd(r2 * sc_), a = r + p * sc_, b2 = r + (p - 22) * sc_; return a < b2 ? a : b2; };
    for (int r = 0; r < 2; ++r) vd[r] = ((lane >> 3) + 7 * r) * 1024 + (((lane & 7) ^ (((lane >> 4) & 1) << 2)) * 16) + ct * 128;
    unsigned so_t = 0; i32x4 sc = swd;
    auto wd_op = [&](auto M, auto E, auto R) { constexpr int m = M, e = E, r = R;
      if constexpr (r == 0) { so_t = kofs(m < 18 ? (m + 14) % 18 : m, 16384); setm0i<cell_c(e)>(cell_k(e) == 0 ? w6 : cell_k(e) == 1 ? w4 : w2); }
      dmao<r * 1024>(swd, vd[r], so_t);
    };
    auto dma_wd = [&](auto M, auto E) { wd_op(M, E, IC(0)); wd_op(M, E, IC(1)); };
    auto c_op = [&](auto N, auto I) { constexpr int n = N, i = I;
      if constexpr (i == 0) { const int kg = ((n >> 1) + lnd(xcd)) & 3; so_t = so_c + (n & 1) * 32768 + (kg & 1) * 128; sc = mk_srd((kg >> 1) ? Cim : Cre, SD * SP * 2); }
      if constexpr (!(i & 1)) setm0i<(n & 1) ? ((i >> 1) ? M_T : cell_c(15)) : cell_c(16 + (i >> 1))>(w2);
      dmao<(i & 1) * 1024>(sc, vb[i & 1], so_t + (i >> 1) * 16384);
    };
    unsigned rcx[4];
    for (int s = 0; s < 4; ++s) rcx[s] = OFF_B + w * 2048 + blk * SLT + (lane & 15) * 128 + (((2 * s + h) ^ ((l5 >> 1) & 7)) * 16);
    const unsigned rcd = 2 * SLT + blk * (M_T - cell_c(15) - SLT);
    auto rd_c = [&](int n, u32x4* d) { for (int s = 0; s < 4; ++s) d[s] = lds_r128(rcx[s] + ((n & 1) ? rcd : 0u)); };
    const unsigned granb = WS_GRAN + (g * 2 + sb_) * 8192;
    const i32x4 sgr = mk_srd(ws + granb, 8192);
    const unsigned vg = (32 - cc) * 256 + va;
    float R[16], I_[16], csr, csi, er[2], ei[2], sr[4], si[4], prer, prei, cr, ci;
    f32x4 tb[8];
    u32x4 ge[8], fr4[4];
    f32x16 td[2];
    unsigned hp[2][8];
    unsigned nbad = 0, fo_g = 0, pa[2], tdl = 0;
    auto side = [&](auto P_) { constexpr int P = P_;
      if constexpr (P == 0) { for (int k = 0; k < 8; ++k) tb[k] = *(LAS f32x4*)(size_t)(M_ST + pi * 512 + (lane >> 4) * 128 + k * 16);
        csr = csa[0]; float t = csr; swap32(csr, t); csi = csr; swap16(csr, csi); }
      if constexpr (P >= 1 && P < 9) { constexpr int k = P - 1;
        for (int r = 0; r < 2; ++r) { R[2 * k + r] = sa[0][2 * k + r]; I_[2 * k + r] = sa[1][2 * k + r]; swap16(R[2 * k + r], I_[2 * k + r]); }
        for (int r = 0; r < 2; ++r) sc4(R[2 * k + r], I_[2 * k + r], tb[k][2 * r], tb[k][2 * r + 1], csr, csi); }
      if constexpr (P == 9) { for (int r = 0; r < 2; ++r) { er[r] = R[r]; ei[r] = I_[r]; cf4(er[r], ei[r], ar, ai, R[2 + r], I_[2 + r]); } }
      if constexpr (P >= 16 && P < 22) { constexpr int k = P - 14; for (int r = 0; r < 2; ++r) cf4(er[r], ei[r], ar, ai, R[2 * k + r], I_[2 * k + r]); }
      if constexpr (P == 22) { sr[0] = er[0]; si[0] = ei[0]; cf4(sr[0], si[0], a8r, a8i, er[1], ei[1]); sr[1] = sr[0]; swap16(sr[0], sr[1]); si[1] = si[0]; swap16(si[0], si[1]); }
      if constexpr (P == 23) { sr[2] = sr[0]; swap32(sr[0], sr[2]); si[2] = si[0]; swap32(si[0], si[2]); sr[3] = sr[1]; swap32(sr[1], sr[3]); si[3] = si[1]; swap32(si[1], si[3]); }
      if constexpr (P == 24) { cr = sr[0]; ci = si[0]; cf4(cr, ci, a16r, a16i, sr[1], si[1]); sr[1] = cr; si[1] = ci; cf4(cr, ci, a16r, a16i, sr[2], si[2]); sr[2] = cr; si[2] = ci; }
      if constexpr (P == PPUB) { cf4(cr, ci, a16r, a16i, sr[3], si[3]);
        __builtin_amdgcn_raw_buffer_store_b128((u32x4){__float_as_uint(cr), t1l, __float_as_uint(ci), t1h}, rws, lane < 16 ? granb + (31 - cc) * 256 + va : OOB, 0, 16); }
      if constexpr (P == 32) { prer = h ? (blk ? sr[2] : sr[1]) : (blk ? sr[0] : 0.f); prei = h ? (blk ? si[2] : si[1]) : (blk ? si[0] : 0.f); }
      sfor<0, 16>([&](auto K) { constexpr int k = K; if constexpr (P == nxt(33, k < 4 ? k / 2 : k - 2)) kc4(R[k], I_[k], kr, nkr, nki); });
      if constexpr (P >= 53 && P < 58) params(IC(P - 43));
      if constexpr (P == PG) sfor<0, 8>([&](auto K) { constexpr int k = K; ld128_sc1<(k & 3) * 1024>(ge[k], sgr, vg, (k >> 2) * 4096); });
      if constexpr (P == PC) { wait_vm<hb_cnt(PC, false) - hb_cnt(PG, true)>(); for (int i = 0; i < 8; ++i) pin(ge[i]); }
      if constexpr (P == PC || P == PC + 1) { constexpr int i = 4 * (P - PC); miss2(nbad, ge[i], ge[i + 1], t1l, t1h); miss2(nbad, ge[i + 2], ge[i + 3], t1l, t1h); }
      if constexpr (P == PC + 2) { const unsigned nout = 8 - ((cc + 3 - (lane >> 4)) >> 2);
#pragma unroll 1
        for (unsigned spins = 0; spins < SPIN_MAX; ++spins) {
          if (__all(nbad == nout)) break;
          __builtin_amdgcn_s_sleep(SLP);
          sfor<0, 8>([&](auto I) { constexpr int i = I; ld128_sc1_rw<(i & 3) * 1024>(ge[i], sgr, vg, (i >> 2) * 4096); });
          wait_vm<0>(); for (int i = 0; i < 8; ++i) pin(ge[i]);
          nbad = 0;
          for (int i = 0; i < 8; i += 2) miss2(nbad, ge[i], ge[i + 1], t1l, t1h);
        }
        cr = __uint_as_float(ge[7][0]); ci = __uint_as_float(ge[7][2]); cf4(cr, ci, a256r, a256i, __uint_as_float(ge[6][0]), __uint_as_float(ge[6][2])); }
      if constexpr (P >= PC + 3 && P < PC + 6) { constexpr int i = 5 - 2 * (P - PC - 3);
        cf4(cr, ci, a256r, a256i, __uint_as_float(ge[i][0]), __uint_as_float(ge[i][2])); cf4(cr, ci, a256r, a256i, __uint_as_float(ge[i - 1][0]), __uint_as_float(ge[i - 1][2])); }
      if constexpr (P == PC + 6) { cf4(cr, ci, mr, mi, 0.f, 0.f); float t = cr, u = ci; swap16(cr, t); swap16(ci, u); cr = vadd(cr, t); ci = vadd(ci, u); }
      if constexpr (P == PC + 7) { float t = cr, u = ci; swap32(cr, t); swap32(ci, u); cr = vadd(cr, t); ci = vadd(ci, u); cf4(cr, ci, pwr, pwi, prer, prei); }
      if constexpr (P == PC + 8) { sr[0] = cr; si[0] = ci; cf4(cr, ci, a8r, a8i, er[0], ei[0]); er[0] = sr[0]; ei[0] = si[0]; er[1] = cr; ei[1] = ci; }
      if constexpr (P == PC + 9) { kc4(er[0], ei[0], kr, nkr, nki); kc4(er[1], ei[1], kr, nkr, nki); }
      if constexpr (P % 16 == 4 && P / 16 >= 2 && P / 16 < 6) { const unsigned o = kofs(16 + P / 16, 16384);
        if (o < (unsigned)cnt * 16384) for (int r = 0; r < 4; ++r) sline((u64)(wd + kb * 8192), o + ct * 128 + (4 * (c & 3) + r) * 1024); }
      sfor<0, 8>([&](auto K) { constexpr int k = K; if constexpr (P == nxt(P2, k)) {
        for (int r = 0; r < 2; ++r) cf4c(k ? R[2 * k - 2 + r] : er[r], k ? I_[2 * k - 2 + r] : ei[r], ar, ai, nai, R[2 * k + r], I_[2 * k + r]);
        hp[0][k] = pk2(R[2 * k], R[2 * k + 1]); hp[1][k] = pk2(I_[2 * k], I_[2 * k + 1]); } });
      static_assert(PT > nxt(P2, 7) && PS > PT && PF > PS && PC % 16 == 0, "scan schedule");
      if constexpr (P == PT) {
        __builtin_amdgcn_s_setprio(3);
        for (int part = 0; part < 2; ++part) {
          td[part] = mfma((u32x4){hp[part][0], hp[part][1], hp[part][2], hp[part][3]}, oh[0], (f32x16){});
          td[part] = mfma((u32x4){hp[part][4], hp[part][5], hp[part][6], hp[part][7]}, oh[1], td[part]); pin(td[part]); }
        __builtin_amdgcn_s_setprio(0); }
      if constexpr (P == PS) { for (int k = 0; k < 4; ++k) for (int d = 0; d < 4; ++d) fr4[k][d] = pk2(td[k >> 1][8 * (k & 1) + 2 * d], td[k >> 1][8 * (k & 1) + 2 * d + 1]); }
      if constexpr (P == PS) { for (int k = 0; k < 4; ++k)
          __builtin_amdgcn_raw_buffer_store_b128(fr4[k], rws, a2pb + (2 * g + sb_) * 1024 + va + ((k & 1) * NKS + (k >> 1) * 32) * 1024, 0, 16); }
      if constexpr (P == 100) { const int ks = kb + lane, p6 = (ks * 43) >> 8, pg = ks < 84 ? p6 + (p6 >= 7) : 7 + 8 * (ks - 84), pb = ks < 84 ? ks - 6 * p6 : 0;
        fo_g = lane < cnt ? (pg * 8 + 4 + 2 * rsel + (pb >= 3)) * 128 : OOB; pin(fo_g); }
      if constexpr (P == 104) { for (int rb = 0; rb < 2; ++rb) { const int e = 4 * rb + (l5 >> 3), d = (e * 11) >> 5;
          pa[rb] = (3 + d) * SLT + (e - 3 * d) * 2048 + w6 + (lane & 7) * 256 + ((h ^ (lane & 7)) * 16); pin(pa[rb]); } }
      if constexpr (P == 106) { tdl = (8 * h + q) * 128 + (((blk * 2 + (pp >> 1)) ^ ((q >> 1) << 2)) * 16) + (pp & 1) * 8; pin(tdl); }
      if constexpr (P == PF) { wait_vm<0>(); set_flag(w);
        if (cc == 31) __builtin_amdgcn_raw_buffer_store_b64((u32x2){t1l, t1h}, rws, (lane == 0 && sb_ == 0) ? WS_E1 + g * 128 : OOB, 0, 16); }
    };
    u32x4 cq[8][4];
    u32x2 fs;
    const unsigned fo_s = lane < 8 ? ((4 * w + (lane >> 1)) * 8 + 2 * rsel + (lane & 1)) * 128 : OOB;
    sfor<0, 8>([&](auto N) { constexpr int n = N, g0 = hb_g0(n), done = n ? hb_cnt(16 * n - 1, true) : 0;
      if constexpr (n > 0) { if constexpr (n == 1) wait_vm<done>(); bar(); }
      if constexpr (n > 1) { wait_vm<done - hb_cnt(16 * (n > 1 ? n - 2 : 0) + hb_g0(n - 2) + 3, false)>(); rd_c(n, cq[n - 2]); sb(); }
      sfor<0, hb_ops(n)>([&](auto I) { constexpr int i = I, io = i < g0 ? i : i - 4;
        if constexpr (i >= g0 && i < g0 + 4) { if constexpr (i == g0 && n > 1) { for (int s = 0; s < 4; ++s) use(cq[n - 2][s]); } c_op(N, IC(i - g0)); }
        else if constexpr (n >= 6) { constexpr int e = 5 * (n - 6) + (io >> 1); wd_op(IC(e < 4 ? 18 + e : e), IC(e), IC(io & 1)); }
        if constexpr (16 * n + i == PPOLL) ld64_sc1(fs, sws, fo_s, flagb);
        side(IC(16 * n + i));
        sb();
      });
    });
    auto poll = [&](unsigned fo, u32x2 fv) {
#pragma unroll 1
      for (unsigned spins = 0; spins < SPIN_MAX; ++spins) {
        const bool ok = fo == OOB || (fv[0] == (unsigned)tag2 && fv[1] == (unsigned)(tag2 >> 32));
        if (__all(ok)) break;
        __builtin_amdgcn_s_sleep(SLP);
        ld64_sc1(fv, sws, fo, flagb); wait_vm<0>(); pin(fv);
      }
    };
    const unsigned a2c = WS_A2 + (unsigned)(c * 4 + 2 * rsel) * (NKS * 1024);
    const i32x4 sg0 = mk_srd(ws + a2c + (64 + kb) * 1024, cnt * 1024), sg1 = mk_srd(ws + a2c + (NKS + 64 + kb) * 1024, cnt * 1024);
    const unsigned vs0 = va + a2c + w * 8192, vs1 = vs0 + NKS * 1024;
    wait_vm<hb_cnt(127, true) - hb_cnt(PPOLL, true)>(); pin(fs);
    rd_c(0, cq[6]);
    poll(fo_s, fs);
    unsigned tb2[4][2];
    auto rd_half = [&](int e, int cb) { const int k = cell_k(e) == 0 ? (cell_c(e) >= 5 * SLT) : cell_k(e) + 1;
      return lds_tr2(tb2[k][cb] + (cell_c(e) - (k == 0 ? 3 * SLT : k == 1 ? 5 * SLT : k == 3 ? OFF_B : 0)), 512); };
    f32x16 acc2[2][2];
    u32x4 L[76], wq[4][2], bf[2][2];
    unsigned so_l = 0;
    auto soL = [&](auto M) { constexpr int m = M;
      if constexpr (m < 16) { if constexpr (!(m & 3)) { const int kg = ((m >> 2) + lnd(xcd)) & 3; so_l = ((kg >> 1) * 32 + (kg & 1) * 4) * 1024; } }
      else so_l = kofs(m - 16, 1024); };
    auto ldH = [&](auto M, auto R) { constexpr int m = M, r = R;
      if constexpr (m < 16) ld128_sc1<(m & 3) * 1024>(L[2 * m + r], sws, r ? vs1 : vs0, so_l); else ld128_sc1<0>(L[2 * m + r], r ? sg1 : sg0, va, so_l); };
    auto ldL = [&](auto M) { soL(M); ldH(M, IC(0)); ldH(M, IC(1)); };
    sfor<0, p2_l0(0)>(ldL);
    wait_lgkm0();
    dma_wd(IC(16), IC(16)); dma_wd(IC(17), IC(17));
    wait_cnt();
    sfor<10, 13>([&](auto E) { dma_wd(E, E); });
    sfor<0, 34>([&](auto M) { constexpr int m = M, mm = m - 16;
      constexpr bool one = p2_l0(m + 1) == p2_l0(m) + 1;
      wait_vm<p2_after(m)>(); pin(L[2 * m]); pin(L[2 * m + 1]);
      sfor<0, 4>([&](auto G) { constexpr int gp = G, cb = gp >> 1, rb = gp & 1;
        if constexpr (m < 16) acc2[cb][rb] = mfma(cq[2 * (m >> 2) + cb][m & 3], L[2 * m + rb], m == 0 ? (f32x16){} : acc2[cb][rb]);
        else acc2[cb][rb] = mfma(mm < 18 ? bf[mm & 1][cb] : wq[mm < 18 ? 0 : mm - 18][cb], L[2 * m + rb], acc2[cb][rb]);
        sb();
        if constexpr (gp < 2) {
          if constexpr (m >= 15 && mm + 1 < 18) bf[(mm + 1) & 1][gp] = rd_half((mm + 5) % 18, gp);
          if constexpr (m == 0 || m == 1) wd_op(IC(13 + m), IC(13 + m), G);
          if constexpr (m == 2 || m == 3) { constexpr int k = 2 * (m - 2) + gp;
            for (int u = 0; u < 2; ++u) { tb2[k][u] = (tdl ^ (u * 64)) + (k == 0 ? 3 * SLT + w6 : k == 1 ? 5 * SLT + w6 : k == 2 ? w4 : OFF_B + w2); pin(tb2[k][u]); } }
          if constexpr (m == 4 && gp == 0) rd_c(1, cq[7]);
          if constexpr (m == 5) { if constexpr (gp == 0) { for (int s = 0; s < 4; ++s) use(cq[7][s]); } wd_op(IC(15), IC(15), G); }
          if constexpr (m >= 7 && m <= 10) { if constexpr (gp == 0) { use(wq[m - 7][0]); use(wq[m - 7][1]); } wd_op(IC(m - 7), IC(m - 7), G); }
          if constexpr (m >= 6 && m <= 9) wq[m - 6][gp] = rd_half(m - 6, gp);
          if constexpr (gp == 1 && m == PSM) ld64_sc1(fs, sws, fo_g, flagb);
          if constexpr (gp == 1 && one) soL(IC(p2_l0(m)));
        } else {
          if constexpr (gp == 2 && m == PCK) { wait_vm<p2_exsum(PSM + 1, PCK) + 2 * (16 - p2_l0(PSM))>(); pin(fs); poll(fo_g, fs); }
          if constexpr (one) ldH(IC(p2_l0(m)), IC(gp - 2));
          else if constexpr (gp == 2) sfor<p2_l0(m), p2_l0(m + 1)>(ldL);
        }
        sb();
      });
    });
    sfor<0, 5>([&](auto B_) { constexpr int bk = B_, cb = bk >> 1, rb = bk & 1, pc = (bk - 1) >> 1, pr = (bk - 1) & 1;
      sfor<0, 4>([&](auto U) { constexpr int u = U, m = 34 + u;
        if constexpr (bk == 0) { wait_vm<6 - 2 * u>(); pin(L[2 * m]); pin(L[2 * m + 1]); }
        if constexpr (bk < 4) { acc2[cb][rb] = mfma(wq[u][cb], L[2 * m + rb], acc2[cb][rb]); pin(acc2[cb][rb]); sb(); }
        if constexpr (bk > 0 && (u > 0 || bk == 4)) sfor<(u == 1 && bk < 4) ? 0 : u, u + 1>([&](auto R) { constexpr int rq = R;
          *(LAS f32x4*)(size_t)((pa[pr] ^ (rq * 32)) + pc * 128) = (f32x4){acc2[pc][pr][4 * rq], acc2[pc][pr][4 * rq + 1], acc2[pc][pr][4 * rq + 2], acc2[pc][pr][4 * rq + 3]}; });
        sb();
      });
    });
    wait_lgkm0(); bar();
  }
  asm volatile(".Lpf_end:");
}

extern "C" void kernel_launch(void* const* d_in, const int* in_sizes, int n_in, void* d_out, int out_size, void* d_ws, size_t ws_size,
                              hipStream_t stream) {
  (void)hipFuncSetAttribute((const void*)fused, hipFuncAttributeMaxDynamicSharedMemorySize, LDS_BYTES);
  fused<<<256, 512, LDS_BYTES, stream>>>((const u16*)d_in[0], (const u16*)d_in[1], (const u16*)d_in[2], (const u16*)d_in[3], (const u16*)d_in[4],
                                         (const u16*)d_in[5], (const u16*)d_in[6], (const u16*)d_in[7], (const u16*)d_in[8], (const u16*)d_in[10],
                                         (const u16*)d_in[11], (const u16*)d_in[12], (char*)d_ws, (float*)d_out);
}
```
